```python
import torch

def _softmax(x, axis=-1):
    return torch.softmax(x, dim=axis)
def _gelu(x):
    return torch.nn.functional.gelu(x, approximate="tanh")
def _silu(x):
    return torch.nn.functional.silu(x)
def _sigmoid(x):
    return torch.sigmoid(x)
def _layernorm(x, axis=-1, eps=1e-5):
    m = x.mean(dim=axis, keepdim=True)
    v = x.var(dim=axis, keepdim=True, unbiased=False)
    return (x - m) / torch.sqrt(v + eps)
def _scatter_add(tgt, idx, src, axis=0):
    return tgt.clone().index_add_(axis, idx, src)
def _segment_sum(src, seg, n):
    out = torch.zeros((n,) + src.shape[1:], dtype=src.dtype)
    return out.index_add_(0, seg, src)
def _pad(x, pw):
    flat = tuple(v for pair in reversed(pw) for v in pair)
    return torch.nn.functional.pad(x, flat)

kernel_name = 'parallel_sum__sliding_local_dilated__dense_activation_squared_relu__prompt'

def setup_inputs():

    _w = (lambda a: (lambda t: t.to(torch.bfloat16) if t.is_floating_point() else t)(torch.as_tensor(a)))
    def _A_setup():
        B, S, D = 1, 2048, 1024
        return {
            "x": _w((lambda *s: torch.randn(*s, dtype=torch.bfloat16) * 0.05)(B, S, D)),
            "wqkv": _w((lambda *s: torch.randn(*s, dtype=torch.bfloat16) * 0.1)(D, 3 * D)),
            "wo": _w((lambda *s: torch.randn(*s, dtype=torch.bfloat16) * 0.05)(D, D)),
        }
    _A_in = _A_setup()

    def _B_setup():
        B, S, D, F = 1, 2048, 1024, 3584
        return {
            "x": _w((lambda *s: torch.randn(*s, dtype=torch.bfloat16) * 0.05)(B, S, D)),
            "w1": _w((lambda *s: torch.randn(*s, dtype=torch.bfloat16) * 0.05)(D, F)),
            "b1": _w((lambda *s: torch.randn(*s, dtype=torch.bfloat16) * 0.4)(F)),
            "w2": _w((lambda *s: torch.randn(*s, dtype=torch.bfloat16) * 0.05)(F, D)),
            "b2": _w((lambda *s: torch.randn(*s, dtype=torch.bfloat16) * 3.2)(D)),
        }
    _B_in = _B_setup()

    return {
        "A_x": _A_in["x"],
        "A_wqkv": _A_in["wqkv"],
        "A_wo": _A_in["wo"],
        "B_x": _B_in["x"],
        "B_w1": _B_in["w1"],
        "B_b1": _B_in["b1"],
        "B_w2": _B_in["w2"],
        "B_b2": _B_in["b2"],
    }

def reference(A_x, A_wqkv, A_wo, B_x, B_w1, B_b1, B_w2, B_b2):
    def _A_ref(x, wqkv, wo):
        B, S, D = x.shape
        H = 32; Dh = D // H
        W, dil = 8, 2
        qkv = (lambda x, *d: x.permute(*d))((x @ wqkv).reshape(B, S, 3, H, Dh), 2, 0, 3, 1, 4)
        q, k, v = qkv[0], qkv[1], qkv[2]
        scale = Dh ** -0.5
        pos = torch.arange(S)
        off = torch.arange(W) * dil
        idx = pos[:, None] - off[None, :]
        mask = idx >= 0
        idx_c = torch.where(mask, idx, 0).reshape(-1)
        kg = k[:, :, idx_c, :].reshape(B, H, S, W, Dh)
        vg = v[:, :, idx_c, :].reshape(B, H, S, W, Dh)
        logits = torch.einsum("bhsd,bhswd->bhsw", q, kg) * scale
        logits = torch.where(mask[None, None, :, :], logits, -1e9)
        attn = _softmax(logits, axis=-1)
        o = torch.einsum("bhsw,bhswd->bhsd", attn, vg)
        return (lambda x, *d: x.permute(*d))(o, 0, 2, 1, 3).reshape(B, S, D) @ wo

    def _B_ref(x, w1, b1, w2, b2):
        h = torch.einsum("bsd,df->bsf", x, w1) + b1
        zero = torch.zeros_like(h)
        r = torch.where(h > zero, h, zero)
        h = r * r
        out = torch.einsum("bsf,fd->bsd", h, w2) + b2
        return out

    x = A_x
    xn = _layernorm(x, axis=-1)
    return x + _A_ref(xn, A_wqkv, A_wo) + _B_ref(xn, B_w1, B_b1, B_w2, B_b2)
```

```cpp
#include <hip/hip_runtime.h>
#include <hip/hip_bf16.h>
typedef unsigned short u16;
__device__ __forceinline__ float bf2f(u16 v){ return __uint_as_float(((unsigned)v)<<16); }
__device__ __forceinline__ u16 f2bf(float f){ unsigned u=__float_as_uint(f); u += 0x7FFF + ((u>>16)&1); return (u16)(u>>16); }
__device__ __forceinline__ float rbf(float f){ return bf2f(f2bf(f)); }

__global__ void n_ln(const u16* x, u16* xn){
  int r=blockIdx.x; int t=threadIdx.x; __shared__ float red[256];
  float v[4]; float s=0; for(int i=0;i<4;i++){ v[i]=bf2f(x[r*1024+t*4+i]); s+=v[i]; }
  red[t]=s; __syncthreads(); for(int o=128;o>0;o>>=1){ if(t<o) red[t]+=red[t+o]; __syncthreads(); }
  float mean=red[0]/1024.f; __syncthreads();
  float q=0; for(int i=0;i<4;i++){ float d=v[i]-mean; q+=d*d; }
  red[t]=q; __syncthreads(); for(int o=128;o>0;o>>=1){ if(t<o) red[t]+=red[t+o]; __syncthreads(); }
  float var=rbf(red[0]/1024.f); float m=rbf(mean);
  float sd=rbf(sqrtf(rbf(var+1e-5f)));
  for(int i=0;i<4;i++) xn[r*1024+t*4+i]=f2bf(rbf(v[i]-m)/sd);
}
__global__ void n_gemm(const u16* A,int lda,const u16* B,int ldb,int K,u16* C,int ldc,const u16* bias,int mode){
  __shared__ float As[16][17], Bs[16][17];
  int tx=threadIdx.x&15, ty=threadIdx.x>>4; int m=blockIdx.y*16+ty, n=blockIdx.x*16+tx; float acc=0;
  for(int k0=0;k0<K;k0+=16){ As[ty][tx]=bf2f(A[(size_t)m*lda+k0+tx]); Bs[ty][tx]=bf2f(B[(size_t)(k0+ty)*ldb+n]); __syncthreads();
    for(int k=0;k<16;k++) acc+=As[ty][k]*Bs[k][tx]; __syncthreads(); }
  if(mode==1){ float h=rbf(rbf(acc)+bf2f(bias[n])); h=h>0?h:0; acc=h*h; }
  if(mode==2){ acc=rbf(acc)+bf2f(bias[n]); }
  C[(size_t)m*ldc+n]=f2bf(acc);
}
__global__ void n_attn(const u16* qkv,u16* o){
  int s=blockIdx.x; int h=threadIdx.x; if(h>=32) return;
  float lg[8]; float mx=-1e30f;
  for(int w=0;w<8;w++){ int idx=s-2*w; float l; if(idx<0) l=rbf(-1e9f); else { float a=0; for(int d=0;d<32;d++) a+=bf2f(qkv[(size_t)s*3072+h*32+d])*bf2f(qkv[(size_t)idx*3072+1024+h*32+d]); l=rbf(rbf(a)*rbf(0.17677669529663687f)); } lg[w]=l; mx=fmaxf(mx,l);}
  float sum=0; for(int w=0;w<8;w++){ lg[w]=expf(lg[w]-mx); sum+=lg[w]; }
  for(int w=0;w<8;w++) lg[w]=rbf(lg[w]/sum);
  for(int d=0;d<32;d++){ float a=0; for(int w=0;w<8;w++){ int idx=s-2*w; if(idx<0) idx=0; a+=lg[w]*bf2f(qkv[(size_t)idx*3072+2048+h*32+d]); } o[(size_t)s*1024+h*32+d]=f2bf(a);}
}
__global__ void n_fin(const u16* x,const u16* a,const u16* b,u16* out){ int i=blockIdx.x*256+threadIdx.x; out[i]=f2bf(rbf(bf2f(x[i])+bf2f(a[i]))+bf2f(b[i])); }

static void naive_launch(void* const* d_in, void* d_out, void* d_ws, hipStream_t stream){
  const u16* x=(const u16*)d_in[0]; const u16* wqkv=(const u16*)d_in[1]; const u16* wo=(const u16*)d_in[2];
  const u16* w1=(const u16*)d_in[4]; const u16* b1=(const u16*)d_in[5]; const u16* w2=(const u16*)d_in[6]; const u16* b2=(const u16*)d_in[7];
  u16* ws=(u16*)d_ws; u16* xn=ws; u16* qkv=xn+2048*1024; u16* o=qkv+2048*3072; u16* h=o+2048*1024; u16* ao=h+2048*3584; u16* bo=ao+2048*1024;
  n_ln<<<2048,256,0,stream>>>(x,xn);
  n_gemm<<<dim3(3072/16,2048/16),256,0,stream>>>(xn,1024,wqkv,3072,1024,qkv,3072,nullptr,0);
  n_gemm<<<dim3(3584/16,2048/16),256,0,stream>>>(xn,1024,w1,3584,1024,h,3584,b1,1);
  n_attn<<<2048,64,0,stream>>>(qkv,o);
  n_gemm<<<dim3(1024/16,2048/16),256,0,stream>>>(o,1024,wo,1024,1024,ao,1024,nullptr,0);
  n_gemm<<<dim3(1024/16,2048/16),256,0,stream>>>(h,3584,w2,1024,3584,bo,1024,b2,2);
  n_fin<<<2048*1024/256,256,0,stream>>>(x,ao,bo,(u16*)d_out);
}
extern "C" void kernel_launch(void* const* d_in, const int* in_sizes, int n_in, void* d_out, int out_size, void* d_ws, size_t ws_size, hipStream_t stream){
  naive_launch(d_in,d_out,d_ws,stream);
}
```

```cpp
#include <hip/hip_runtime.h>

__global__ __launch_bounds__(256, 1) void k_fused(const void* x, void* ws, const void* wqkv, const void* w1, const void* b1, const void* wo, const void* w2, const void* b2, void* out) {
  const void* ka = (const void*)__builtin_amdgcn_kernarg_segment_ptr();
  const unsigned bid = blockIdx.x, xcd = bid & 7, jj = bid >> 3;
  const unsigned q = (xcd & 3) ^ 0, ty = jj & 1, p = (xcd >> 2) * 4 + ((jj >> 1) & 3), t = jj >> 3;
  asm volatile(R"ASM(.macro LX_aa d,p,q
v_mfma_f32_16x16x32_bf16 v[\d:\d+3], a[\p:\p+3], a[\q:\q+3], v[\d:\d+3]
.endm
.macro LX_av d,p,q
v_mfma_f32_16x16x32_bf16 v[\d:\d+3], a[\p:\p+3], v[\q:\q+3], v[\d:\d+3]
.endm
.macro LX_va d,p,q
v_mfma_f32_16x16x32_bf16 v[\d:\d+3], v[\p:\p+3], a[\q:\q+3], v[\d:\d+3]
.endm
.macro LX_vv d,p,q
v_mfma_f32_16x16x32_bf16 v[\d:\d+3], v[\p:\p+3], v[\q:\q+3], v[\d:\d+3]
.endm
.macro LX_t d,r,o
ds_read_b64_tr_b16 a[\d:\d+1], v\r offset:\o
.endm
.macro LX_l d,r,o
buffer_load_dwordx4 a[\d:\d+3], v\r, s[20:23], s33 offen offset:\o sc1
.endm
.macro LX_w n
s_waitcnt lgkmcnt(\n)
.endm
.macro LX_c d,p,q
v_cvt_pk_bf16_f32 v\d, v\p, v\q
.endm
s_load_dwordx2 s[24:25], %0, 0
s_load_dwordx2 s[20:21], %0, 8
v_and_b32 v24, 63, %4
v_lshrrev_b32 v27, 5, %4
v_and_b32 v26, 31, %4
v_lshlrev_b32 v26, 4, v26
v_lshl_add_u32 v29, v27, 12, v26
s_lshl_b32 s48, %1, 8
s_lshl_b32 s49, %2, 6
s_add_u32 s48, s48, s49
s_lshr_b32 s49, %3, 1
s_lshl_b32 s49, s49, 5
s_add_u32 s48, s48, s49
s_and_b32 s49, %3, 1
s_add_u32 s48, s48, s49
s_lshl_b32 s49, %5, 4
s_add_u32 s48, s48, s49
s_lshl_b32 s48, s48, 11
s_mov_b32 s26, 4194304
s_mov_b32 s27, 0x20000
s_mov_b32 s22, 42274816
s_mov_b32 s23, 0x20000
v_and_b32 v22, 1, v24
v_lshlrev_b32 v22, 8, v22
s_lshl_b32 s49, %1, 8
s_add_u32 s49, s49, 35651584
s_getpc_b64 s[80:81]
LX_pc:
s_and_b32 s51, %2, 0
s_lshl_b32 s51, s51, 1
s_add_u32 s51, s51, %5
s_mul_i32 s50, s51, 61440
s_add_u32 s50, s50, LX_Q0_begin-LX_pc
s_mov_b32 s82, 61440
s_add_u32 s80, s80, s50
s_addc_u32 s81, s81, 0
s_mov_b64 s[50:51], s[80:81]
s_and_b32 s81, s81, 0xffff
s_mov_b32 s83, 0x20000
v_min_u32 v30, 31, v24
v_lshlrev_b32 v30, 7, v30
v_lshrrev_b32 v31, 6, %4
v_lshl_add_u32 v30, v31, 12, v30
s_and_b32 s52, %1, 3
s_lshl_b32 s52, s52, 14
v_add_u32 v30, s52, v30
LX_w 0
s_and_b32 s25, s25, 0xffff
buffer_load_dwordx4 v[32:35], v29, s[24:27], s48 offen offset:0
buffer_load_dwordx4 v[36:39], v29, s[24:27], s48 offen offset:512
buffer_load_dwordx4 v[40:43], v29, s[24:27], s48 offen offset:1024
buffer_load_dwordx4 v[44:47], v29, s[24:27], s48 offen offset:1536
s_and_b32 s21, s21, 0xffff
buffer_load_dwordx2 v[98:99], v22, s[20:23], s49 offen sc1
buffer_load_dword a223, v30, s[80:83], 0 offen
s_setpc_b64 s[50:51]
.p2align 12
LX_Q0_begin:
s_load_dwordx2 s[16:17], %0, 16
s_load_dwordx2 s[28:29], %0, 32
s_load_dwordx2 s[84:85], %0, 40
s_load_dwordx2 s[58:59], %0, 48
s_load_dwordx2 s[96:97], %0, 56
s_load_dwordx2 s[98:99], %0, 64
v_lshrrev_b32 v25, 6, %4
v_lshlrev_b32 v1, 4, v24
v_readfirstlane_b32 s61, v25
s_lshl_b32 s55, %2, 2
s_add_u32 s55, s55, %3
s_and_b32 s54, %1, 0
s_lshl_b32 s54, s54, 2
s_xor_b32 s54, s54, s55
s_lshl_b32 s43, s54, 7
s_lshl_b32 s39, s61, 11
s_lshl_b32 s42, s61, 4
v_lshrrev_b32 v25, 3, v24
v_add_u32 v25, s42, v25
v_mul_u32_u24 v25, 6144, v25
v_lshrrev_b32 v26, 4, v24
v_lshlrev_b32 v26, 1, v26
v_and_b32 v27, 7, v24
v_xor_b32 v26, v26, v27
v_lshlrev_b32 v26, 4, v26
v_add3_u32 v2, v25, v26, s43
v_add_u32 v3, 49152, v2
v_add_u32 v4, 2048, v2
v_add_u32 v5, 51200, v2
v_add_u32 v6, 4096, v2
v_add_u32 v7, 53248, v2
s_and_b32 s55, %2, 1
s_lshl_b32 s54, %1, 1
s_add_u32 s55, s54, s55
s_mul_i32 s55, s55, 3
s_add_u32 s55, s55, %3
s_lshl_b32 s55, s55, 2
s_add_u32 s55, s55, s61
s_cmp_eq_u32 %3, 3
s_cselect_b32 s55, 131072, s55
s_lshl_b32 s33, s55, 11
s_cmp_lt_u32 %2, 2
s_cselect_b32 s54, 1024, 0
s_add_u32 s33, s33, s54
s_cmp_lt_u32 s55, 192
s_cselect_b32 s55, s33, 1073737728
v_lshrrev_b32 v25, 3, v24
v_add_u32 v25, 1, v25
v_mul_u32_u24 v25, 393216, v25
v_and_b32 v28, 7, v24
v_lshlrev_b32 v28, 7, v28
v_cmp_gt_u32 vcc, 16, v24
v_add3_u32 v28, v28, v25, s55
v_mov_b32 v25, 1073737728
v_cndmask_b32 v28, v25, v28, vcc
LX_w 0
s_and_b32 s17, s17, 0xffff
s_mov_b32 s18, 6291456
s_mov_b32 s19, 0x20000
s_mov_b32 s35, 0
s_add_u32 m0, s39, 0
s_nop 0
buffer_load_dwordx4 v2, s[16:19], s35 offen  lds
s_add_u32 m0, s39, 1024
s_nop 0
buffer_load_dwordx4 v3, s[16:19], s35 offen  lds
s_add_u32 m0, s39, 32768
s_nop 0
buffer_load_dwordx4 v4, s[16:19], s35 offen  lds
s_add_u32 m0, s39, 33792
s_nop 0
buffer_load_dwordx4 v5, s[16:19], s35 offen  lds
s_add_u32 m0, s39, 65536
s_nop 0
buffer_load_dwordx4 v6, s[16:19], s35 offen  lds
s_add_u32 m0, s39, 66560
s_nop 0
buffer_load_dwordx4 v7, s[16:19], s35 offen  lds
buffer_load_dword a223, v28, s[16:19], 0 offen
s_mov_b32 s47, 0xffff0000
s_lshl_b32 s52, %3, 1
s_add_u32 s52, s52, 0
s_lshl_b32 s53, %1, 2
s_add_u32 s53, s53, %2
s_lshl_b32 s50, s53, 12
s_lshl_b32 s51, s61, 10
s_add_u32 s50, s50, s51
s_lshl_b32 s51, s52, 7
s_add_u32 s50, s50, s51
s_add_u32 s50, s50, 35782656
v_mov_b32 v18, 1073737728
v_writelane_b32 v18, s50, 0
buffer_load_dword a223, v18, s[20:23], 0 offen sc1
v_and_b32 v25, 7, v24
v_lshrrev_b32 v27, 5, v24
v_bfe_u32 v26, v24, 3, 2
v_lshlrev_b32 v19, 12, v27
v_lshl_add_u32 v19, v26, 8, v19
v_lshl_add_u32 v19, v25, 4, v19
s_mul_i32 s49, s53, 1114112
s_lshl_b32 s50, s61, 15
s_add_u32 s49, s49, s50
s_lshl_b32 s50, %3, 10
s_add_u32 s49, s49, s50
s_add_u32 s49, s49, 0
v_add_u32 v19, s49, v19
v_bfe_u32 v25, v24, 2, 3
v_lshlrev_b32 v86, 9, v25
v_and_b32 v26, 1, v24
v_lshl_add_u32 v86, v26, 8, v86
v_bfe_u32 v27, v24, 1, 1
v_lshl_add_u32 v86, v27, 3, v86
v_and_b32 v25, 3, v25
v_lshl_add_u32 v25, v25, 1, v26
v_lshrrev_b32 v27, 5, v24
s_lshl_b32 s50, s61, 1
v_add_u32 v26, s50, v27
v_xor_b32 v25, v25, v26
v_lshl_add_u32 v86, v25, 4, v86
v_add_u32 v86, 114688, v86
v_bfe_u32 v25, v24, 4, 2
v_xor_b32 v25, v25, v24
v_lshlrev_b32 v87, 4, v25
s_lshl_b32 s50, s61, 12
s_add_u32 s50, s50, 114688
v_add_u32 v87, s50, v87
v_xor_b32 v88, 64, v87
s_waitcnt vmcnt(9)
v_lshlrev_b32 v48, 16, v32
v_and_b32 v49, s47, v32
v_lshlrev_b32 v50, 16, v33
v_and_b32 v51, s47, v33
v_lshlrev_b32 v52, 16, v34
v_and_b32 v53, s47, v34
v_lshlrev_b32 v54, 16, v35
v_and_b32 v55, s47, v35
v_lshlrev_b32 v56, 16, v36
v_and_b32 v57, s47, v36
v_lshlrev_b32 v58, 16, v37
v_and_b32 v59, s47, v37
v_lshlrev_b32 v60, 16, v38
v_and_b32 v61, s47, v38
v_lshlrev_b32 v62, 16, v39
v_and_b32 v63, s47, v39
v_lshlrev_b32 v64, 16, v40
v_and_b32 v65, s47, v40
v_lshlrev_b32 v66, 16, v41
v_and_b32 v67, s47, v41
v_lshlrev_b32 v68, 16, v42
v_and_b32 v69, s47, v42
v_lshlrev_b32 v70, 16, v43
v_and_b32 v71, s47, v43
v_lshlrev_b32 v72, 16, v44
v_and_b32 v73, s47, v44
v_lshlrev_b32 v74, 16, v45
v_and_b32 v75, s47, v45
v_lshlrev_b32 v76, 16, v46
v_and_b32 v77, s47, v46
v_lshlrev_b32 v78, 16, v47
v_and_b32 v79, s47, v47
v_pk_add_f32 v[80:81], v[48:49], v[50:51]
v_pk_add_f32 v[82:83], v[52:53], v[54:55]
v_pk_add_f32 v[80:81], v[80:81], v[56:57]
v_pk_add_f32 v[82:83], v[82:83], v[58:59]
v_pk_add_f32 v[80:81], v[80:81], v[60:61]
v_pk_add_f32 v[82:83], v[82:83], v[62:63]
v_pk_add_f32 v[80:81], v[80:81], v[64:65]
v_pk_add_f32 v[82:83], v[82:83], v[66:67]
v_pk_add_f32 v[80:81], v[80:81], v[68:69]
v_pk_add_f32 v[82:83], v[82:83], v[70:71]
v_pk_add_f32 v[80:81], v[80:81], v[72:73]
v_pk_add_f32 v[82:83], v[82:83], v[74:75]
v_pk_add_f32 v[80:81], v[80:81], v[76:77]
v_pk_add_f32 v[82:83], v[82:83], v[78:79]
v_pk_add_f32 v[80:81], v[80:81], v[82:83]
v_add_f32 v80, v80, v81
s_nop 1
v_add_f32_dpp v80, v80, v80 quad_perm:[1,0,3,2] row_mask:0xf bank_mask:0xf
s_nop 1
v_add_f32_dpp v80, v80, v80 quad_perm:[2,3,0,1] row_mask:0xf bank_mask:0xf
s_nop 1
v_add_f32_dpp v80, v80, v80 row_half_mirror row_mask:0xf bank_mask:0xf
s_nop 1
v_add_f32_dpp v80, v80, v80 row_mirror row_mask:0xf bank_mask:0xf
v_mov_b32 v81, v80
s_nop 1
v_permlane16_swap_b32 v80, v81
v_add_f32 v80, v80, v81
v_mul_f32 v84, 0xba800000, v80
v_mul_f32 v85, 0xba800000, v80
v_pk_add_f32 v[48:49], v[48:49], v[84:85]
v_pk_add_f32 v[50:51], v[50:51], v[84:85]
v_pk_add_f32 v[52:53], v[52:53], v[84:85]
v_pk_add_f32 v[54:55], v[54:55], v[84:85]
v_pk_add_f32 v[56:57], v[56:57], v[84:85]
v_pk_add_f32 v[58:59], v[58:59], v[84:85]
v_pk_add_f32 v[60:61], v[60:61], v[84:85]
v_pk_add_f32 v[62:63], v[62:63], v[84:85]
v_pk_add_f32 v[64:65], v[64:65], v[84:85]
v_pk_add_f32 v[66:67], v[66:67], v[84:85]
v_pk_add_f32 v[68:69], v[68:69], v[84:85]
v_pk_add_f32 v[70:71], v[70:71], v[84:85]
v_pk_add_f32 v[72:73], v[72:73], v[84:85]
v_pk_add_f32 v[74:75], v[74:75], v[84:85]
v_pk_add_f32 v[76:77], v[76:77], v[84:85]
v_pk_add_f32 v[78:79], v[78:79], v[84:85]
v_pk_mul_f32 v[80:81], v[48:49], v[48:49]
v_pk_mul_f32 v[82:83], v[50:51], v[50:51]
v_pk_fma_f32 v[80:81], v[52:53], v[52:53], v[80:81]
v_pk_fma_f32 v[82:83], v[54:55], v[54:55], v[82:83]
v_pk_fma_f32 v[80:81], v[56:57], v[56:57], v[80:81]
v_pk_fma_f32 v[82:83], v[58:59], v[58:59], v[82:83]
v_pk_fma_f32 v[80:81], v[60:61], v[60:61], v[80:81]
v_pk_fma_f32 v[82:83], v[62:63], v[62:63], v[82:83]
v_pk_fma_f32 v[80:81], v[64:65], v[64:65], v[80:81]
v_pk_fma_f32 v[82:83], v[66:67], v[66:67], v[82:83]
v_pk_fma_f32 v[80:81], v[68:69], v[68:69], v[80:81]
v_pk_fma_f32 v[82:83], v[70:71], v[70:71], v[82:83]
v_pk_fma_f32 v[80:81], v[72:73], v[72:73], v[80:81]
v_pk_fma_f32 v[82:83], v[74:75], v[74:75], v[82:83]
v_pk_fma_f32 v[80:81], v[76:77], v[76:77], v[80:81]
v_pk_fma_f32 v[82:83], v[78:79], v[78:79], v[82:83]
v_pk_add_f32 v[80:81], v[80:81], v[82:83]
v_add_f32 v82, v80, v81
LX_c 85,85,85
v_and_b32 v85, s47, v85
v_add_f32_dpp v82, v82, v82 quad_perm:[1,0,3,2] row_mask:0xf bank_mask:0xf
s_nop 1
v_add_f32_dpp v82, v82, v82 quad_perm:[2,3,0,1] row_mask:0xf bank_mask:0xf
s_nop 1
v_add_f32_dpp v82, v82, v82 row_half_mirror row_mask:0xf bank_mask:0xf
s_nop 1
v_add_f32_dpp v82, v82, v82 row_mirror row_mask:0xf bank_mask:0xf
v_mov_b32 v81, v82
s_nop 1
v_permlane16_swap_b32 v82, v81
v_add_f32 v82, v82, v81
v_mul_f32 v82, 0x3a800000, v82
LX_c 82,82,82
v_and_b32 v82, s47, v82
v_add_f32 v82, 0x3727c5ac, v82
LX_c 82,82,82
v_and_b32 v82, s47, v82
v_sqrt_f32 v82, v82
v_sub_f32 v80, v85, v84
LX_c 82,82,82
v_and_b32 v82, s47, v82
v_rcp_f32 v82, v82
s_nop 0
v_mul_f32 v80, v80, v82
v_pk_fma_f32 v[48:49], v[48:49], v[82:83], v[80:81] op_sel_hi:[1,0,0]
v_pk_fma_f32 v[50:51], v[50:51], v[82:83], v[80:81] op_sel_hi:[1,0,0]
v_pk_fma_f32 v[52:53], v[52:53], v[82:83], v[80:81] op_sel_hi:[1,0,0]
v_pk_fma_f32 v[54:55], v[54:55], v[82:83], v[80:81] op_sel_hi:[1,0,0]
LX_c 32,48,49
LX_c 33,50,51
LX_c 34,52,53
LX_c 35,54,55
ds_write_b64 v86, v[32:33] offset:0
ds_write_b64 v86, v[34:35] offset:128
v_pk_fma_f32 v[56:57], v[56:57], v[82:83], v[80:81] op_sel_hi:[1,0,0]
v_pk_fma_f32 v[58:59], v[58:59], v[82:83], v[80:81] op_sel_hi:[1,0,0]
v_pk_fma_f32 v[60:61], v[60:61], v[82:83], v[80:81] op_sel_hi:[1,0,0]
v_pk_fma_f32 v[62:63], v[62:63], v[82:83], v[80:81] op_sel_hi:[1,0,0]
LX_c 36,56,57
LX_c 37,58,59
LX_c 38,60,61
LX_c 39,62,63
ds_write_b64 v86, v[36:37] offset:4096
ds_write_b64 v86, v[38:39] offset:4224
v_pk_fma_f32 v[64:65], v[64:65], v[82:83], v[80:81] op_sel_hi:[1,0,0]
v_pk_fma_f32 v[66:67], v[66:67], v[82:83], v[80:81] op_sel_hi:[1,0,0]
v_pk_fma_f32 v[68:69], v[68:69], v[82:83], v[80:81] op_sel_hi:[1,0,0]
v_pk_fma_f32 v[70:71], v[70:71], v[82:83], v[80:81] op_sel_hi:[1,0,0]
LX_c 40,64,65
LX_c 41,66,67
LX_c 42,68,69
LX_c 43,70,71
ds_write_b64 v86, v[40:41] offset:8192
ds_write_b64 v86, v[42:43] offset:8320
v_pk_fma_f32 v[72:73], v[72:73], v[82:83], v[80:81] op_sel_hi:[1,0,0]
v_pk_fma_f32 v[74:75], v[74:75], v[82:83], v[80:81] op_sel_hi:[1,0,0]
v_pk_fma_f32 v[76:77], v[76:77], v[82:83], v[80:81] op_sel_hi:[1,0,0]
v_pk_fma_f32 v[78:79], v[78:79], v[82:83], v[80:81] op_sel_hi:[1,0,0]
LX_c 44,72,73
LX_c 45,74,75
LX_c 46,76,77
LX_c 47,78,79
ds_write_b64 v86, v[44:45] offset:12288
ds_write_b64 v86, v[46:47] offset:12416
v_add_co_u32 v26, vcc, 1, v98
s_nop 1
v_addc_co_u32 v27, vcc, 0, v99, vcc
s_mov_b32 s36, 8192
s_mov_b32 s37, 16384
s_mov_b32 s49, 24576
LX_w 0
s_barrier
ds_read_b128 a[200:203], v87 offset:0
ds_read_b128 a[204:207], v88 offset:1024
ds_read_b128 a[208:211], v87 offset:2048
ds_read_b128 a[212:215], v88 offset:3072
v_readlane_b32 s56, v26, 0
v_readlane_b32 s57, v27, 0
s_cmp_ge_u32 s61, 1
s_cbranch_scc1 LX_Q0_nost
LX_w 3
buffer_store_dwordx4 a[200:203], v19, s[20:23], 0 offen sc1
LX_w 2
buffer_store_dwordx4 a[204:207], v19, s[20:23], s36 offen sc1
LX_w 1
buffer_store_dwordx4 a[208:211], v19, s[20:23], s37 offen sc1
)ASM"
R"ASM(LX_w 0
buffer_store_dwordx4 a[212:215], v19, s[20:23], s49 offen sc1
v_mov_b32 v19, 1073737728
LX_Q0_nost:
v_lshrrev_b32 v25, 2, v24
v_lshlrev_b32 v25, 7, v25
v_and_b32 v94, 3, v24
v_lshlrev_b32 v94, 3, v94
v_bfe_u32 v95, v24, 3, 2
v_lshlrev_b32 v95, 5, v95
v_add3_u32 v9, v25, v94, v95
v_xor_b32 v10, 32, v9
v_xor_b32 v11, 64, v9
v_xor_b32 v12, 96, v9
v_add_u32 v13, 65536, v9
v_add_u32 v14, 65536, v10
v_add_u32 v15, 65536, v11
v_add_u32 v16, 65536, v12
v_bfe_u32 v25, v24, 3, 1
v_sub_u32 v25, 3, v25
v_lshlrev_b32 v25, 10, v25
v_or_b32 v94, 8, v24
v_lshlrev_b32 v94, 4, v94
v_add_u32 v8, v25, v94
s_lshl_b32 s45, %1, 2
s_max_u32 s45, s45, 1
s_sub_u32 s45, s45, 1
s_mul_i32 s50, s45, 37
s_lshr_b32 s50, s50, 8
s_mul_i32 s50, s50, 1113216
s_mul_i32 s54, s45, 1113984
s_sub_u32 s50, s50, s54
s_add_u32 s50, s50, 36700288
s_mul_i32 s45, s45, 1114112
v_lshrrev_b32 v25, 4, v24
v_cmp_eq_u32 vcc, 1, v25
v_add_u32 v25, s50, v8
s_nop 0
v_cndmask_b32 v8, v8, v25, vcc
s_cmp_eq_u32 %1, 0
s_cselect_b32 s50, 1073737728, 0
v_add_u32 v8, s50, v8
s_lshl_b32 s50, s61, 5
v_xor_b32 v17, s50, v9
v_add_u32 v20, 65536, v17
s_lshl_b32 s41, %1, 2
s_add_u32 s41, s41, s61
s_mul_i32 s38, s41, 1114112
s_mul_i32 s50, s41, 37
s_lshr_b32 s50, s50, 8
s_mul_i32 s50, s50, 1113216
s_mul_i32 s54, s41, 1113984
s_sub_u32 s50, s50, s54
s_add_u32 s50, s50, 36700288
v_lshrrev_b32 v25, 3, v24
v_cmp_eq_u32 vcc, 3, v25
v_add_u32 v25, s50, v1
s_nop 0
v_cndmask_b32 v1, v1, v25, vcc
v_add_u32 v23, 4096, v1
v_lshrrev_b32 v25, 3, v24
v_add_u32 v25, 4, v25
v_mul_u32_u24 v25, 393216, v25
v_and_b32 v21, 7, v24
v_lshlrev_b32 v21, 7, v21
v_cmp_gt_u32 vcc, 8, v24
v_add3_u32 v21, v21, v25, s55
v_mov_b32 v25, 1073737728
v_cndmask_b32 v21, v25, v21, vcc
s_cmp_lt_u32 s61, 1
s_cbranch_scc1 LX_Q0_nob
s_mov_b32 s35, 393216
s_add_u32 m0, s39, 8192
s_nop 0
buffer_load_dwordx4 v2, s[16:19], s35 offen  lds
s_add_u32 m0, s39, 9216
s_nop 0
buffer_load_dwordx4 v3, s[16:19], s35 offen  lds
s_add_u32 m0, s39, 40960
s_nop 0
buffer_load_dwordx4 v4, s[16:19], s35 offen  lds
s_add_u32 m0, s39, 41984
s_nop 0
buffer_load_dwordx4 v5, s[16:19], s35 offen  lds
s_add_u32 m0, s39, 73728
s_nop 0
buffer_load_dwordx4 v6, s[16:19], s35 offen  lds
s_add_u32 m0, s39, 74752
s_nop 0
buffer_load_dwordx4 v7, s[16:19], s35 offen  lds
s_mov_b32 s35, 786432
s_add_u32 m0, s39, 16384
s_nop 0
buffer_load_dwordx4 v2, s[16:19], s35 offen  lds
s_add_u32 m0, s39, 17408
s_nop 0
buffer_load_dwordx4 v3, s[16:19], s35 offen  lds
s_add_u32 m0, s39, 49152
s_nop 0
buffer_load_dwordx4 v4, s[16:19], s35 offen  lds
s_add_u32 m0, s39, 50176
s_nop 0
buffer_load_dwordx4 v5, s[16:19], s35 offen  lds
s_add_u32 m0, s39, 81920
s_nop 0
buffer_load_dwordx4 v6, s[16:19], s35 offen  lds
s_add_u32 m0, s39, 82944
s_nop 0
buffer_load_dwordx4 v7, s[16:19], s35 offen  lds
LX_Q0_nob:
s_add_u32 s51, s33, 0
v_lshlrev_b32 v22, 7, v24
v_add_u32 v22, s51, v22
v_cmp_gt_u32 vcc, 8, v24
s_nop 1
v_cndmask_b32 v22, v25, v22, vcc
s_lshr_b32 s50, %2, 1
s_lshl_b32 s51, %1, 1
s_add_u32 s50, s50, s51
s_lshl_b32 s50, s50, 2
s_add_u32 s50, s50, s61
s_mul_i32 s50, s50, 12288
s_add_u32 s50, s50, 1573248
s_and_b32 s51, %2, 1
s_cselect_b32 s50, s50, 1073737728
s_cmp_eq_u32 %3, 3
s_cselect_b32 s50, s50, 1073737728
v_cmp_gt_u32 vcc, 12, v24
v_lshl_add_u32 v21, v24, 10, s50
s_nop 0
v_cndmask_b32 v21, v25, v21, vcc
v_cmp_eq_u32 vcc, 0, v24
s_lshl_b32 s51, %3, 9
s_add_u32 s51, s51, 0
v_cndmask_b32 v30, v25, 0, vcc
v_lshrrev_b32 v25, 3, v24
v_add_u32 v25, s42, v25
v_mul_u32_u24 v25, 2048, v25
v_lshrrev_b32 v94, 4, v24
v_lshlrev_b32 v94, 1, v94
v_and_b32 v95, 7, v24
v_xor_b32 v94, v94, v95
v_lshlrev_b32 v94, 4, v94
v_add3_u32 v28, v25, v94, s51
v_add_u32 v29, 16384, v28
s_mov_b64 s[28:29], s[58:59]
s_and_b32 s29, s29, 0xffff
s_mov_b32 s30, 7340032
s_mov_b32 s31, 0x20000
s_and_b32 s85, s85, 0xffff
s_mov_b32 s86, 2097152
s_mov_b32 s87, 0x20000
s_lshl_b32 s48, s61, 12
s_mul_i32 s60, %2, 1835008
s_and_b32 s54, %1, 0
s_xor_b32 s54, s54, %2
s_lshl_b32 s46, s54, 19
s_mul_i32 s51, %2, 147456
s_add_u32 s34, s38, s51
s_add_u32 s34, s34, 131072
s_mov_b32 s51, %3
s_lshl_b32 s54, s51, 17
s_add_u32 s92, s54, s46
s_lshl_b32 s51, s51, 13
s_add_u32 s88, s51, s34
s_cmp_le_u32 %3, 0
s_cselect_b32 s51, 1, 0
s_lshl_b32 s54, s51, 17
s_add_u32 s93, s54, s46
s_lshl_b32 s51, s51, 13
s_add_u32 s89, s51, s34
s_cmp_le_u32 %3, 1
s_cselect_b32 s51, 2, 1
s_lshl_b32 s54, s51, 17
s_add_u32 s94, s54, s46
s_lshl_b32 s51, s51, 13
s_add_u32 s90, s51, s34
s_cmp_le_u32 %3, 2
s_cselect_b32 s51, 3, 2
s_lshl_b32 s54, s51, 17
s_add_u32 s95, s54, s46
s_lshl_b32 s51, s51, 13
s_add_u32 s91, s51, s34
s_cmp_ge_u32 s61, 1
s_cbranch_scc1 LX_Q0_nofl
s_waitcnt vmcnt(0)
buffer_store_dwordx2 v[26:27], v18, s[20:23], 0 offen sc1
v_mov_b32 v18, 1073737728
s_mov_b32 s35, 393216
s_add_u32 m0, s39, 8192
s_nop 0
buffer_load_dwordx4 v2, s[16:19], s35 offen  lds
s_add_u32 m0, s39, 9216
s_nop 0
buffer_load_dwordx4 v3, s[16:19], s35 offen  lds
s_add_u32 m0, s39, 40960
s_nop 0
buffer_load_dwordx4 v4, s[16:19], s35 offen  lds
s_add_u32 m0, s39, 41984
s_nop 0
buffer_load_dwordx4 v5, s[16:19], s35 offen  lds
s_add_u32 m0, s39, 73728
s_nop 0
buffer_load_dwordx4 v6, s[16:19], s35 offen  lds
s_add_u32 m0, s39, 74752
s_nop 0
buffer_load_dwordx4 v7, s[16:19], s35 offen  lds
s_mov_b32 s35, 786432
s_add_u32 m0, s39, 16384
s_nop 0
buffer_load_dwordx4 v2, s[16:19], s35 offen  lds
s_add_u32 m0, s39, 17408
s_nop 0
buffer_load_dwordx4 v3, s[16:19], s35 offen  lds
s_add_u32 m0, s39, 49152
s_nop 0
buffer_load_dwordx4 v4, s[16:19], s35 offen  lds
s_add_u32 m0, s39, 50176
s_nop 0
buffer_load_dwordx4 v5, s[16:19], s35 offen  lds
s_add_u32 m0, s39, 81920
s_nop 0
buffer_load_dwordx4 v6, s[16:19], s35 offen  lds
s_add_u32 m0, s39, 82944
s_nop 0
buffer_load_dwordx4 v7, s[16:19], s35 offen  lds
s_branch LX_Q0_fl0
LX_Q0_nofl:
s_waitcnt vmcnt(12)
LX_Q0_fl0:
s_lshl_b32 s50, s53, 2
s_add_u32 s50, s50, s61
s_lshl_b32 s50, s50, 11
s_add_u32 s80, s50, 35913728
s_lshl_b32 s50, %3, 7
s_add_u32 s82, s50, s80
s_add_u32 s83, s50, s82
s_lshl_b32 s50, %1, 3
s_add_u32 s50, s50, s52
s_lshl_b32 s50, s50, 2
s_add_u32 s50, s50, %2
s_lshl_b32 s50, s50, 2
s_add_u32 s81, s50, s61
s_lshl_b32 s50, s52, 8
s_add_u32 s50, s50, s80
s_lshl_b32 s51, s81, 9
s_add_u32 s51, s51, 36175616
v_mov_b32 v94, s50
v_mov_b32 v95, s51
v_cmp_gt_u32 vcc, 2, v24
v_min_u32 v31, 4, v24
s_nop 0
v_cndmask_b32 v94, v95, v94, vcc
v_lshl_add_u32 v31, v31, 7, v94
v_and_b32 v25, 15, v24
v_lshrrev_b32 v94, 4, v24
v_lshlrev_b32 v94, 2, v94
v_sub_u32 v25, v25, v94
v_mov_b32 v95, 0xf149f2ca
s_cmp_eq_u32 s61, 0
s_cselect_b32 s50, 8, 0
s_cmp_eq_u32 s41, 0
s_cselect_b32 s51, 64, 0
v_subrev_u32 v252, 0, v25
v_cmp_gt_u32 vcc, 8, v252
v_add_u32 v253, 16, v252
v_add_u32 v254, s51, v253
v_cndmask_b32 v232, v95, 0, vcc
v_cmp_gt_u32 vcc, 8, v253
v_add_u32 v255, 0, v94
v_add_u32 v255, s50, v255
v_cndmask_b32 v236, v95, 0, vcc
v_cmp_gt_u32 vcc, 8, v254
v_lshrrev_b32 v255, 4, v255
v_subrev_u32 v254, s50, v254
v_cndmask_b32 v240, v95, 0, vcc
v_lshl_add_u32 v254, v255, 6, v254
v_cmp_gt_u32 vcc, 8, v254
v_mov_b32 v248, 0x3f803f80
s_nop 0
v_cndmask_b32 v244, v95, 0, vcc
v_subrev_u32 v252, 1, v25
v_cmp_gt_u32 vcc, 8, v252
v_add_u32 v253, 16, v252
v_add_u32 v254, s51, v253
v_cndmask_b32 v233, v95, 0, vcc
v_cmp_gt_u32 vcc, 8, v253
v_add_u32 v255, 1, v94
v_add_u32 v255, s50, v255
v_cndmask_b32 v237, v95, 0, vcc
v_cmp_gt_u32 vcc, 8, v254
v_lshrrev_b32 v255, 4, v255
v_subrev_u32 v254, s50, v254
v_cndmask_b32 v241, v95, 0, vcc
v_lshl_add_u32 v254, v255, 6, v254
v_cmp_gt_u32 vcc, 8, v254
v_mov_b32 v249, 0x3f803f80
s_nop 0
v_cndmask_b32 v245, v95, 0, vcc
v_subrev_u32 v252, 2, v25
v_cmp_gt_u32 vcc, 8, v252
v_add_u32 v253, 16, v252
v_add_u32 v254, s51, v253
v_cndmask_b32 v234, v95, 0, vcc
v_cmp_gt_u32 vcc, 8, v253
v_add_u32 v255, 2, v94
v_add_u32 v255, s50, v255
v_cndmask_b32 v238, v95, 0, vcc
v_cmp_gt_u32 vcc, 8, v254
v_lshrrev_b32 v255, 4, v255
v_subrev_u32 v254, s50, v254
v_cndmask_b32 v242, v95, 0, vcc
v_lshl_add_u32 v254, v255, 6, v254
v_cmp_gt_u32 vcc, 8, v254
v_mov_b32 v250, 0x3f803f80
s_nop 0
v_cndmask_b32 v246, v95, 0, vcc
v_subrev_u32 v252, 3, v25
v_cmp_gt_u32 vcc, 8, v252
v_add_u32 v253, 16, v252
v_add_u32 v254, s51, v253
v_cndmask_b32 v235, v95, 0, vcc
v_cmp_gt_u32 vcc, 8, v253
v_add_u32 v255, 3, v94
v_add_u32 v255, s50, v255
v_cndmask_b32 v239, v95, 0, vcc
v_cmp_gt_u32 vcc, 8, v254
v_lshrrev_b32 v255, 4, v255
v_subrev_u32 v254, s50, v254
v_cndmask_b32 v243, v95, 0, vcc
v_lshl_add_u32 v254, v255, 6, v254
v_cmp_gt_u32 vcc, 8, v254
v_mov_b32 v251, 0x3f803f80
s_nop 0
v_cndmask_b32 v247, v95, 0, vcc
v_and_b32 v94, 3, v24
v_min_u32 v90, 0, v94
v_max_u32 v91, 1, v94
v_bfe_u32 v95, v24, 2, 3
v_lshlrev_b32 v95, 7, v95
s_lshl_b32 s50, s41, 12
s_add_u32 s50, s50, 35782656
v_add_u32 v95, s50, v95
v_lshl_add_u32 v92, v90, 10, v95
v_lshl_add_u32 v93, v91, 10, v95
s_lshl_b32 s50, %1, 11
s_add_u32 s50, s50, 35684352
v_bfe_u32 v95, v24, 2, 1
v_lshl_add_u32 v95, v95, 7, s50
v_lshl_add_u32 v90, v90, 8, v95
v_lshl_add_u32 v91, v91, 8, v95
s_cmp_eq_u32 %1, 0
s_cselect_b32 s50, 64, 32
v_cmp_le_u32 vcc, s50, v24
s_nop 1
v_cndmask_b32 v92, v92, v90, vcc
v_cndmask_b32 v93, v93, v91, vcc
v_mov_b32 v25, v92
v_mov_b32 v24, v93
LX_Q0_fl:
s_mov_b64 exec, 0
s_cmp_eq_u32 s61, 1
s_cbranch_scc1 LX_Q0_pad1
s_cmp_eq_u32 s61, 2
s_cbranch_scc1 LX_Q0_pad2
s_cmp_eq_u32 s61, 3
s_cbranch_scc1 LX_Q0_pad3
LX_Q0_padret:
s_mov_b64 exec, -1
LX_w 0
s_barrier
LX_t 224,9,0
LX_t 226,9,2048
LX_t 228,9,4096
LX_t 230,9,6144
LX_t 232,10,0
LX_t 234,10,2048
LX_t 236,10,4096
LX_t 238,10,6144
s_mov_b32 s40, 262144
LX_1:
buffer_load_dwordx2 v[14:15], v25, s[20:23], 0 offen sc1
s_waitcnt vmcnt(0)
v_cmp_ne_u64 vcc, s[56:57], v[14:15]
s_sub_u32 s40, s40, 1
s_cbranch_scc1 LX_1d
s_nop 2
s_cbranch_vccnz LX_1
LX_1d:
v_add_u32 v14, 65536, v10
v_add_u32 v15, 65536, v11
s_add_u32 s33, s38, 0
LX_l 0,1,0
LX_l 16,23,0
LX_l 4,1,1024
LX_l 20,23,1024
LX_l 8,1,2048
LX_l 24,23,2048
LX_l 12,1,3072
LX_l 28,23,3072
s_add_u32 s36, s45, 0
buffer_load_dwordx4 a[160:163], v8, s[20:23], s36 offen sc1
s_add_u32 s36, s45, 4096
buffer_load_dwordx4 a[164:167], v8, s[20:23], s36 offen sc1
s_add_u32 s33, s38, 8192
LX_Q0_pad1:
s_cbranch_execz LX_Q0_pad4
LX_l 32,1,0
LX_l 36,1,1024
LX_l 40,1,2048
LX_l 44,1,3072
LX_l 48,23,0
LX_l 52,23,1024
LX_l 56,23,2048
LX_l 60,23,3072
s_waitcnt vmcnt(16)
LX_w 4
v_mfma_f32_16x16x32_bf16 v[32:35], a[224:227], a[0:3], 0
s_add_u32 s36, s45, 8192
LX_aa 32,228,16
s_waitcnt vmcnt(14)
buffer_load_dwordx4 a[168:171], v8, s[20:23], s36 offen sc1
v_mfma_f32_16x16x32_bf16 v[36:39], a[224:227], a[4:7], 0
s_add_u32 s36, s45, 12288
buffer_load_dwordx4 a[172:175], v8, s[20:23], s36 offen sc1
LX_aa 36,228,20
s_waitcnt vmcnt(14)
LX_t 240,11,0
v_mfma_f32_16x16x32_bf16 v[40:43], a[224:227], a[8:11], 0
LX_t 242,11,2048
LX_aa 40,228,24
s_waitcnt vmcnt(12)
LX_t 244,11,4096
v_mfma_f32_16x16x32_bf16 v[44:47], a[224:227], a[12:15], 0
s_mov_b32 s53, 0
LX_t 246,11,6144
LX_aa 44,228,28
LX_w 4
buffer_store_dwordx4 a[200:203], v19, s[20:23], s53 offen sc1
v_mfma_f32_16x16x32_bf16 v[48:51], a[232:235], a[0:3], 0
s_mov_b32 s53, 8192
LX_aa 48,236,16
buffer_store_dwordx4 a[204:207], v19, s[20:23], s53 offen sc1
v_mfma_f32_16x16x32_bf16 v[52:55], a[232:235], a[4:7], 0
s_mov_b32 s53, 16384
buffer_store_dwordx4 a[208:211], v19, s[20:23], s53 offen sc1
LX_aa 52,236,20
LX_t 248,12,0
v_mfma_f32_16x16x32_bf16 v[56:59], a[232:235], a[8:11], 0
LX_t 250,12,2048
LX_aa 56,236,24
LX_t 252,12,4096
v_mfma_f32_16x16x32_bf16 v[60:63], a[232:235], a[12:15], 0
s_mov_b32 s53, 24576
LX_t 254,12,6144
LX_aa 60,236,28
LX_w 4
buffer_store_dwordx4 a[212:215], v19, s[20:23], s53 offen sc1
v_mfma_f32_16x16x32_bf16 v[64:67], a[240:243], a[0:3], 0
LX_aa 64,244,16
LX_t 224,17,32768
v_mfma_f32_16x16x32_bf16 v[68:71], a[240:243], a[4:7], 0
LX_t 226,17,34816
LX_aa 68,244,20
LX_t 228,17,36864
v_mfma_f32_16x16x32_bf16 v[72:75], a[240:243], a[8:11], 0
LX_t 230,17,38912
LX_aa 72,244,24
LX_t 232,9,32768
v_mfma_f32_16x16x32_bf16 v[76:79], a[240:243], a[12:15], 0
LX_t 234,9,34816
LX_aa 76,244,28
LX_w 6
LX_t 236,9,36864
v_mfma_f32_16x16x32_bf16 v[80:83], a[248:251], a[0:3], 0
)ASM"
R"ASM(s_add_u32 s33, s38, 16384
LX_t 238,9,38912
LX_aa 80,252,16
LX_l 64,1,0
v_mfma_f32_16x16x32_bf16 v[84:87], a[248:251], a[4:7], 0
LX_l 68,1,1024
LX_aa 84,252,20
v_mfma_f32_16x16x32_bf16 v[88:91], a[248:251], a[8:11], 0
LX_l 72,1,2048
LX_aa 88,252,24
LX_t 240,10,32768
v_mfma_f32_16x16x32_bf16 v[92:95], a[248:251], a[12:15], 0
LX_t 242,10,34816
LX_aa 92,252,28
LX_w 6
s_waitcnt vmcnt(17)
LX_t 244,10,36864
v_mfma_f32_16x16x32_bf16 v[224:227], a[224:227], a[160:163], 0
LX_t 246,10,38912
LX_aa 224,228,164
LX_w 4
v_mfma_f32_16x16x32_bf16 v[96:99], a[232:235], a[0:3], 0
LX_l 76,1,3072
LX_aa 96,236,16
LX_l 80,23,0
v_mfma_f32_16x16x32_bf16 v[100:103], a[232:235], a[4:7], 0
LX_aa 100,236,20
LX_t 248,11,32768
v_mfma_f32_16x16x32_bf16 v[104:107], a[232:235], a[8:11], 0
LX_t 250,11,34816
LX_aa 104,236,24
LX_t 252,11,36864
v_mfma_f32_16x16x32_bf16 v[108:111], a[232:235], a[12:15], 0
LX_t 254,11,38912
LX_aa 108,236,28
LX_w 4
LX_l 84,23,1024
v_mfma_f32_16x16x32_bf16 v[112:115], a[240:243], a[0:3], 0
LX_aa 112,244,16
LX_l 88,23,2048
v_mfma_f32_16x16x32_bf16 v[116:119], a[240:243], a[4:7], 0
LX_l 92,23,3072
LX_aa 116,244,20
LX_t 224,12,32768
v_mfma_f32_16x16x32_bf16 v[120:123], a[240:243], a[8:11], 0
LX_t 226,12,34816
LX_aa 120,244,24
LX_t 228,12,36864
v_mfma_f32_16x16x32_bf16 v[124:127], a[240:243], a[12:15], 0
LX_t 230,12,38912
LX_aa 124,244,28
LX_w 4
s_add_u32 s36, s45, 16384
v_mfma_f32_16x16x32_bf16 v[128:131], a[248:251], a[0:3], 0
buffer_load_dwordx4 a[176:179], v8, s[20:23], s36 offen sc1
LX_aa 128,252,16
s_add_u32 s36, s45, 20480
v_mfma_f32_16x16x32_bf16 v[132:135], a[248:251], a[4:7], 0
buffer_load_dwordx4 a[180:183], v8, s[20:23], s36 offen sc1
LX_aa 132,252,20
LX_t 232,13,0
v_mfma_f32_16x16x32_bf16 v[136:139], a[248:251], a[8:11], 0
LX_t 234,13,2048
LX_aa 136,252,24
LX_t 236,13,4096
v_mfma_f32_16x16x32_bf16 v[140:143], a[248:251], a[12:15], 0
LX_t 238,13,6144
LX_aa 140,252,28
LX_w 4
v_mfma_f32_16x16x32_bf16 v[144:147], a[224:227], a[0:3], 0
buffer_load_dword a223, v21, s[16:19], 0 offen
LX_aa 144,228,16
LX_t 240,20,0
v_add_u32 v21, 786432, v21
v_mfma_f32_16x16x32_bf16 v[148:151], a[224:227], a[4:7], 0
LX_t 242,20,2048
LX_aa 148,228,20
LX_t 244,20,4096
v_mfma_f32_16x16x32_bf16 v[152:155], a[224:227], a[8:11], 0
LX_t 246,20,6144
LX_aa 152,228,24
LX_t 248,14,0
v_mfma_f32_16x16x32_bf16 v[156:159], a[224:227], a[12:15], 0
LX_t 250,14,2048
LX_aa 156,228,28
LX_w 6
LX_t 252,14,4096
v_mfma_f32_16x16x32_bf16 v[160:163], a[0:3], a[232:235], 0
s_mov_b32 s35, 1179648
s_add_u32 m0, s39, 24576
LX_t 254,14,6144
LX_aa 160,16,236
buffer_load_dwordx4 v2, s[16:19], s35 offen  lds
v_mfma_f32_16x16x32_bf16 v[164:167], a[4:7], a[232:235], 0
s_add_u32 m0, s39, 25600
LX_aa 164,20,236
buffer_load_dwordx4 v3, s[16:19], s35 offen  lds
v_mfma_f32_16x16x32_bf16 v[168:171], a[8:11], a[232:235], 0
LX_aa 168,24,236
LX_t 224,15,0
v_mfma_f32_16x16x32_bf16 v[172:175], a[12:15], a[232:235], 0
LX_t 226,15,2048
LX_aa 172,28,236
LX_w 6
LX_t 228,15,4096
v_mfma_f32_16x16x32_bf16 v[228:231], a[160:163], a[240:243], 0
s_add_u32 m0, s39, 57344
LX_t 230,15,6144
LX_aa 228,164,244
LX_w 4
buffer_load_dwordx4 v4, s[16:19], s35 offen  lds
v_mfma_f32_16x16x32_bf16 v[176:179], a[0:3], a[248:251], 0
s_add_u32 m0, s39, 58368
s_nop 0
buffer_load_dwordx4 v5, s[16:19], s35 offen  lds
LX_aa 176,16,252
s_add_u32 m0, s39, 90112
v_mfma_f32_16x16x32_bf16 v[180:183], a[4:7], a[248:251], 0
buffer_load_dwordx4 v6, s[16:19], s35 offen  lds
LX_aa 180,20,252
LX_t 232,16,0
v_mfma_f32_16x16x32_bf16 v[184:187], a[8:11], a[248:251], 0
LX_t 234,16,2048
LX_aa 184,24,252
LX_t 236,16,4096
v_mfma_f32_16x16x32_bf16 v[188:191], a[12:15], a[248:251], 0
LX_t 238,16,6144
LX_aa 188,28,252
LX_w 4
s_add_u32 m0, s39, 91136
v_mfma_f32_16x16x32_bf16 v[192:195], a[0:3], a[224:227], 0
buffer_load_dwordx4 v7, s[16:19], s35 offen  lds
LX_aa 192,16,228
v_mfma_f32_16x16x32_bf16 v[196:199], a[4:7], a[224:227], 0
LX_aa 196,20,228
s_waitcnt vmcnt(47) lgkmcnt(0)
s_barrier
LX_t 240,9,8192
v_mfma_f32_16x16x32_bf16 v[200:203], a[8:11], a[224:227], 0
LX_t 242,9,10240
LX_aa 200,24,228
LX_t 244,9,12288
v_mfma_f32_16x16x32_bf16 v[204:207], a[12:15], a[224:227], 0
LX_t 246,9,14336
LX_aa 204,28,228
v_mfma_f32_16x16x32_bf16 v[208:211], a[0:3], a[232:235], 0
LX_aa 208,16,236
v_mfma_f32_16x16x32_bf16 v[212:215], a[4:7], a[232:235], 0
LX_aa 212,20,236
LX_t 248,10,8192
v_mfma_f32_16x16x32_bf16 v[216:219], a[8:11], a[232:235], 0
LX_t 250,10,10240
LX_aa 216,24,236
LX_t 252,10,12288
v_mfma_f32_16x16x32_bf16 v[220:223], a[12:15], a[232:235], 0
LX_t 254,10,14336
LX_aa 220,28,236
LX_w 4
s_waitcnt vmcnt(21)
LX_aa 32,240,32
s_add_u32 s33, s38, 24576
LX_aa 32,244,48
LX_l 96,1,0
LX_aa 36,240,36
LX_aa 36,244,52
LX_t 224,11,8192
LX_aa 40,240,40
LX_t 226,11,10240
LX_aa 40,244,56
LX_t 228,11,12288
LX_aa 44,240,44
LX_t 230,11,14336
LX_aa 44,244,60
LX_w 4
LX_l 100,1,1024
LX_aa 48,248,32
LX_aa 48,252,48
LX_l 104,1,2048
LX_aa 52,248,36
LX_aa 52,252,52
LX_t 232,12,8192
LX_aa 56,248,40
LX_t 234,12,10240
LX_aa 56,252,56
LX_t 236,12,12288
LX_aa 60,248,44
LX_t 238,12,14336
LX_aa 60,252,60
LX_w 4
LX_l 108,1,3072
LX_aa 64,224,32
LX_aa 64,228,48
LX_t 240,17,40960
LX_aa 68,224,36
LX_t 242,17,43008
LX_aa 68,228,52
LX_t 244,17,45056
LX_aa 72,224,40
LX_t 246,17,47104
LX_aa 72,228,56
LX_t 248,9,40960
LX_aa 76,224,44
LX_t 250,9,43008
LX_aa 76,228,60
LX_w 6
LX_t 252,9,45056
LX_aa 80,232,32
LX_t 254,9,47104
LX_aa 80,236,48
LX_l 112,23,0
LX_aa 84,232,36
LX_aa 84,236,52
LX_l 116,23,1024
LX_aa 88,232,40
LX_aa 88,236,56
LX_t 224,10,40960
LX_aa 92,232,44
LX_t 226,10,43008
LX_aa 92,236,60
LX_w 6
LX_t 228,10,45056
LX_aa 224,240,168
LX_t 230,10,47104
LX_aa 224,244,172
LX_w 4
LX_l 120,23,2048
LX_aa 96,248,32
LX_aa 96,252,48
LX_l 124,23,3072
LX_aa 100,248,36
LX_aa 100,252,52
LX_t 232,11,40960
LX_aa 104,248,40
LX_t 234,11,43008
LX_aa 104,252,56
LX_t 236,11,45056
LX_aa 108,248,44
s_add_u32 s36, s45, 24576
LX_t 238,11,47104
LX_aa 108,252,60
LX_w 4
buffer_load_dwordx4 a[184:187], v8, s[20:23], s36 offen sc1
LX_aa 112,224,32
s_add_u32 s36, s45, 28672
LX_aa 112,228,48
buffer_load_dwordx4 a[188:191], v8, s[20:23], s36 offen sc1
LX_aa 116,224,36
LX_aa 116,228,52
LX_t 240,12,40960
LX_aa 120,224,40
LX_t 242,12,43008
LX_aa 120,228,56
LX_t 244,12,45056
LX_aa 124,224,44
LX_t 246,12,47104
LX_aa 124,228,60
LX_w 4
buffer_load_dword a223, v21, s[16:19], 0 offen
LX_aa 128,232,32
s_waitcnt vmcnt(28)
buffer_store_dwordx2 v[26:27], v18, s[20:23], 0 offen sc1
v_add_u32 v21, 786432, v21
LX_aa 128,236,48
LX_aa 132,232,36
buffer_load_dword a223, v22, s[28:31], 0 offen
LX_aa 132,236,52
LX_t 248,13,8192
v_add_u32 v22, 917504, v22
LX_aa 136,232,40
LX_t 250,13,10240
LX_aa 136,236,56
LX_t 252,13,12288
LX_aa 140,232,44
LX_t 254,13,14336
LX_aa 140,236,60
LX_w 4
s_mov_b32 s35, 1572864
s_add_u32 m0, s39, 0
LX_aa 144,240,32
buffer_load_dwordx4 v2, s[16:19], s35 offen  lds
LX_aa 144,244,48
LX_t 224,20,8192
LX_aa 148,240,36
LX_t 226,20,10240
LX_aa 148,244,52
LX_t 228,20,12288
LX_aa 152,240,40
LX_t 230,20,14336
LX_aa 152,244,56
LX_t 232,14,8192
LX_aa 156,240,44
LX_t 234,14,10240
LX_aa 156,244,60
LX_w 6
LX_t 236,14,12288
LX_aa 160,32,248
LX_t 238,14,14336
LX_aa 160,48,252
s_add_u32 m0, s39, 1024
LX_aa 164,36,248
buffer_load_dwordx4 v3, s[16:19], s35 offen  lds
LX_aa 164,52,252
s_add_u32 m0, s39, 32768
LX_aa 168,40,248
buffer_load_dwordx4 v4, s[16:19], s35 offen  lds
LX_aa 168,56,252
LX_t 240,15,8192
LX_aa 172,44,248
LX_t 242,15,10240
LX_aa 172,60,252
LX_w 6
LX_t 244,15,12288
LX_aa 228,168,224
LX_t 246,15,14336
LX_aa 228,172,228
LX_w 4
s_add_u32 m0, s39, 33792
LX_aa 176,32,232
buffer_load_dwordx4 v5, s[16:19], s35 offen  lds
LX_aa 176,48,236
s_add_u32 m0, s39, 65536
LX_aa 180,36,232
buffer_load_dwordx4 v6, s[16:19], s35 offen  lds
LX_aa 180,52,236
LX_t 248,16,8192
LX_aa 184,40,232
LX_t 250,16,10240
LX_aa 184,56,236
LX_t 252,16,12288
LX_aa 188,44,232
LX_t 254,16,14336
LX_aa 188,60,236
LX_w 4
s_add_u32 m0, s39, 66560
LX_aa 192,32,240
buffer_load_dwordx4 v7, s[16:19], s35 offen  lds
LX_aa 192,48,244
LX_aa 196,36,240
LX_aa 196,52,244
s_waitcnt vmcnt(60) lgkmcnt(0)
s_barrier
LX_t 224,9,16384
LX_aa 200,40,240
LX_t 226,9,18432
LX_aa 200,56,244
LX_t 228,9,20480
LX_aa 204,44,240
LX_t 230,9,22528
LX_aa 204,60,244
LX_aa 208,32,248
LX_aa 208,48,252
LX_aa 212,36,248
LX_aa 212,52,252
LX_t 232,10,16384
LX_aa 216,40,248
LX_t 234,10,18432
LX_aa 216,56,252
LX_t 236,10,20480
LX_aa 220,44,248
LX_t 238,10,22528
LX_aa 220,60,252
LX_w 4
s_waitcnt vmcnt(26)
LX_aa 32,224,64
LX_aa 32,228,80
buffer_load_dword a223, v21, s[16:19], 0 offen
LX_aa 36,224,68
v_add_u32 v21, 786432, v21
LX_aa 36,228,84
LX_t 240,11,16384
LX_aa 40,224,72
LX_t 242,11,18432
LX_aa 40,228,88
LX_t 244,11,20480
LX_aa 44,224,76
LX_t 246,11,22528
LX_aa 44,228,92
LX_w 4
LX_aa 48,232,64
LX_aa 48,236,80
buffer_load_dword a223, v22, s[28:31], 0 offen
LX_aa 52,232,68
v_add_u32 v22, 917504, v22
LX_aa 52,236,84
LX_t 248,12,16384
LX_aa 56,232,72
LX_t 250,12,18432
LX_aa 56,236,88
LX_t 252,12,20480
LX_aa 60,232,76
LX_t 254,12,22528
LX_aa 60,236,92
LX_w 4
LX_aa 64,240,64
LX_aa 64,244,80
LX_t 224,17,49152
LX_aa 68,240,68
LX_t 226,17,51200
LX_aa 68,244,84
LX_t 228,17,53248
LX_aa 72,240,72
LX_t 230,17,55296
LX_aa 72,244,88
LX_t 232,9,49152
LX_aa 76,240,76
LX_t 234,9,51200
LX_aa 76,244,92
LX_w 6
LX_t 236,9,53248
LX_aa 80,248,64
LX_t 238,9,55296
LX_aa 80,252,80
LX_aa 84,248,68
s_mov_b32 s35, 1966080
s_add_u32 m0, s39, 8192
buffer_load_dwordx2 v[18:19], v24, s[20:23], 0 offen sc1
LX_aa 84,252,84
buffer_load_dwordx4 v2, s[16:19], s35 offen  lds
LX_aa 88,248,72
LX_aa 88,252,88
LX_t 240,10,49152
LX_aa 92,248,76
LX_t 242,10,51200
LX_aa 92,252,92
LX_w 6
LX_t 244,10,53248
LX_aa 224,224,176
LX_t 246,10,55296
LX_aa 224,228,180
LX_w 4
LX_aa 96,232,64
s_add_u32 m0, s39, 9216
LX_aa 96,236,80
buffer_load_dwordx4 v3, s[16:19], s35 offen  lds
LX_aa 100,232,68
LX_aa 100,236,84
LX_t 248,11,49152
LX_aa 104,232,72
LX_t 250,11,51200
LX_aa 104,236,88
LX_t 252,11,53248
LX_aa 108,232,76
LX_t 254,11,55296
LX_aa 108,236,92
LX_w 4
LX_aa 112,240,64
LX_aa 112,244,80
s_add_u32 m0, s39, 40960
LX_aa 116,240,68
buffer_load_dwordx4 v4, s[16:19], s35 offen  lds
LX_aa 116,244,84
LX_t 224,12,49152
LX_aa 120,240,72
LX_t 226,12,51200
LX_aa 120,244,88
LX_t 228,12,53248
LX_aa 124,240,76
LX_t 230,12,55296
LX_aa 124,244,92
LX_w 4
LX_aa 128,248,64
LX_aa 128,252,80
s_add_u32 m0, s39, 41984
LX_aa 132,248,68
LX_Q0_pad2:
s_cbranch_execz LX_Q0_pad5
buffer_load_dwordx4 v5, s[16:19], s35 offen  lds
LX_aa 132,252,84
LX_t 232,13,16384
LX_aa 136,248,72
LX_t 234,13,18432
LX_aa 136,252,88
LX_t 236,13,20480
LX_aa 140,248,76
LX_t 238,13,22528
LX_aa 140,252,92
LX_w 4
LX_aa 144,224,64
LX_aa 144,228,80
LX_t 240,20,16384
LX_aa 148,224,68
LX_t 242,20,18432
LX_aa 148,228,84
LX_t 244,20,20480
LX_aa 152,224,72
LX_t 246,20,22528
LX_aa 152,228,88
LX_t 248,14,16384
LX_aa 156,224,76
LX_t 250,14,18432
LX_aa 156,228,92
LX_w 6
LX_t 252,14,20480
LX_aa 160,64,232
LX_t 254,14,22528
LX_aa 160,80,236
LX_aa 164,68,232
s_add_u32 m0, s39, 73728
LX_aa 164,84,236
buffer_load_dwordx4 v6, s[16:19], s35 offen  lds
LX_aa 168,72,232
LX_aa 168,88,236
LX_t 224,15,16384
LX_aa 172,76,232
LX_t 226,15,18432
LX_aa 172,92,236
LX_w 6
LX_t 228,15,20480
LX_aa 228,176,240
LX_t 230,15,22528
LX_aa 228,180,244
LX_w 4
LX_aa 176,64,248
s_add_u32 m0, s39, 74752
LX_aa 176,80,252
buffer_load_dwordx4 v7, s[16:19], s35 offen  lds
LX_aa 180,68,248
LX_aa 180,84,252
LX_t 232,16,16384
LX_aa 184,72,248
LX_t 234,16,18432
LX_aa 184,88,252
LX_t 236,16,20480
LX_aa 188,76,248
LX_t 238,16,22528
LX_aa 188,92,252
LX_w 4
LX_aa 192,64,224
LX_aa 192,80,228
LX_aa 196,68,224
LX_aa 196,84,228
s_waitcnt vmcnt(28) lgkmcnt(0)
s_barrier
LX_t 240,9,24576
LX_aa 200,72,224
LX_t 242,9,26624
LX_aa 200,88,228
LX_t 244,9,28672
LX_aa 204,76,224
LX_t 246,9,30720
LX_aa 204,92,228
LX_aa 208,64,232
LX_aa 208,80,236
LX_aa 212,68,232
s_waitcnt vmcnt(6)
v_cmp_ne_u64 vcc, s[56:57], v[18:19]
LX_aa 212,84,236
LX_t 248,10,24576
LX_aa 216,72,232
s_cbranch_vccnz LX_c2
LX_c2r:
LX_t 250,10,26624
LX_aa 216,88,236
LX_t 252,10,28672
LX_aa 220,76,232
LX_t 254,10,30720
LX_aa 220,92,236
LX_w 4
s_waitcnt vmcnt(18)
)ASM"
R"ASM(LX_aa 32,240,96
s_add_u32 s33, s38, 32768
LX_aa 32,244,112
LX_l 128,1,0
LX_aa 36,240,100
LX_l 132,1,1024
LX_aa 36,244,116
LX_t 224,11,24576
LX_aa 40,240,104
LX_t 226,11,26624
LX_aa 40,244,120
LX_t 228,11,28672
LX_aa 44,240,108
LX_t 230,11,30720
LX_aa 44,244,124
LX_w 4
LX_aa 48,248,96
LX_l 136,1,2048
LX_aa 48,252,112
LX_aa 52,248,100
LX_l 140,1,3072
LX_aa 52,252,116
LX_t 232,12,24576
LX_aa 56,248,104
LX_t 234,12,26624
LX_aa 56,252,120
LX_t 236,12,28672
LX_aa 60,248,108
LX_t 238,12,30720
LX_aa 60,252,124
LX_w 4
LX_l 144,23,0
LX_aa 64,224,96
LX_aa 64,228,112
LX_t 240,17,57344
LX_aa 68,224,100
LX_t 242,17,59392
LX_aa 68,228,116
LX_t 244,17,61440
LX_aa 72,224,104
LX_t 246,17,63488
LX_aa 72,228,120
LX_t 248,9,57344
LX_aa 76,224,108
LX_t 250,9,59392
LX_aa 76,228,124
LX_w 6
LX_t 252,9,61440
LX_aa 80,232,96
LX_t 254,9,63488
LX_aa 80,236,112
LX_l 148,23,1024
LX_aa 84,232,100
LX_aa 84,236,116
LX_l 152,23,2048
LX_aa 88,232,104
LX_l 156,23,3072
LX_aa 88,236,120
LX_t 224,10,57344
LX_aa 92,232,108
LX_t 226,10,59392
LX_aa 92,236,124
LX_w 6
LX_t 228,10,61440
LX_aa 224,240,184
LX_t 230,10,63488
LX_aa 224,244,188
LX_w 4
s_add_u32 s36, s45, 32768
LX_aa 96,248,96
buffer_load_dwordx4 a[192:195], v8, s[20:23], s36 offen sc1
LX_aa 96,252,112
s_add_u32 s36, s45, 36864
LX_aa 100,248,100
buffer_load_dwordx4 a[196:199], v8, s[20:23], s36 offen sc1
LX_aa 100,252,116
LX_t 232,11,57344
LX_aa 104,248,104
LX_t 234,11,59392
LX_aa 104,252,120
LX_t 236,11,61440
LX_aa 108,248,108
s_add_u32 s33, s38, 40960
LX_t 238,11,63488
LX_aa 108,252,124
LX_w 4
LX_l 0,1,0
LX_aa 112,224,96
LX_aa 112,228,112
LX_l 4,1,1024
LX_aa 116,224,100
LX_aa 116,228,116
LX_t 240,12,57344
LX_aa 120,224,104
LX_t 242,12,59392
LX_aa 120,228,120
LX_t 244,12,61440
LX_aa 124,224,108
LX_t 246,12,63488
LX_aa 124,228,124
LX_w 4
LX_l 8,1,2048
LX_aa 128,232,96
LX_l 12,1,3072
LX_aa 128,236,112
LX_aa 132,232,100
buffer_load_dword a223, v21, s[16:19], 0 offen
LX_aa 132,236,116
LX_t 248,13,24576
v_add_u32 v21, 786432, v21
LX_aa 136,232,104
LX_t 250,13,26624
LX_aa 136,236,120
LX_t 252,13,28672
LX_aa 140,232,108
LX_t 254,13,30720
LX_aa 140,236,124
LX_w 4
LX_aa 144,240,96
buffer_load_dword a223, v22, s[28:31], 0 offen
LX_aa 144,244,112
LX_t 224,20,24576
v_add_u32 v22, 917504, v22
LX_aa 148,240,100
LX_t 226,20,26624
LX_aa 148,244,116
LX_t 228,20,28672
LX_aa 152,240,104
LX_t 230,20,30720
LX_aa 152,244,120
LX_t 232,14,24576
LX_aa 156,240,108
LX_t 234,14,26624
LX_aa 156,244,124
LX_w 6
LX_t 236,14,28672
LX_aa 160,96,248
s_mov_b32 s35, 2359296
s_add_u32 m0, s39, 16384
LX_t 238,14,30720
LX_aa 160,112,252
buffer_load_dwordx4 v2, s[16:19], s35 offen  lds
LX_aa 164,100,248
s_add_u32 m0, s39, 17408
LX_aa 164,116,252
buffer_load_dwordx4 v3, s[16:19], s35 offen  lds
LX_aa 168,104,248
LX_aa 168,120,252
LX_t 240,15,24576
LX_aa 172,108,248
LX_t 242,15,26624
LX_aa 172,124,252
LX_w 6
LX_t 244,15,28672
LX_aa 228,184,224
s_add_u32 m0, s39, 49152
LX_t 246,15,30720
LX_aa 228,188,228
LX_w 4
buffer_load_dwordx4 v4, s[16:19], s35 offen  lds
LX_aa 176,96,232
s_add_u32 m0, s39, 50176
s_nop 0
buffer_load_dwordx4 v5, s[16:19], s35 offen  lds
LX_aa 176,112,236
s_add_u32 m0, s39, 81920
LX_aa 180,100,232
buffer_load_dwordx4 v6, s[16:19], s35 offen  lds
LX_aa 180,116,236
LX_t 248,16,24576
LX_aa 184,104,232
LX_t 250,16,26624
LX_aa 184,120,236
LX_t 252,16,28672
LX_aa 188,108,232
LX_t 254,16,30720
LX_aa 188,124,236
LX_w 4
s_add_u32 m0, s39, 82944
LX_aa 192,96,240
buffer_load_dwordx4 v7, s[16:19], s35 offen  lds
LX_aa 192,112,244
LX_aa 196,100,240
LX_aa 196,116,244
s_waitcnt vmcnt(31) lgkmcnt(0)
s_barrier
LX_t 224,9,0
LX_aa 200,104,240
LX_t 226,9,2048
LX_aa 200,120,244
LX_t 228,9,4096
LX_aa 204,108,240
LX_t 230,9,6144
LX_aa 204,124,244
LX_aa 208,96,248
LX_aa 208,112,252
LX_aa 212,100,248
LX_aa 212,116,252
LX_t 232,10,0
LX_aa 216,104,248
LX_t 234,10,2048
LX_aa 216,120,252
LX_t 236,10,4096
LX_aa 220,108,248
LX_t 238,10,6144
LX_aa 220,124,252
LX_w 4
s_waitcnt vmcnt(12)
LX_aa 32,224,128
LX_aa 32,228,144
LX_l 16,23,0
LX_aa 36,224,132
LX_l 20,23,1024
LX_aa 36,228,148
LX_t 240,11,0
s_or_b32 s50, %2, %3
s_or_b32 s50, s50, s61
s_cbranch_scc1 LX_Q0_noep
s_lshl_b32 s50, %1, 8
s_add_u32 s50, s50, 35651584
buffer_store_dwordx2 v[26:27], v30, s[20:23], s50 offen offset:0 sc1
LX_Q0_noep:
LX_aa 40,224,136
LX_t 242,11,2048
LX_aa 40,228,152
LX_t 244,11,4096
LX_aa 44,224,140
LX_t 246,11,6144
LX_aa 44,228,156
LX_w 4
LX_aa 48,232,128
LX_l 24,23,2048
LX_aa 48,236,144
LX_aa 52,232,132
LX_l 28,23,3072
LX_aa 52,236,148
LX_t 248,12,0
LX_aa 56,232,136
LX_t 250,12,2048
LX_aa 56,236,152
LX_t 252,12,4096
LX_aa 60,232,140
LX_t 254,12,6144
LX_aa 60,236,156
LX_w 4
s_add_u32 s36, s45, 40960
LX_aa 64,240,128
buffer_load_dwordx4 a[160:163], v8, s[20:23], s36 offen sc1
LX_aa 64,244,144
LX_t 224,17,32768
LX_aa 68,240,132
LX_t 226,17,34816
LX_aa 68,244,148
LX_t 228,17,36864
LX_aa 72,240,136
LX_t 230,17,38912
LX_aa 72,244,152
LX_t 232,9,32768
LX_aa 76,240,140
LX_t 234,9,34816
LX_aa 76,244,156
LX_w 6
LX_t 236,9,36864
LX_aa 80,248,128
LX_t 238,9,38912
LX_aa 80,252,144
s_add_u32 s36, s45, 45056
LX_aa 84,248,132
buffer_load_dwordx4 a[164:167], v8, s[20:23], s36 offen sc1
LX_aa 84,252,148
s_add_u32 s33, s38, 49152
LX_aa 88,248,136
LX_l 32,1,0
LX_aa 88,252,152
LX_t 240,10,32768
LX_aa 92,248,140
LX_t 242,10,34816
LX_aa 92,252,156
LX_w 6
LX_t 244,10,36864
LX_aa 224,224,192
LX_t 246,10,38912
LX_aa 224,228,196
LX_w 4
LX_l 36,1,1024
LX_aa 96,232,128
LX_aa 96,236,144
LX_l 40,1,2048
LX_aa 100,232,132
LX_aa 100,236,148
LX_t 248,11,32768
LX_aa 104,232,136
LX_t 250,11,34816
LX_aa 104,236,152
LX_t 252,11,36864
LX_aa 108,232,140
LX_t 254,11,38912
LX_aa 108,236,156
LX_w 4
LX_l 44,1,3072
LX_aa 112,240,128
LX_aa 112,244,144
LX_l 48,23,0
LX_aa 116,240,132
LX_aa 116,244,148
LX_t 224,12,32768
LX_aa 120,240,136
LX_t 226,12,34816
LX_aa 120,244,152
LX_t 228,12,36864
LX_aa 124,240,140
LX_t 230,12,38912
LX_aa 124,244,156
LX_w 4
LX_l 52,23,1024
LX_aa 128,248,128
LX_aa 128,252,144
buffer_load_dword a223, v21, s[16:19], 0 offen
LX_aa 132,248,132
v_add_u32 v21, 786432, v21
LX_aa 132,252,148
LX_t 232,13,0
LX_aa 136,248,136
LX_t 234,13,2048
LX_aa 136,252,152
LX_t 236,13,4096
LX_aa 140,248,140
LX_t 238,13,6144
LX_aa 140,252,156
LX_w 4
buffer_load_dword a223, v22, s[28:31], 0 offen
v_add_u32 v22, 917504, v22
LX_aa 144,224,128
s_mov_b32 s35, 2752512
s_add_u32 m0, s39, 24576
s_nop 0
buffer_load_dwordx4 v2, s[16:19], s35 offen  lds
LX_aa 144,228,144
LX_t 240,20,0
LX_aa 148,224,132
LX_t 242,20,2048
LX_aa 148,228,148
LX_t 244,20,4096
LX_aa 152,224,136
LX_t 246,20,6144
LX_aa 152,228,152
LX_t 248,14,0
LX_aa 156,224,140
LX_t 250,14,2048
LX_aa 156,228,156
LX_w 6
LX_t 252,14,4096
LX_aa 160,128,232
LX_t 254,14,6144
LX_aa 160,144,236
s_add_u32 m0, s39, 25600
LX_aa 164,132,232
buffer_load_dwordx4 v3, s[16:19], s35 offen  lds
LX_aa 164,148,236
s_add_u32 m0, s39, 57344
LX_aa 168,136,232
buffer_load_dwordx4 v4, s[16:19], s35 offen  lds
LX_aa 168,152,236
LX_t 224,15,0
LX_aa 172,140,232
LX_t 226,15,2048
LX_aa 172,156,236
LX_w 6
LX_t 228,15,4096
LX_aa 228,192,240
LX_t 230,15,6144
LX_aa 228,196,244
LX_w 4
s_add_u32 m0, s39, 58368
LX_aa 176,128,248
buffer_load_dwordx4 v5, s[16:19], s35 offen  lds
LX_aa 176,144,252
s_add_u32 m0, s39, 90112
LX_aa 180,132,248
buffer_load_dwordx4 v6, s[16:19], s35 offen  lds
LX_aa 180,148,252
LX_t 232,16,0
LX_aa 184,136,248
LX_t 234,16,2048
LX_aa 184,152,252
LX_t 236,16,4096
LX_aa 188,140,248
LX_t 238,16,6144
LX_aa 188,156,252
LX_w 4
s_add_u32 m0, s39, 91136
LX_aa 192,128,224
buffer_load_dwordx4 v7, s[16:19], s35 offen  lds
LX_aa 192,144,228
LX_aa 196,132,224
LX_aa 196,148,228
s_waitcnt vmcnt(42) lgkmcnt(0)
s_barrier
LX_t 240,9,8192
LX_aa 200,136,224
LX_t 242,9,10240
LX_aa 200,152,228
LX_t 244,9,12288
LX_aa 204,140,224
LX_t 246,9,14336
LX_aa 204,156,228
LX_aa 208,128,232
LX_aa 208,144,236
LX_aa 212,132,232
LX_aa 212,148,236
LX_t 248,10,8192
LX_aa 216,136,232
LX_t 250,10,10240
LX_aa 216,152,236
LX_t 252,10,12288
LX_aa 220,140,232
LX_t 254,10,14336
LX_aa 220,156,236
LX_w 4
s_waitcnt vmcnt(14)
LX_aa 32,240,0
LX_aa 32,244,16
LX_l 56,23,2048
LX_aa 36,240,4
LX_l 60,23,3072
LX_aa 36,244,20
LX_t 224,11,8192
LX_aa 40,240,8
LX_t 226,11,10240
LX_aa 40,244,24
LX_t 228,11,12288
LX_aa 44,240,12
LX_t 230,11,14336
LX_aa 44,244,28
LX_w 4
s_add_u32 s36, s45, 49152
LX_aa 48,248,0
buffer_load_dwordx4 a[168:171], v8, s[20:23], s36 offen sc1
LX_aa 48,252,16
s_add_u32 s36, s45, 53248
LX_aa 52,248,4
buffer_load_dwordx4 a[172:175], v8, s[20:23], s36 offen sc1
LX_aa 52,252,20
LX_t 232,12,8192
LX_aa 56,248,8
LX_Q0_pad3:
s_cbranch_execz LX_Q0_pad6
LX_t 234,12,10240
LX_aa 56,252,24
LX_t 236,12,12288
LX_aa 60,248,12
LX_t 238,12,14336
LX_aa 60,252,28
LX_w 4
s_add_u32 s33, s38, 57344
LX_aa 64,224,0
LX_l 64,1,0
LX_aa 64,228,16
LX_t 240,17,40960
LX_aa 68,224,4
LX_t 242,17,43008
LX_aa 68,228,20
LX_t 244,17,45056
LX_aa 72,224,8
LX_t 246,17,47104
LX_aa 72,228,24
LX_t 248,9,40960
LX_aa 76,224,12
LX_t 250,9,43008
LX_aa 76,228,28
LX_w 6
LX_t 252,9,45056
LX_aa 80,232,0
LX_t 254,9,47104
LX_aa 80,236,16
LX_aa 84,232,4
LX_l 68,1,1024
LX_aa 84,236,20
LX_aa 88,232,8
LX_l 72,1,2048
LX_aa 88,236,24
LX_t 224,10,40960
LX_aa 92,232,12
LX_t 226,10,43008
LX_aa 92,236,28
LX_w 6
LX_t 228,10,45056
LX_aa 224,240,160
LX_t 230,10,47104
LX_aa 224,244,164
LX_w 4
LX_l 76,1,3072
LX_aa 96,248,0
LX_aa 96,252,16
LX_l 80,23,0
LX_aa 100,248,4
LX_aa 100,252,20
LX_t 232,11,40960
LX_aa 104,248,8
LX_t 234,11,43008
LX_aa 104,252,24
LX_t 236,11,45056
LX_aa 108,248,12
LX_t 238,11,47104
LX_aa 108,252,28
LX_w 4
LX_l 84,23,1024
LX_aa 112,224,0
LX_aa 112,228,16
LX_l 88,23,2048
LX_aa 116,224,4
LX_aa 116,228,20
LX_t 240,12,40960
LX_aa 120,224,8
LX_t 242,12,43008
LX_aa 120,228,24
LX_t 244,12,45056
LX_aa 124,224,12
LX_t 246,12,47104
LX_aa 124,228,28
LX_w 4
LX_l 92,23,3072
LX_aa 128,232,0
LX_aa 128,236,16
buffer_load_dword a223, v21, s[16:19], 0 offen
LX_aa 132,232,4
v_add_u32 v21, 786432, v21
LX_aa 132,236,20
LX_t 248,13,8192
LX_aa 136,232,8
LX_t 250,13,10240
LX_aa 136,236,24
LX_t 252,13,12288
LX_aa 140,232,12
LX_t 254,13,14336
LX_aa 140,236,28
LX_w 4
buffer_load_dword a223, v22, s[28:31], 0 offen
v_add_u32 v22, 917504, v22
LX_aa 144,240,0
s_mov_b32 s35, 3145728
s_add_u32 m0, s39, 0
s_nop 0
buffer_load_dwordx4 v2, s[16:19], s35 offen  lds
LX_aa 144,244,16
LX_t 224,20,8192
LX_aa 148,240,4
LX_t 226,20,10240
LX_aa 148,244,20
LX_t 228,20,12288
LX_aa 152,240,8
LX_t 230,20,14336
LX_aa 152,244,24
LX_t 232,14,8192
LX_aa 156,240,12
LX_t 234,14,10240
LX_aa 156,244,28
LX_w 6
LX_t 236,14,12288
LX_aa 160,0,248
LX_t 238,14,14336
LX_aa 160,16,252
s_add_u32 m0, s39, 1024
LX_aa 164,4,248
buffer_load_dwordx4 v3, s[16:19], s35 offen  lds
LX_aa 164,20,252
s_add_u32 m0, s39, 32768
LX_aa 168,8,248
buffer_load_dwordx4 v4, s[16:19], s35 offen  lds
LX_aa 168,24,252
LX_t 240,15,8192
LX_aa 172,12,248
LX_t 242,15,10240
LX_aa 172,28,252
LX_w 6
LX_t 244,15,12288
LX_aa 228,160,224
LX_t 246,15,14336
LX_aa 228,164,228
LX_w 4
s_add_u32 m0, s39, 33792
LX_aa 176,0,232
buffer_load_dwordx4 v5, s[16:19], s35 offen  lds
LX_aa 176,16,236
s_add_u32 m0, s39, 65536
LX_aa 180,4,232
buffer_load_dwordx4 v6, s[16:19], s35 offen  lds
LX_aa 180,20,236
LX_t 248,16,8192
LX_aa 184,8,232
LX_t 250,16,10240
LX_aa 184,24,236
LX_t 252,16,12288
LX_aa 188,12,232
LX_t 254,16,14336
LX_aa 188,28,236
LX_w 4
s_add_u32 m0, s39, 66560
LX_aa 192,0,240
buffer_load_dwordx4 v7, s[16:19], s35 offen  lds
LX_aa 192,16,244
LX_aa 196,4,240
LX_aa 196,20,244
s_waitcnt vmcnt(40) lgkmcnt(0)
s_barrier
LX_t 224,9,16384
LX_aa 200,8,240
LX_t 226,9,18432
LX_aa 200,24,244
LX_t 228,9,20480
LX_aa 204,12,240
LX_t 230,9,22528
LX_aa 204,28,244
LX_aa 208,0,248
LX_aa 208,16,252
LX_aa 212,4,248
LX_aa 212,20,252
LX_t 232,10,16384
LX_aa 216,8,248
LX_t 234,10,18432
LX_aa 216,24,252
LX_t 236,10,20480
LX_aa 220,12,248
LX_t 238,10,22528
LX_aa 220,28,252
LX_w 4
s_waitcnt vmcnt(16)
LX_aa 32,224,32
s_add_u32 s36, s45, 57344
LX_aa 32,228,48
buffer_load_dwordx4 a[176:179], v8, s[20:23], s36 offen sc1
LX_aa 36,224,36
s_add_u32 s36, s45, 61440
buffer_load_dwordx4 a[180:183], v8, s[20:23], s36 offen sc1
LX_aa 36,228,52
LX_t 240,11,16384
LX_aa 40,224,40
)ASM"
R"ASM(LX_t 242,11,18432
LX_aa 40,228,56
LX_t 244,11,20480
LX_aa 44,224,44
LX_t 246,11,22528
LX_aa 44,228,60
LX_w 4
s_add_u32 s33, s38, 65536
LX_aa 48,232,32
LX_l 96,1,0
LX_aa 48,236,48
LX_aa 52,232,36
LX_l 100,1,1024
LX_aa 52,236,52
LX_t 248,12,16384
LX_aa 56,232,40
LX_t 250,12,18432
LX_aa 56,236,56
LX_t 252,12,20480
LX_aa 60,232,44
LX_t 254,12,22528
LX_aa 60,236,60
LX_w 4
LX_aa 64,240,32
LX_l 104,1,2048
LX_aa 64,244,48
LX_t 224,17,49152
LX_aa 68,240,36
LX_t 226,17,51200
LX_aa 68,244,52
LX_t 228,17,53248
LX_aa 72,240,40
LX_t 230,17,55296
LX_aa 72,244,56
LX_t 232,9,49152
LX_aa 76,240,44
LX_t 234,9,51200
LX_aa 76,244,60
LX_w 6
LX_t 236,9,53248
LX_aa 80,248,32
LX_t 238,9,55296
LX_aa 80,252,48
LX_aa 84,248,36
LX_l 108,1,3072
LX_aa 84,252,52
LX_aa 88,248,40
LX_l 112,23,0
LX_aa 88,252,56
LX_t 240,10,49152
LX_aa 92,248,44
LX_t 242,10,51200
LX_aa 92,252,60
LX_w 6
LX_t 244,10,53248
LX_aa 224,224,168
LX_t 246,10,55296
LX_aa 224,228,172
LX_w 4
LX_aa 96,232,32
LX_l 116,23,1024
LX_aa 96,236,48
LX_aa 100,232,36
LX_l 120,23,2048
LX_aa 100,236,52
LX_t 248,11,49152
LX_aa 104,232,40
LX_t 250,11,51200
LX_aa 104,236,56
LX_t 252,11,53248
LX_aa 108,232,44
LX_t 254,11,55296
LX_aa 108,236,60
LX_w 4
LX_aa 112,240,32
LX_l 124,23,3072
LX_aa 112,244,48
s_add_u32 s36, s45, 65536
LX_aa 116,240,36
buffer_load_dwordx4 a[184:187], v8, s[20:23], s36 offen sc1
LX_aa 116,244,52
LX_t 224,12,49152
LX_aa 120,240,40
LX_t 226,12,51200
LX_aa 120,244,56
LX_t 228,12,53248
LX_aa 124,240,44
LX_t 230,12,55296
LX_aa 124,244,60
LX_w 4
s_add_u32 s36, s45, 69632
LX_aa 128,248,32
buffer_load_dwordx4 a[188:191], v8, s[20:23], s36 offen sc1
LX_aa 128,252,48
LX_aa 132,248,36
buffer_load_dword a223, v22, s[28:31], 0 offen
LX_aa 132,252,52
LX_t 232,13,16384
v_add_u32 v22, 917504, v22
LX_aa 136,248,40
LX_t 234,13,18432
LX_aa 136,252,56
LX_t 236,13,20480
LX_aa 140,248,44
LX_t 238,13,22528
LX_aa 140,252,60
LX_w 4
s_mov_b32 s35, 3538944
s_add_u32 m0, s39, 8192
LX_aa 144,224,32
buffer_load_dwordx4 v2, s[16:19], s35 offen  lds
LX_aa 144,228,48
LX_t 240,20,16384
LX_aa 148,224,36
LX_t 242,20,18432
LX_aa 148,228,52
LX_t 244,20,20480
LX_aa 152,224,40
LX_t 246,20,22528
LX_aa 152,228,56
LX_t 248,14,16384
LX_aa 156,224,44
LX_t 250,14,18432
LX_aa 156,228,60
LX_w 6
LX_t 252,14,20480
LX_aa 160,32,232
LX_t 254,14,22528
LX_aa 160,48,236
s_add_u32 m0, s39, 9216
LX_aa 164,36,232
buffer_load_dwordx4 v3, s[16:19], s35 offen  lds
LX_aa 164,52,236
s_add_u32 m0, s39, 40960
LX_aa 168,40,232
buffer_load_dwordx4 v4, s[16:19], s35 offen  lds
LX_aa 168,56,236
LX_t 224,15,16384
LX_aa 172,44,232
LX_t 226,15,18432
LX_aa 172,60,236
LX_w 6
LX_t 228,15,20480
LX_aa 228,168,240
LX_t 230,15,22528
LX_aa 228,172,244
LX_w 4
s_add_u32 m0, s39, 41984
LX_aa 176,32,248
buffer_load_dwordx4 v5, s[16:19], s35 offen  lds
LX_aa 176,48,252
s_add_u32 m0, s39, 73728
LX_aa 180,36,248
buffer_load_dwordx4 v6, s[16:19], s35 offen  lds
LX_aa 180,52,252
LX_t 232,16,16384
LX_aa 184,40,248
LX_t 234,16,18432
LX_aa 184,56,252
LX_t 236,16,20480
LX_aa 188,44,248
LX_t 238,16,22528
LX_aa 188,60,252
LX_w 4
s_add_u32 m0, s39, 74752
LX_aa 192,32,224
buffer_load_dwordx4 v7, s[16:19], s35 offen  lds
LX_aa 192,48,228
LX_aa 196,36,224
LX_aa 196,52,228
s_waitcnt vmcnt(39) lgkmcnt(0)
s_barrier
LX_t 240,9,24576
LX_aa 200,40,224
LX_t 242,9,26624
LX_aa 200,56,228
LX_t 244,9,28672
LX_aa 204,44,224
LX_t 246,9,30720
LX_aa 204,60,228
LX_aa 208,32,232
LX_aa 208,48,236
LX_aa 212,36,232
LX_aa 212,52,236
LX_t 248,10,24576
LX_aa 216,40,232
LX_t 250,10,26624
LX_aa 216,56,236
LX_t 252,10,28672
LX_aa 220,44,232
LX_t 254,10,30720
LX_aa 220,60,236
LX_w 4
s_waitcnt vmcnt(17)
LX_aa 32,240,64
s_add_u32 s33, s38, 73728
LX_aa 32,244,80
LX_l 128,1,0
LX_aa 36,240,68
LX_aa 36,244,84
LX_t 224,11,24576
LX_aa 40,240,72
LX_t 226,11,26624
LX_aa 40,244,88
LX_t 228,11,28672
LX_aa 44,240,76
LX_t 230,11,30720
LX_aa 44,244,92
LX_w 4
LX_l 132,1,1024
LX_aa 48,248,64
LX_aa 48,252,80
LX_l 136,1,2048
LX_aa 52,248,68
LX_aa 52,252,84
LX_t 232,12,24576
LX_aa 56,248,72
LX_t 234,12,26624
LX_aa 56,252,88
LX_t 236,12,28672
LX_aa 60,248,76
LX_t 238,12,30720
LX_aa 60,252,92
LX_w 4
LX_l 140,1,3072
LX_aa 64,224,64
LX_aa 64,228,80
LX_t 240,17,57344
LX_aa 68,224,68
LX_t 242,17,59392
LX_aa 68,228,84
LX_t 244,17,61440
LX_aa 72,224,72
LX_t 246,17,63488
LX_aa 72,228,88
LX_t 248,9,57344
LX_aa 76,224,76
LX_t 250,9,59392
LX_aa 76,228,92
LX_w 6
LX_t 252,9,61440
LX_aa 80,232,64
LX_t 254,9,63488
LX_aa 80,236,80
LX_l 144,23,0
LX_aa 84,232,68
LX_aa 84,236,84
LX_l 148,23,1024
LX_aa 88,232,72
LX_aa 88,236,88
LX_t 224,10,57344
LX_aa 92,232,76
LX_t 226,10,59392
LX_aa 92,236,92
LX_w 6
LX_t 228,10,61440
LX_aa 224,240,176
LX_t 230,10,63488
LX_aa 224,244,180
LX_w 4
LX_l 152,23,2048
LX_aa 96,248,64
LX_aa 96,252,80
LX_l 156,23,3072
LX_aa 100,248,68
LX_aa 100,252,84
LX_t 232,11,57344
LX_aa 104,248,72
LX_t 234,11,59392
LX_aa 104,252,88
LX_t 236,11,61440
LX_aa 108,248,76
s_add_u32 s36, s45, 73728
LX_t 238,11,63488
LX_aa 108,252,92
LX_w 4
buffer_load_dwordx4 a[192:195], v8, s[20:23], s36 offen sc1
LX_aa 112,224,64
s_add_u32 s36, s45, 77824
LX_aa 112,228,80
buffer_load_dwordx4 a[196:199], v8, s[20:23], s36 offen sc1
LX_aa 116,224,68
LX_aa 116,228,84
LX_t 240,12,57344
LX_aa 120,224,72
LX_t 242,12,59392
LX_aa 120,228,88
LX_t 244,12,61440
LX_aa 124,224,76
LX_t 246,12,63488
LX_aa 124,228,92
LX_w 4
buffer_load_dword a223, v31, s[20:23], 0 offen sc1
LX_aa 128,232,64
LX_aa 128,236,80
buffer_load_dword a223, v22, s[28:31], 0 offen
LX_aa 132,232,68
v_add_u32 v22, 917504, v22
LX_aa 132,236,84
LX_t 248,13,24576
LX_aa 136,232,72
LX_t 250,13,26624
LX_aa 136,236,88
LX_t 252,13,28672
LX_aa 140,232,76
s_mov_b32 s35, 3932160
s_add_u32 m0, s39, 16384
LX_t 254,13,30720
LX_aa 140,236,92
LX_w 4
buffer_load_dwordx4 v2, s[16:19], s35 offen  lds
LX_aa 144,240,64
LX_aa 144,244,80
LX_t 224,20,24576
LX_aa 148,240,68
LX_t 226,20,26624
LX_aa 148,244,84
LX_t 228,20,28672
LX_aa 152,240,72
LX_t 230,20,30720
LX_aa 152,244,88
LX_t 232,14,24576
LX_aa 156,240,76
LX_t 234,14,26624
LX_aa 156,244,92
LX_w 6
LX_t 236,14,28672
LX_aa 160,64,248
s_add_u32 m0, s39, 17408
LX_t 238,14,30720
LX_aa 160,80,252
buffer_load_dwordx4 v3, s[16:19], s35 offen  lds
LX_aa 164,68,248
s_add_u32 m0, s39, 49152
LX_aa 164,84,252
buffer_load_dwordx4 v4, s[16:19], s35 offen  lds
LX_aa 168,72,248
LX_aa 168,88,252
LX_t 240,15,24576
LX_aa 172,76,248
LX_t 242,15,26624
LX_aa 172,92,252
LX_Q0_pad4:
s_cbranch_execz LX_Q0_pad7
LX_w 6
LX_t 244,15,28672
LX_aa 228,176,224
s_add_u32 m0, s39, 50176
LX_t 246,15,30720
LX_aa 228,180,228
LX_w 4
buffer_load_dwordx4 v5, s[16:19], s35 offen  lds
LX_aa 176,64,232
s_add_u32 m0, s39, 81920
LX_aa 176,80,236
buffer_load_dwordx4 v6, s[16:19], s35 offen  lds
LX_aa 180,68,232
LX_aa 180,84,236
LX_t 248,16,24576
LX_aa 184,72,232
LX_t 250,16,26624
LX_aa 184,88,236
LX_t 252,16,28672
LX_aa 188,76,232
s_add_u32 m0, s39, 82944
LX_t 254,16,30720
LX_aa 188,92,236
LX_w 4
buffer_load_dwordx4 v7, s[16:19], s35 offen  lds
LX_aa 192,64,240
LX_aa 192,80,244
LX_aa 196,68,240
LX_aa 196,84,244
s_waitcnt vmcnt(37) lgkmcnt(0)
s_barrier
LX_t 224,9,0
LX_aa 200,72,240
LX_t 226,9,2048
LX_aa 200,88,244
LX_t 228,9,4096
LX_aa 204,76,240
LX_t 230,9,6144
LX_aa 204,92,244
LX_aa 208,64,248
LX_aa 208,80,252
LX_aa 212,68,248
LX_aa 212,84,252
LX_t 232,10,0
LX_aa 216,72,248
LX_t 234,10,2048
LX_aa 216,88,252
LX_t 236,10,4096
LX_aa 220,76,248
LX_t 238,10,6144
LX_aa 220,92,252
LX_w 4
s_waitcnt vmcnt(25)
LX_aa 32,224,96
s_add_u32 s33, s38, 81920
LX_aa 32,228,112
LX_l 0,1,0
LX_aa 36,224,100
LX_aa 36,228,116
LX_t 240,11,0
LX_aa 40,224,104
LX_t 242,11,2048
LX_aa 40,228,120
LX_t 244,11,4096
LX_aa 44,224,108
LX_t 246,11,6144
LX_aa 44,228,124
LX_w 4
LX_l 4,1,1024
LX_aa 48,232,96
LX_aa 48,236,112
LX_l 8,1,2048
LX_aa 52,232,100
LX_aa 52,236,116
LX_t 248,12,0
LX_aa 56,232,104
LX_t 250,12,2048
LX_aa 56,236,120
LX_t 252,12,4096
LX_aa 60,232,108
LX_t 254,12,6144
LX_aa 60,236,124
LX_w 4
LX_l 12,1,3072
LX_aa 64,240,96
LX_aa 64,244,112
LX_t 224,17,32768
LX_aa 68,240,100
LX_t 226,17,34816
LX_aa 68,244,116
LX_t 228,17,36864
LX_aa 72,240,104
LX_t 230,17,38912
LX_aa 72,244,120
LX_t 232,9,32768
LX_aa 76,240,108
LX_t 234,9,34816
LX_aa 76,244,124
LX_w 6
LX_t 236,9,36864
LX_aa 80,248,96
LX_t 238,9,38912
LX_aa 80,252,112
LX_l 16,23,0
LX_aa 84,248,100
LX_aa 84,252,116
LX_l 20,23,1024
LX_aa 88,248,104
LX_aa 88,252,120
LX_t 240,10,32768
LX_aa 92,248,108
LX_t 242,10,34816
LX_aa 92,252,124
LX_w 6
LX_t 244,10,36864
LX_aa 224,224,184
LX_t 246,10,38912
LX_aa 224,228,188
LX_w 4
LX_aa 96,232,96
LX_l 24,23,2048
LX_aa 96,236,112
LX_aa 100,232,100
LX_l 28,23,3072
LX_aa 100,236,116
LX_t 248,11,32768
LX_aa 104,232,104
LX_t 250,11,34816
LX_aa 104,236,120
LX_t 252,11,36864
LX_aa 108,232,108
LX_t 254,11,38912
LX_aa 108,236,124
LX_w 4
s_add_u32 s36, s45, 81920
LX_aa 112,240,96
buffer_load_dwordx4 a[160:163], v8, s[20:23], s36 offen sc1
LX_aa 112,244,112
s_add_u32 s36, s45, 86016
LX_aa 116,240,100
buffer_load_dwordx4 a[164:167], v8, s[20:23], s36 offen sc1
LX_aa 116,244,116
LX_t 224,12,32768
LX_aa 120,240,104
LX_t 226,12,34816
LX_aa 120,244,120
LX_t 228,12,36864
LX_aa 124,240,108
LX_t 230,12,38912
LX_aa 124,244,124
LX_w 4
LX_aa 128,248,96
buffer_load_dword a223, v22, s[28:31], 0 offen
v_add_u32 v22, 4288544768, v22
LX_aa 128,252,112
s_mov_b32 s35, 4325376
s_add_u32 m0, s39, 24576
LX_aa 132,248,100
buffer_load_dwordx4 v2, s[16:19], s35 offen  lds
LX_aa 132,252,116
LX_t 232,13,0
LX_aa 136,248,104
LX_t 234,13,2048
LX_aa 136,252,120
LX_t 236,13,4096
LX_aa 140,248,108
LX_t 238,13,6144
LX_aa 140,252,124
LX_w 4
LX_aa 144,224,96
LX_aa 144,228,112
LX_t 240,20,0
LX_aa 148,224,100
LX_t 242,20,2048
LX_aa 148,228,116
LX_t 244,20,4096
LX_aa 152,224,104
LX_t 246,20,6144
LX_aa 152,228,120
LX_t 248,14,0
LX_aa 156,224,108
LX_t 250,14,2048
LX_aa 156,228,124
LX_w 6
LX_t 252,14,4096
LX_aa 160,96,232
s_add_u32 m0, s39, 25600
LX_t 254,14,6144
LX_aa 160,112,236
buffer_load_dwordx4 v3, s[16:19], s35 offen  lds
LX_aa 164,100,232
s_add_u32 m0, s39, 57344
LX_aa 164,116,236
buffer_load_dwordx4 v4, s[16:19], s35 offen  lds
LX_aa 168,104,232
LX_aa 168,120,236
LX_t 224,15,0
LX_aa 172,108,232
LX_t 226,15,2048
LX_aa 172,124,236
LX_w 6
LX_t 228,15,4096
LX_aa 228,184,240
s_add_u32 m0, s39, 58368
LX_t 230,15,6144
LX_aa 228,188,244
LX_w 4
buffer_load_dwordx4 v5, s[16:19], s35 offen  lds
LX_aa 176,96,248
s_add_u32 m0, s39, 90112
LX_aa 176,112,252
buffer_load_dwordx4 v6, s[16:19], s35 offen  lds
LX_aa 180,100,248
LX_aa 180,116,252
LX_t 232,16,0
LX_aa 184,104,248
LX_t 234,16,2048
LX_aa 184,120,252
LX_t 236,16,4096
LX_aa 188,108,248
s_add_u32 m0, s39, 91136
LX_t 238,16,6144
LX_aa 188,124,252
LX_w 4
buffer_load_dwordx4 v7, s[16:19], s35 offen  lds
LX_aa 192,96,224
LX_aa 192,112,228
LX_aa 196,100,224
LX_aa 196,116,228
s_waitcnt vmcnt(35) lgkmcnt(0)
s_barrier
LX_t 240,9,8192
LX_aa 200,104,224
LX_t 242,9,10240
LX_aa 200,120,228
LX_t 244,9,12288
LX_aa 204,108,224
LX_t 246,9,14336
LX_aa 204,124,228
LX_aa 208,96,232
LX_aa 208,112,236
LX_aa 212,100,232
LX_aa 212,116,236
LX_t 248,10,8192
LX_aa 216,104,232
LX_t 250,10,10240
LX_aa 216,120,236
LX_t 252,10,12288
LX_aa 220,108,232
LX_t 254,10,14336
LX_aa 220,124,236
LX_w 4
s_waitcnt vmcnt(25)
LX_aa 32,240,128
s_add_u32 s33, s38, 90112
LX_aa 32,244,144
LX_l 32,1,0
LX_aa 36,240,132
LX_l 36,1,1024
LX_aa 36,244,148
LX_t 224,11,8192
LX_aa 40,240,136
LX_t 226,11,10240
LX_aa 40,244,152
LX_t 228,11,12288
LX_aa 44,240,140
LX_t 230,11,14336
LX_aa 44,244,156
LX_w 4
LX_aa 48,248,128
LX_l 40,1,2048
LX_aa 48,252,144
LX_aa 52,248,132
LX_l 44,1,3072
LX_aa 52,252,148
LX_t 232,12,8192
LX_aa 56,248,136
LX_t 234,12,10240
LX_aa 56,252,152
LX_t 236,12,12288
LX_aa 60,248,140
LX_t 238,12,14336
LX_aa 60,252,156
LX_w 4
LX_aa 64,224,128
LX_l 48,23,0
LX_aa 64,228,144
LX_t 240,17,40960
LX_aa 68,224,132
LX_t 242,17,43008
LX_aa 68,228,148
LX_t 244,17,45056
LX_aa 72,224,136
LX_t 246,17,47104
LX_aa 72,228,152
LX_t 248,9,40960
LX_aa 76,224,140
LX_t 250,9,43008
LX_aa 76,228,156
LX_w 6
LX_t 252,9,45056
LX_aa 80,232,128
LX_t 254,9,47104
LX_aa 80,236,144
LX_aa 84,232,132
LX_l 52,23,1024
LX_aa 84,236,148
LX_aa 88,232,136
LX_l 56,23,2048
LX_aa 88,236,152
)ASM"
R"ASM(LX_t 224,10,40960
LX_aa 92,232,140
LX_t 226,10,43008
LX_aa 92,236,156
LX_w 6
LX_t 228,10,45056
LX_aa 224,240,192
LX_t 230,10,47104
LX_aa 224,244,196
LX_w 4
LX_l 60,23,3072
LX_aa 96,248,128
s_add_u32 s36, s45, 90112
LX_aa 96,252,144
buffer_load_dwordx4 a[168:171], v8, s[20:23], s36 offen sc1
LX_aa 100,248,132
LX_aa 100,252,148
LX_t 232,11,40960
LX_aa 104,248,136
LX_t 234,11,43008
LX_aa 104,252,152
LX_t 236,11,45056
LX_aa 108,248,140
s_add_u32 s36, s45, 94208
LX_t 238,11,47104
LX_aa 108,252,156
LX_w 4
buffer_load_dwordx4 a[172:175], v8, s[20:23], s36 offen sc1
LX_aa 112,224,128
s_add_u32 s33, s38, 98304
LX_aa 112,228,144
LX_l 64,1,0
LX_aa 116,224,132
LX_aa 116,228,148
LX_t 240,12,40960
LX_aa 120,224,136
LX_t 242,12,43008
LX_aa 120,228,152
LX_t 244,12,45056
LX_aa 124,224,140
LX_t 246,12,47104
LX_aa 124,228,156
LX_w 4
LX_l 68,1,1024
LX_aa 128,232,128
LX_aa 128,236,144
LX_l 72,1,2048
LX_aa 132,232,132
LX_aa 132,236,148
LX_t 248,13,8192
LX_aa 136,232,136
LX_t 250,13,10240
LX_aa 136,236,152
LX_t 252,13,12288
LX_aa 140,232,140
LX_t 254,13,14336
LX_aa 140,236,156
LX_w 4
buffer_load_dword a223, v22, s[84:87], 0 offen
v_add_u32 v22, 917504, v22
LX_aa 144,240,128
s_mov_b32 s35, 4718592
s_add_u32 m0, s39, 0
s_nop 0
buffer_load_dwordx4 v2, s[16:19], s35 offen  lds
LX_aa 144,244,144
LX_t 224,20,8192
LX_aa 148,240,132
LX_t 226,20,10240
LX_aa 148,244,148
LX_t 228,20,12288
LX_aa 152,240,136
LX_t 230,20,14336
LX_aa 152,244,152
LX_t 232,14,8192
LX_aa 156,240,140
LX_t 234,14,10240
LX_aa 156,244,156
LX_w 6
LX_t 236,14,12288
LX_aa 160,128,248
LX_t 238,14,14336
LX_aa 160,144,252
s_add_u32 m0, s39, 1024
LX_aa 164,132,248
buffer_load_dwordx4 v3, s[16:19], s35 offen  lds
LX_aa 164,148,252
s_add_u32 m0, s39, 32768
LX_aa 168,136,248
buffer_load_dwordx4 v4, s[16:19], s35 offen  lds
LX_aa 168,152,252
LX_t 240,15,8192
LX_aa 172,140,248
LX_t 242,15,10240
LX_aa 172,156,252
LX_w 6
LX_t 244,15,12288
LX_aa 228,192,224
LX_t 246,15,14336
LX_aa 228,196,228
LX_w 4
s_add_u32 m0, s39, 33792
LX_aa 176,128,232
buffer_load_dwordx4 v5, s[16:19], s35 offen  lds
LX_aa 176,144,236
s_add_u32 m0, s39, 65536
LX_aa 180,132,232
buffer_load_dwordx4 v6, s[16:19], s35 offen  lds
LX_aa 180,148,236
LX_t 248,16,8192
LX_aa 184,136,232
LX_t 250,16,10240
LX_aa 184,152,236
LX_t 252,16,12288
LX_aa 188,140,232
LX_t 254,16,14336
LX_aa 188,156,236
LX_w 4
s_add_u32 m0, s39, 66560
LX_aa 192,128,240
buffer_load_dwordx4 v7, s[16:19], s35 offen  lds
LX_aa 192,144,244
LX_aa 196,132,240
LX_aa 196,148,244
s_waitcnt vmcnt(37) lgkmcnt(0)
s_barrier
LX_t 224,9,16384
LX_aa 200,136,240
LX_t 226,9,18432
LX_aa 200,152,244
LX_t 228,9,20480
LX_aa 204,140,240
LX_t 230,9,22528
LX_aa 204,156,244
LX_aa 208,128,248
LX_aa 208,144,252
LX_aa 212,132,248
LX_aa 212,148,252
LX_t 232,10,16384
LX_aa 216,136,248
LX_t 234,10,18432
LX_aa 216,152,252
LX_t 236,10,20480
LX_aa 220,140,248
LX_t 238,10,22528
LX_aa 220,156,252
LX_w 4
s_waitcnt vmcnt(27)
LX_aa 32,224,0
LX_aa 32,228,16
LX_l 76,1,3072
LX_aa 36,224,4
LX_l 80,23,0
LX_aa 36,228,20
LX_t 240,11,16384
LX_aa 40,224,8
LX_t 242,11,18432
LX_aa 40,228,24
LX_t 244,11,20480
LX_aa 44,224,12
LX_t 246,11,22528
LX_aa 44,228,28
LX_w 4
LX_aa 48,232,0
LX_l 84,23,1024
LX_aa 48,236,16
LX_aa 52,232,4
LX_l 88,23,2048
LX_aa 52,236,20
LX_t 248,12,16384
LX_aa 56,232,8
LX_t 250,12,18432
LX_aa 56,236,24
LX_t 252,12,20480
LX_aa 60,232,12
LX_t 254,12,22528
LX_aa 60,236,28
LX_w 4
LX_aa 64,240,0
LX_l 92,23,3072
LX_aa 64,244,16
LX_t 224,17,49152
LX_aa 68,240,4
LX_t 226,17,51200
LX_aa 68,244,20
LX_t 228,17,53248
LX_aa 72,240,8
LX_t 230,17,55296
LX_aa 72,244,24
LX_t 232,9,49152
LX_aa 76,240,12
LX_t 234,9,51200
LX_aa 76,244,28
LX_w 6
LX_t 236,9,53248
LX_aa 80,248,0
LX_t 238,9,55296
LX_aa 80,252,16
s_add_u32 s36, s45, 98304
LX_aa 84,248,4
buffer_load_dwordx4 a[176:179], v8, s[20:23], s36 offen sc1
LX_aa 84,252,20
s_add_u32 s36, s45, 102400
LX_aa 88,248,8
buffer_load_dwordx4 a[180:183], v8, s[20:23], s36 offen sc1
LX_aa 88,252,24
LX_t 240,10,49152
LX_aa 92,248,12
LX_t 242,10,51200
LX_aa 92,252,28
LX_w 6
LX_t 244,10,53248
LX_aa 224,224,160
s_add_u32 s33, s38, 106496
LX_t 246,10,55296
LX_aa 224,228,164
LX_w 4
LX_l 96,1,0
LX_aa 96,232,0
LX_aa 96,236,16
LX_l 100,1,1024
LX_aa 100,232,4
LX_aa 100,236,20
LX_t 248,11,49152
LX_aa 104,232,8
LX_Q0_pad5:
s_cbranch_execz LX_Q0_pad8
LX_t 250,11,51200
LX_aa 104,236,24
LX_t 252,11,53248
LX_aa 108,232,12
LX_t 254,11,55296
LX_aa 108,236,28
LX_w 4
LX_l 104,1,2048
LX_aa 112,240,0
LX_aa 112,244,16
LX_l 108,1,3072
LX_aa 116,240,4
LX_aa 116,244,20
LX_t 224,12,49152
LX_aa 120,240,8
LX_t 226,12,51200
LX_aa 120,244,24
LX_t 228,12,53248
LX_aa 124,240,12
LX_t 230,12,55296
LX_aa 124,244,28
LX_w 4
LX_l 112,23,0
LX_aa 128,248,0
LX_aa 128,252,16
LX_l 116,23,1024
LX_aa 132,248,4
LX_aa 132,252,20
LX_t 232,13,16384
LX_aa 136,248,8
LX_t 234,13,18432
LX_aa 136,252,24
LX_t 236,13,20480
LX_aa 140,248,12
LX_t 238,13,22528
LX_aa 140,252,28
LX_w 4
buffer_load_dword a223, v22, s[84:87], 0 offen
v_add_u32 v22, 917504, v22
LX_aa 144,224,0
s_mov_b32 s35, 5111808
s_add_u32 m0, s39, 8192
s_nop 0
buffer_load_dwordx4 v2, s[16:19], s35 offen  lds
LX_aa 144,228,16
LX_t 240,20,16384
LX_aa 148,224,4
LX_t 242,20,18432
LX_aa 148,228,20
LX_t 244,20,20480
LX_aa 152,224,8
LX_t 246,20,22528
LX_aa 152,228,24
LX_t 248,14,16384
LX_aa 156,224,12
LX_t 250,14,18432
LX_aa 156,228,28
LX_w 6
LX_t 252,14,20480
LX_aa 160,0,232
LX_t 254,14,22528
LX_aa 160,16,236
s_add_u32 m0, s39, 9216
LX_aa 164,4,232
buffer_load_dwordx4 v3, s[16:19], s35 offen  lds
LX_aa 164,20,236
s_add_u32 m0, s39, 40960
LX_aa 168,8,232
buffer_load_dwordx4 v4, s[16:19], s35 offen  lds
LX_aa 168,24,236
LX_t 224,15,16384
LX_aa 172,12,232
LX_t 226,15,18432
LX_aa 172,28,236
LX_w 6
LX_t 228,15,20480
LX_aa 228,160,240
LX_t 230,15,22528
LX_aa 228,164,244
LX_w 4
s_add_u32 m0, s39, 41984
LX_aa 176,0,248
buffer_load_dwordx4 v5, s[16:19], s35 offen  lds
LX_aa 176,16,252
s_add_u32 m0, s39, 73728
LX_aa 180,4,248
buffer_load_dwordx4 v6, s[16:19], s35 offen  lds
LX_aa 180,20,252
LX_t 232,16,16384
LX_aa 184,8,248
LX_t 234,16,18432
LX_aa 184,24,252
LX_t 236,16,20480
LX_aa 188,12,248
LX_t 238,16,22528
LX_aa 188,28,252
LX_w 4
s_add_u32 m0, s39, 74752
LX_aa 192,0,224
buffer_load_dwordx4 v7, s[16:19], s35 offen  lds
LX_aa 192,16,228
LX_aa 196,4,224
LX_aa 196,20,228
s_waitcnt vmcnt(40) lgkmcnt(0)
s_barrier
LX_t 240,9,24576
LX_aa 200,8,224
LX_t 242,9,26624
LX_aa 200,24,228
LX_t 244,9,28672
LX_aa 204,12,224
LX_t 246,9,30720
LX_aa 204,28,228
LX_aa 208,0,232
LX_aa 208,16,236
LX_aa 212,4,232
LX_aa 212,20,236
LX_t 248,10,24576
LX_aa 216,8,232
LX_t 250,10,26624
LX_aa 216,24,236
LX_t 252,10,28672
LX_aa 220,12,232
LX_t 254,10,30720
LX_aa 220,28,236
LX_w 4
s_waitcnt vmcnt(30)
LX_aa 32,240,32
LX_aa 32,244,48
LX_l 120,23,2048
LX_aa 36,240,36
LX_l 124,23,3072
LX_aa 36,244,52
LX_t 224,11,24576
LX_aa 40,240,40
LX_t 226,11,26624
LX_aa 40,244,56
LX_t 228,11,28672
LX_aa 44,240,44
LX_t 230,11,30720
LX_aa 44,244,60
LX_w 4
s_add_u32 s36, s45, 106496
LX_aa 48,248,32
buffer_load_dwordx4 a[184:187], v8, s[20:23], s36 offen sc1
LX_aa 48,252,48
s_add_u32 s36, s45, 110592
LX_aa 52,248,36
buffer_load_dwordx4 a[188:191], v8, s[20:23], s36 offen sc1
LX_aa 52,252,52
LX_t 232,12,24576
LX_aa 56,248,40
LX_t 234,12,26624
LX_aa 56,252,56
LX_t 236,12,28672
LX_aa 60,248,44
LX_t 238,12,30720
LX_aa 60,252,60
LX_w 4
s_add_u32 s33, s38, 114688
LX_aa 64,224,32
LX_l 128,1,0
LX_aa 64,228,48
LX_t 240,17,57344
LX_aa 68,224,36
LX_t 242,17,59392
LX_aa 68,228,52
LX_t 244,17,61440
LX_aa 72,224,40
LX_t 246,17,63488
LX_aa 72,228,56
LX_t 248,9,57344
LX_aa 76,224,44
LX_t 250,9,59392
LX_aa 76,228,60
LX_w 6
LX_t 252,9,61440
LX_aa 80,232,32
LX_t 254,9,63488
LX_aa 80,236,48
LX_l 132,1,1024
LX_aa 84,232,36
LX_aa 84,236,52
LX_l 136,1,2048
LX_aa 88,232,40
LX_aa 88,236,56
LX_t 224,10,57344
LX_aa 92,232,44
LX_t 226,10,59392
LX_aa 92,236,60
LX_w 6
LX_t 228,10,61440
LX_aa 224,240,168
LX_t 230,10,63488
LX_aa 224,244,172
LX_w 4
LX_l 140,1,3072
LX_aa 96,248,32
LX_aa 96,252,48
LX_l 144,23,0
LX_aa 100,248,36
LX_l 148,23,1024
LX_aa 100,252,52
LX_t 232,11,57344
LX_aa 104,248,40
LX_t 234,11,59392
LX_aa 104,252,56
LX_t 236,11,61440
LX_aa 108,248,44
LX_t 238,11,63488
LX_aa 108,252,60
LX_w 4
LX_aa 112,224,32
LX_l 152,23,2048
LX_aa 112,228,48
LX_aa 116,224,36
LX_l 156,23,3072
LX_aa 116,228,52
LX_t 240,12,57344
LX_aa 120,224,40
LX_t 242,12,59392
LX_aa 120,228,56
LX_t 244,12,61440
LX_aa 124,224,44
LX_t 246,12,63488
LX_aa 124,228,60
LX_w 4
s_add_u32 s36, s45, 114688
LX_aa 128,232,32
buffer_load_dwordx4 a[192:195], v8, s[20:23], s36 offen sc1
LX_aa 128,236,48
s_add_u32 s36, s45, 118784
buffer_load_dwordx4 a[196:199], v8, s[20:23], s36 offen sc1
LX_aa 132,232,36
LX_aa 132,236,52
LX_t 248,13,24576
LX_aa 136,232,40
LX_t 250,13,26624
LX_aa 136,236,56
LX_t 252,13,28672
LX_aa 140,232,44
LX_t 254,13,30720
LX_aa 140,236,60
LX_w 4
buffer_load_dword a223, v22, s[84:87], 0 offen
LX_aa 144,240,32
v_add_u32 v22, 917504, v22
LX_aa 144,244,48
LX_t 224,20,24576
LX_aa 148,240,36
LX_t 226,20,26624
LX_aa 148,244,52
LX_t 228,20,28672
LX_aa 152,240,40
LX_t 230,20,30720
LX_aa 152,244,56
LX_t 232,14,24576
LX_aa 156,240,44
LX_t 234,14,26624
LX_aa 156,244,60
LX_w 6
LX_t 236,14,28672
LX_aa 160,32,248
s_mov_b32 s35, 5505024
s_add_u32 m0, s39, 16384
LX_t 238,14,30720
LX_aa 160,48,252
buffer_load_dwordx4 v2, s[16:19], s35 offen  lds
LX_aa 164,36,248
s_add_u32 m0, s39, 17408
LX_aa 164,52,252
buffer_load_dwordx4 v3, s[16:19], s35 offen  lds
LX_aa 168,40,248
s_add_u32 m0, s39, 49152
s_nop 0
buffer_load_dwordx4 v4, s[16:19], s35 offen  lds
LX_aa 168,56,252
LX_t 240,15,24576
LX_aa 172,44,248
LX_t 242,15,26624
LX_aa 172,60,252
LX_w 6
LX_t 244,15,28672
LX_aa 228,168,224
LX_t 246,15,30720
LX_aa 228,172,228
LX_w 4
s_add_u32 m0, s39, 50176
LX_aa 176,32,232
buffer_load_dwordx4 v5, s[16:19], s35 offen  lds
LX_aa 176,48,236
s_add_u32 m0, s39, 81920
LX_aa 180,36,232
buffer_load_dwordx4 v6, s[16:19], s35 offen  lds
LX_aa 180,52,236
LX_t 248,16,24576
LX_aa 184,40,232
LX_t 250,16,26624
LX_aa 184,56,236
LX_t 252,16,28672
LX_aa 188,44,232
LX_t 254,16,30720
LX_aa 188,60,236
LX_w 4
s_add_u32 m0, s39, 82944
LX_aa 192,32,240
buffer_load_dwordx4 v7, s[16:19], s35 offen  lds
LX_aa 192,48,244
LX_aa 196,36,240
LX_aa 196,52,244
s_waitcnt vmcnt(41) lgkmcnt(0)
s_barrier
LX_t 224,9,32768
LX_aa 200,40,240
LX_t 226,9,34816
LX_aa 200,56,244
LX_t 228,9,36864
LX_aa 204,44,240
s_mov_b32 s35, 5898240
s_add_u32 m0, s39, 24576
LX_t 230,9,38912
LX_aa 204,60,244
buffer_load_dwordx4 v2, s[16:19], s35 offen  lds
LX_aa 208,32,248
s_add_u32 m0, s39, 25600
s_nop 0
buffer_load_dwordx4 v3, s[16:19], s35 offen  lds
LX_aa 208,48,252
s_add_u32 m0, s39, 57344
LX_aa 212,36,248
buffer_load_dwordx4 v4, s[16:19], s35 offen  lds
LX_aa 212,52,252
LX_t 232,10,32768
LX_aa 216,40,248
LX_t 234,10,34816
LX_aa 216,56,252
LX_t 236,10,36864
LX_aa 220,44,248
s_add_u32 m0, s39, 58368
LX_t 238,10,38912
LX_aa 220,60,252
LX_w 4
s_waitcnt vmcnt(37)
buffer_load_dwordx4 v5, s[16:19], s35 offen  lds
LX_aa 96,224,64
LX_aa 96,228,80
LX_t 240,17,32768
LX_aa 100,224,68
LX_t 242,17,34816
LX_aa 100,228,84
LX_t 244,17,36864
LX_aa 104,224,72
LX_t 246,17,38912
LX_aa 104,228,88
LX_t 248,9,0
LX_aa 108,224,76
LX_t 250,9,2048
LX_aa 108,228,92
LX_w 6
LX_t 252,9,4096
LX_aa 112,232,64
s_add_u32 m0, s39, 90112
LX_t 254,9,6144
LX_aa 112,236,80
buffer_load_dwordx4 v6, s[16:19], s35 offen  lds
LX_aa 116,232,68
s_add_u32 m0, s39, 91136
s_nop 0
buffer_load_dwordx4 v7, s[16:19], s35 offen  lds
LX_aa 116,236,84
s_add_u32 s33, s38, 122880
LX_aa 120,232,72
LX_l 0,1,0
LX_aa 120,236,88
LX_t 224,10,0
LX_aa 124,232,76
LX_t 226,10,2048
LX_aa 124,236,92
LX_w 6
LX_t 228,10,4096
LX_aa 224,240,176
LX_t 230,10,6144
LX_aa 224,244,180
LX_w 4
LX_l 4,1,1024
LX_aa 32,248,64
LX_aa 32,252,80
LX_t 232,20,0
LX_aa 36,248,68
LX_t 234,20,2048
LX_aa 36,252,84
LX_t 236,20,4096
LX_aa 40,248,72
LX_t 238,20,6144
LX_aa 40,252,88
s_waitcnt vmcnt(29) lgkmcnt(0)
s_barrier
LX_t 240,9,40960
LX_aa 44,248,76
LX_t 242,9,43008
LX_aa 44,252,92
LX_t 244,9,45056
LX_aa 48,224,64
LX_t 246,9,47104
LX_aa 48,228,80
LX_l 8,1,2048
LX_aa 52,224,68
LX_l 12,1,3072
LX_aa 52,228,84
LX_aa 56,224,72
)ASM"
R"ASM(LX_l 16,23,0
LX_aa 56,228,88
LX_t 248,10,40960
LX_aa 60,224,76
LX_t 250,10,43008
LX_aa 60,228,92
LX_t 252,10,45056
LX_aa 228,176,232
LX_t 254,10,47104
LX_aa 228,180,236
LX_w 4
s_waitcnt vmcnt(28)
LX_l 20,23,1024
LX_aa 96,240,96
LX_aa 96,244,112
LX_t 224,17,40960
LX_aa 100,240,100
LX_t 226,17,43008
LX_aa 100,244,116
LX_t 228,17,45056
LX_aa 104,240,104
LX_t 230,17,47104
LX_aa 104,244,120
LX_t 232,9,8192
LX_aa 108,240,108
LX_t 234,9,10240
LX_aa 108,244,124
LX_w 6
LX_t 236,9,12288
LX_aa 112,248,96
LX_t 238,9,14336
LX_aa 112,252,112
LX_l 24,23,2048
LX_aa 116,248,100
LX_l 28,23,3072
LX_aa 116,252,116
s_add_u32 s36, s45, 122880
LX_aa 120,248,104
buffer_load_dwordx4 a[160:163], v8, s[20:23], s36 offen sc1
LX_aa 120,252,120
LX_t 240,10,8192
LX_aa 124,248,108
LX_t 242,10,10240
LX_aa 124,252,124
LX_w 6
LX_t 244,10,12288
LX_aa 224,224,184
s_add_u32 s36, s45, 126976
LX_t 246,10,14336
LX_aa 224,228,188
LX_w 4
buffer_load_dwordx4 a[164:167], v8, s[20:23], s36 offen sc1
LX_aa 32,232,96
LX_aa 32,236,112
LX_t 248,20,8192
v_bfe_u32 v31, %4, 4, 2
LX_aa 36,232,100
LX_t 250,20,10240
v_lshlrev_b32 v31, 8, v31
LX_aa 36,236,116
LX_t 252,20,12288
v_add_u32 v22, 128, v31
LX_aa 40,232,104
LX_t 254,20,14336
v_lshrrev_b32 v24, 1, v31
LX_aa 40,236,120
s_waitcnt vmcnt(16) lgkmcnt(0)
s_barrier
LX_t 224,9,49152
LX_aa 44,232,108
LX_t 226,9,51200
v_add_u32 v25, 1536, v24
LX_aa 44,236,124
LX_t 228,9,53248
v_add_u32 v24, 1024, v24
LX_aa 48,240,96
s_add_u32 s35, s92, 0
s_add_u32 m0, s48, 114688
LX_t 230,9,55296
LX_aa 48,244,112
buffer_load_dwordx4 v28, s[84:87], s35 offen offset:0  lds
v_and_b32 v6, 63, %4
LX_aa 52,240,100
v_lshlrev_b32 v7, 4, v6
v_lshlrev_b32 v6, 3, v6
LX_aa 52,244,116
s_add_u32 m0, s48, 116608
s_lshl_b32 s50, s61, 12
s_add_u32 s51, s50, 98304
LX_aa 56,240,104
buffer_load_dwordx4 v28, s[84:87], s35 offen offset:128  lds
v_add_u32 v3, s51, v7
LX_aa 56,244,120
s_cmp_eq_u32 s61, 3
s_cselect_b64 s[52:53], 0, -1
LX_t 232,10,49152
LX_aa 60,240,108
s_add_u32 s50, s50, 98304
LX_t 234,10,51200
v_add_u32 v5, s50, v6
LX_aa 60,244,124
LX_t 236,10,53248
LX_aa 228,184,248
s_lshr_b32 s50, s61, 1
s_lshl_b32 s50, s50, 10
LX_t 238,10,55296
LX_aa 228,188,252
LX_w 4
s_waitcnt vmcnt(25)
LX_aa 96,224,128
s_and_b32 s51, s61, 1
s_lshl_b32 s51, s51, 3
LX_aa 96,228,144
s_add_u32 s50, s50, s51
s_add_u32 s50, s50, 98304
LX_t 240,17,49152
LX_aa 100,224,132
LX_t 242,17,51200
v_add_u32 v4, s50, v7
LX_aa 100,228,148
s_lshl_b32 s50, s61, 9
s_add_u32 s50, s50, 98304
LX_t 244,17,53248
LX_aa 104,224,136
LX_t 246,17,55296
v_add_u32 v6, s50, v6
LX_aa 104,228,152
s_mov_b32 s54, 0x3e8293ee
LX_t 248,9,16384
LX_aa 108,224,140
LX_t 250,9,18432
LX_aa 108,228,156
LX_w 6
LX_t 252,9,20480
LX_aa 112,232,128
LX_Q0_pad6:
s_cbranch_execz LX_Q0_pad9
s_add_u32 m0, s48, 115712
LX_t 254,9,22528
LX_aa 112,236,144
buffer_load_dwordx4 v29, s[84:87], s35 offen offset:0  lds
LX_aa 116,232,132
LX_aa 116,236,148
s_add_u32 m0, s48, 117632
LX_aa 120,232,136
buffer_load_dwordx4 v29, s[84:87], s35 offen offset:128  lds
LX_aa 120,236,152
LX_t 224,10,16384
LX_aa 124,232,140
LX_t 226,10,18432
LX_aa 124,236,156
LX_w 6
LX_t 228,10,20480
LX_aa 224,240,192
LX_t 230,10,22528
LX_aa 224,244,196
LX_w 4
LX_aa 32,248,128
LX_aa 32,252,144
LX_t 232,20,16384
LX_aa 36,248,132
LX_t 234,20,18432
LX_aa 36,252,148
LX_t 236,20,20480
LX_aa 40,248,136
LX_t 238,20,22528
LX_aa 40,252,152
s_waitcnt vmcnt(14) lgkmcnt(0)
s_barrier
LX_t 240,9,57344
LX_aa 44,248,140
LX_t 242,9,59392
LX_aa 44,252,156
LX_t 244,9,61440
LX_aa 48,224,128
LX_t 246,9,63488
LX_aa 48,228,144
s_add_u32 s35, s60, 0
s_add_u32 m0, s48, 131072
LX_aa 52,224,132
buffer_load_dwordx4 v28, s[28:31], s35 offen offset:0  lds
LX_aa 52,228,148
LX_aa 56,224,136
LX_aa 56,228,152
LX_t 248,10,57344
LX_aa 60,224,140
LX_t 250,10,59392
LX_aa 60,228,156
LX_t 252,10,61440
LX_aa 228,192,232
s_add_u32 m0, s48, 132992
LX_t 254,10,63488
LX_aa 228,196,236
LX_w 4
s_waitcnt vmcnt(5)
buffer_load_dwordx4 v28, s[28:31], s35 offen offset:128  lds
LX_aa 96,240,0
LX_aa 96,244,16
LX_t 224,17,57344
LX_aa 100,240,4
LX_t 226,17,59392
LX_aa 100,244,20
LX_t 228,17,61440
LX_aa 104,240,8
LX_t 230,17,63488
LX_aa 104,244,24
LX_t 232,9,24576
LX_aa 108,240,12
LX_t 234,9,26624
LX_aa 108,244,28
LX_w 6
LX_t 236,9,28672
LX_aa 112,248,0
LX_t 238,9,30720
LX_aa 112,252,16
s_add_u32 m0, s48, 132096
LX_aa 116,248,4
buffer_load_dwordx4 v29, s[28:31], s35 offen offset:0  lds
LX_aa 116,252,20
LX_aa 120,248,8
LX_aa 120,252,24
LX_t 240,10,24576
LX_aa 124,248,12
LX_t 242,10,26624
LX_aa 124,252,28
LX_w 6
LX_t 244,10,28672
LX_aa 224,224,160
s_add_u32 m0, s48, 134016
LX_t 246,10,30720
LX_aa 224,228,164
LX_w 4
buffer_load_dwordx4 v29, s[28:31], s35 offen offset:128  lds
LX_aa 32,232,0
LX_aa 32,236,16
LX_t 248,20,24576
LX_aa 36,232,4
LX_t 250,20,26624
LX_aa 36,236,20
LX_t 252,20,28672
LX_aa 40,232,8
LX_t 254,20,30720
LX_aa 40,236,24
LX_t 224,11,32768
LX_aa 44,232,12
LX_t 226,11,34816
LX_aa 44,236,28
LX_w 6
LX_t 228,11,36864
LX_aa 48,240,0
LX_t 230,11,38912
LX_aa 48,244,16
LX_aa 52,240,4
s_add_u32 s35, s60, 524288
s_add_u32 m0, s48, 147456
LX_aa 52,244,20
buffer_load_dwordx4 v28, s[28:31], s35 offen offset:0  lds
LX_aa 56,240,8
LX_aa 56,244,24
LX_t 232,12,32768
LX_aa 60,240,12
LX_t 234,12,34816
LX_aa 60,244,28
LX_w 6
LX_t 236,12,36864
LX_aa 228,160,248
LX_t 238,12,38912
LX_aa 228,164,252
LX_w 4
s_add_u32 m0, s48, 149376
LX_aa 128,224,64
buffer_load_dwordx4 v28, s[28:31], s35 offen offset:128  lds
LX_aa 128,228,80
LX_aa 132,224,68
LX_aa 132,228,84
LX_t 240,11,40960
LX_aa 136,224,72
LX_t 242,11,43008
LX_aa 136,228,88
LX_t 244,11,45056
LX_c 104,104,105
LX_c 105,106,107
LX_aa 140,224,76
s_add_u32 m0, s48, 148480
LX_t 246,11,47104
LX_c 106,120,121
LX_aa 140,228,92
LX_w 4
buffer_load_dwordx4 v29, s[28:31], s35 offen offset:0  lds
LX_c 107,122,123
LX_aa 144,232,64
s_mov_b64 exec, s[52:53]
ds_write_b128 v3, v[104:107] offset:4096
s_mov_b64 exec, -1
LX_aa 144,236,80
s_add_u32 m0, s48, 150400
LX_c 108,108,109
LX_c 109,110,111
LX_aa 148,232,68
buffer_load_dwordx4 v29, s[28:31], s35 offen offset:128  lds
LX_c 110,124,125
LX_c 111,126,127
LX_aa 148,236,84
LX_t 248,12,40960
LX_c 224,224,225
LX_aa 152,232,72
LX_t 250,12,43008
LX_c 225,226,227
LX_aa 152,236,88
LX_t 252,12,45056
LX_c 96,96,97
LX_aa 156,232,76
LX_t 254,12,47104
LX_c 97,98,99
LX_aa 156,236,92
LX_w 5
s_mov_b64 exec, s[52:53]
ds_write_b128 v3, v[108:111] offset:6144
s_mov_b64 exec, -1
LX_aa 128,240,96
ds_write_b64 v4, v[224:225] offset:0
LX_c 98,112,113
LX_aa 128,244,112
ds_write_b64 v4, v[224:225] offset:2048
LX_c 99,114,115
LX_aa 132,240,100
LX_w 0
s_barrier
LX_c 100,100,101
LX_aa 132,244,116
LX_t 224,11,49152
LX_c 101,102,103
LX_aa 136,240,104
LX_t 226,11,51200
LX_c 102,116,117
LX_c 103,118,119
LX_aa 136,244,120
LX_t 228,11,53248
LX_c 40,40,41
LX_aa 140,240,108
LX_t 230,11,55296
LX_c 41,42,43
LX_aa 140,244,124
ds_read_b128 v[112:115], v3 offset:0
LX_c 42,56,57
LX_aa 144,248,96
ds_read_b128 v[116:119], v3 offset:2048
LX_c 43,58,59
LX_aa 144,252,112
LX_c 44,44,45
LX_c 45,46,47
LX_c 46,60,61
LX_aa 148,248,100
LX_c 47,62,63
LX_c 32,32,33
LX_aa 148,252,116
LX_t 232,12,49152
LX_c 33,34,35
v_mfma_f32_16x16x32_bf16 v[56:59], v[100:103], v[44:47], v[236:239]
LX_c 34,48,49
LX_c 35,50,51
LX_aa 152,248,104
LX_t 234,12,51200
LX_c 36,36,37
v_mfma_f32_16x16x32_bf16 v[48:51], v[96:99], v[40:43], v[236:239]
LX_c 37,38,39
LX_c 38,52,53
LX_aa 152,252,120
LX_t 236,12,53248
LX_c 39,54,55
v_mfma_f32_16x16x32_bf16 v[60:63], v[108:111], v[44:47], v[232:235]
LX_aa 156,248,108
LX_t 238,12,55296
v_mfma_f32_16x16x32_bf16 v[52:55], v[104:107], v[40:43], v[232:235]
LX_aa 156,252,124
LX_w 6
v_max3_f32 v122, v56, v57, v58
LX_aa 128,224,128
v_max3_f32 v120, v48, v49, v50
LX_aa 128,228,144
v_max3_f32 v123, v59, v60, v61
v_max3_f32 v122, v122, v62, v63
LX_aa 132,224,132
v_max3_f32 v121, v51, v52, v53
v_max3_f32 v120, v120, v54, v55
LX_aa 132,228,148
LX_t 240,11,57344
v_max_f32 v120, v120, v121
v_max_f32 v122, v122, v123
LX_aa 136,224,136
LX_t 242,11,59392
v_mov_b32 v121, v120
LX_aa 136,228,152
LX_t 244,11,61440
v_mov_b32 v123, v122
LX_aa 140,224,140
LX_t 246,11,63488
v_permlane16_swap_b32 v120, v121
LX_aa 140,228,156
LX_w 4
v_permlane16_swap_b32 v122, v123
v_max_f32 v120, v120, v121
LX_aa 144,232,128
v_max_f32 v122, v122, v123
v_mov_b32 v121, v120
v_mov_b32 v123, v122
LX_aa 144,236,144
v_permlane32_swap_b32 v120, v121
v_permlane32_swap_b32 v122, v123
LX_aa 148,232,132
v_max_f32 v120, v120, v121
v_max_f32 v122, v122, v123
LX_aa 148,236,148
LX_t 248,12,57344
v_mul_f32 v120, s54, v120
LX_aa 152,232,136
LX_t 250,12,59392
v_mul_f32 v122, s54, v122
LX_aa 152,236,152
LX_t 252,12,61440
v_fma_f32 v48, v48, s54, -v120
v_fma_f32 v49, v49, s54, -v120
LX_aa 156,232,140
LX_t 254,12,63488
v_fma_f32 v50, v50, s54, -v120
LX_aa 156,236,156
LX_w 4
v_exp_f32 v48, v48
LX_aa 128,240,0
v_exp_f32 v49, v49
LX_aa 128,244,16
v_exp_f32 v50, v50
LX_aa 132,240,4
v_fma_f32 v51, v51, s54, -v120
v_exp_f32 v51, v51
LX_aa 132,244,20
LX_t 224,11,0
v_fma_f32 v52, v52, s54, -v120
LX_aa 136,240,8
LX_t 226,11,2048
v_fma_f32 v53, v53, s54, -v120
LX_aa 136,244,24
LX_t 228,11,4096
v_fma_f32 v54, v54, s54, -v120
LX_aa 140,240,12
LX_t 230,11,6144
v_fma_f32 v55, v55, s54, -v120
LX_aa 140,244,28
LX_w 4
v_exp_f32 v52, v52
LX_c 40,48,49
LX_aa 144,248,0
v_exp_f32 v53, v53
LX_aa 144,252,16
v_exp_f32 v54, v54
LX_aa 148,248,4
v_exp_f32 v55, v55
LX_aa 148,252,20
LX_t 232,12,0
v_fma_f32 v56, v56, s54, -v122
LX_aa 152,248,8
LX_t 234,12,2048
v_exp_f32 v56, v56
LX_aa 152,252,24
LX_t 236,12,4096
v_fma_f32 v57, v57, s54, -v122
LX_aa 156,248,12
LX_t 238,12,6144
LX_c 41,50,51
LX_aa 156,252,28
LX_w 4
v_exp_f32 v57, v57
LX_aa 64,224,64
v_fma_f32 v58, v58, s54, -v122
v_fma_f32 v59, v59, s54, -v122
LX_aa 64,228,80
v_exp_f32 v58, v58
LX_c 42,52,53
LX_aa 68,224,68
v_exp_f32 v59, v59
LX_aa 68,228,84
LX_t 240,11,8192
v_fma_f32 v60, v60, s54, -v122
LX_aa 72,224,72
LX_t 242,11,10240
v_fma_f32 v61, v61, s54, -v122
LX_aa 72,228,88
LX_t 244,11,12288
LX_c 43,54,55
LX_aa 76,224,76
LX_t 246,11,14336
v_exp_f32 v60, v60
v_mfma_f32_16x16x32_bf16 v[48:51], v[248:251], v[40:43], 0
v_exp_f32 v61, v61
LX_aa 76,228,92
LX_w 4
v_fma_f32 v62, v62, s54, -v122
v_fma_f32 v63, v63, s54, -v122
v_mfma_f32_16x16x32_bf16 v[52:55], v[96:99], v[32:35], v[232:235]
v_exp_f32 v62, v62
LX_aa 80,232,64
v_exp_f32 v63, v63
LX_aa 80,236,80
LX_c 44,56,57
LX_c 45,58,59
LX_aa 84,232,68
v_rcp_f32 v126, v48
LX_aa 84,236,84
LX_t 248,12,8192
LX_c 136,136,137
LX_c 137,138,139
LX_aa 88,232,72
LX_t 250,12,10240
LX_c 138,152,153
LX_aa 88,236,88
LX_t 252,12,12288
LX_c 139,154,155
LX_aa 92,232,76
LX_t 254,12,14336
LX_c 140,140,141
LX_aa 92,236,92
LX_w 4
LX_c 141,142,143
LX_c 142,156,157
LX_aa 64,240,96
s_mov_b64 exec, s[52:53]
ds_write_b128 v3, v[136:139] offset:5120
s_mov_b64 exec, -1
LX_c 143,158,159
LX_aa 64,244,112
s_mov_b64 exec, s[52:53]
ds_write_b128 v3, v[140:143] offset:7168
s_mov_b64 exec, -1
LX_aa 68,240,100
LX_c 128,128,129
LX_c 129,130,131
LX_aa 68,244,116
LX_t 224,11,16384
LX_c 130,144,145
v_mfma_f32_16x16x32_bf16 v[48:51], v[112:115], v[32:35], v[240:243]
LX_c 131,146,147
LX_c 132,132,133
LX_aa 72,240,104
LX_t 226,11,18432
LX_c 133,134,135
LX_aa 72,244,120
LX_t 228,11,20480
LX_c 134,148,149
LX_c 135,150,151
LX_aa 76,240,108
LX_t 230,11,22528
LX_c 46,60,61
LX_aa 76,244,124
LX_w 6
LX_c 47,62,63
v_max3_f32 v120, v48, v49, v50
LX_aa 80,248,96
v_max3_f32 v121, v51, v52, v53
v_max3_f32 v120, v120, v54, v55
v_mfma_f32_16x16x32_bf16 v[56:59], v[248:251], v[44:47], 0
v_max_f32 v120, v120, v121
v_mov_b32 v121, v120
LX_aa 80,252,112
s_nop 0
v_permlane16_swap_b32 v120, v121
v_max_f32 v120, v120, v121
v_mfma_f32_16x16x32_bf16 v[60:63], v[100:103], v[36:39], v[232:235]
v_mov_b32 v121, v120
LX_aa 84,248,100
s_nop 0
v_permlane32_swap_b32 v120, v121
v_max_f32 v120, v120, v121
v_mul_f32 v120, s54, v120
LX_aa 84,252,116
LX_t 232,12,16384
v_fma_f32 v48, v48, s54, -v120
LX_aa 88,248,104
LX_t 234,12,18432
v_fma_f32 v49, v49, s54, -v120
LX_aa 88,252,120
LX_t 236,12,20480
v_fma_f32 v50, v50, s54, -v120
LX_aa 92,248,108
LX_t 238,12,22528
v_fma_f32 v51, v51, s54, -v120
LX_aa 92,252,124
LX_w 4
)ASM"
R"ASM(v_rcp_f32 v127, v56
v_fma_f32 v52, v52, s54, -v120
LX_aa 64,224,128
v_exp_f32 v48, v48
v_mfma_f32_16x16x32_bf16 v[56:59], v[116:119], v[36:39], v[244:247]
v_exp_f32 v49, v49
LX_aa 64,228,144
v_exp_f32 v50, v50
LX_aa 68,224,132
s_barrier
v_fma_f32 v53, v53, s54, -v120
LX_aa 68,228,148
LX_t 240,11,24576
v_fma_f32 v54, v54, s54, -v120
LX_aa 72,224,136
LX_t 242,11,26624
v_exp_f32 v51, v51
LX_aa 72,228,152
LX_t 244,11,28672
v_max3_f32 v122, v56, v57, v58
LX_aa 76,224,140
LX_t 246,11,30720
v_max3_f32 v123, v59, v60, v61
LX_aa 76,228,156
LX_w 4
ds_read_b128 v[144:147], v3 offset:1024
v_max3_f32 v122, v122, v62, v63
LX_aa 80,232,128
ds_read_b128 v[148:151], v3 offset:3072
v_max_f32 v122, v122, v123
LX_aa 80,236,144
v_exp_f32 v52, v52
v_mov_b32 v123, v122
LX_aa 84,232,132
v_exp_f32 v53, v53
LX_aa 84,236,148
LX_t 248,12,24576
v_permlane16_swap_b32 v122, v123
LX_aa 88,232,136
LX_t 250,12,26624
v_max_f32 v122, v122, v123
LX_aa 88,236,152
LX_t 252,12,28672
v_mov_b32 v123, v122
LX_aa 92,232,140
LX_t 254,12,30720
v_exp_f32 v54, v54
LX_aa 92,236,156
LX_w 6
v_permlane32_swap_b32 v122, v123
v_max_f32 v122, v122, v123
LX_aa 64,240,0
v_mul_f32 v122, s54, v122
v_fma_f32 v55, v55, s54, -v120
LX_aa 64,244,16
v_exp_f32 v55, v55
LX_aa 68,240,4
LX_c 32,48,49
v_fma_f32 v56, v56, s54, -v122
LX_aa 68,244,20
LX_w 0
s_barrier
LX_t 224,13,0
LX_aa 72,240,8
LX_t 226,13,2048
v_fma_f32 v57, v57, s54, -v122
LX_aa 72,244,24
LX_t 228,13,4096
LX_c 33,50,51
LX_aa 76,240,12
s_add_u32 s35, s60, 917504
s_add_u32 m0, s48, 0
LX_t 230,13,6144
LX_aa 76,244,28
buffer_load_dwordx4 v28, s[28:31], s35 offen offset:0  lds
v_fma_f32 v58, v58, s54, -v122
LX_aa 80,248,0
v_exp_f32 v56, v56
v_fma_f32 v59, v59, s54, -v122
LX_aa 80,252,16
v_exp_f32 v57, v57
LX_aa 84,248,4
v_exp_f32 v58, v58
LX_aa 84,252,20
LX_t 232,14,0
LX_c 34,52,53
LX_aa 88,248,8
LX_t 234,14,2048
v_fma_f32 v60, v60, s54, -v122
LX_aa 88,252,24
LX_t 236,14,4096
v_exp_f32 v59, v59
LX_aa 92,248,12
s_add_u32 m0, s48, 1920
LX_t 238,14,6144
v_fma_f32 v61, v61, s54, -v122
LX_aa 92,252,28
LX_w 4
buffer_load_dwordx4 v28, s[28:31], s35 offen offset:128  lds
LX_c 35,54,55
LX_aa 160,64,224
v_exp_f32 v60, v60
v_mfma_f32_16x16x32_bf16 v[48:51], v[248:251], v[32:35], 0
v_exp_f32 v61, v61
LX_aa 160,80,228
v_fma_f32 v62, v62, s54, -v122
v_fma_f32 v63, v63, s54, -v122
LX_aa 164,68,224
v_exp_f32 v62, v62
LX_c 36,56,57
LX_aa 164,84,228
LX_t 240,13,8192
LX_c 37,58,59
LX_aa 168,72,224
LX_Q0_pad7:
s_cbranch_execz LX_Q0_pad10
LX_t 242,13,10240
LX_c 38,60,61
LX_aa 168,88,228
LX_t 244,13,12288
LX_c 72,72,73
LX_aa 172,76,224
LX_t 246,13,14336
LX_c 73,74,75
LX_aa 172,92,228
LX_w 4
s_add_u32 m0, s48, 1024
v_exp_f32 v63, v63
LX_aa 176,64,232
buffer_load_dwordx4 v29, s[28:31], s35 offen offset:0  lds
LX_c 39,62,63
LX_aa 176,80,236
v_rcp_f32 v124, v48
v_mfma_f32_16x16x32_bf16 v[56:59], v[248:251], v[36:39], 0
LX_c 74,88,89
LX_c 75,90,91
LX_aa 180,68,232
LX_c 76,76,77
LX_c 77,78,79
LX_aa 180,84,236
LX_t 248,14,8192
LX_c 78,92,93
LX_aa 184,72,232
LX_t 250,14,10240
LX_c 79,94,95
LX_c 64,64,65
LX_aa 184,88,236
LX_t 252,14,12288
LX_c 65,66,67
v_mfma_f32_16x16x32_bf16 v[88:91], v[132:135], v[76:79], v[236:239]
v_rcp_f32 v125, v56
LX_aa 188,76,232
LX_t 254,14,14336
LX_c 66,80,81
v_mfma_f32_16x16x32_bf16 v[92:95], v[140:143], v[76:79], v[232:235]
LX_c 67,82,83
LX_c 68,68,69
LX_aa 188,92,236
LX_w 4
s_add_u32 m0, s48, 2944
LX_c 69,70,71
v_mfma_f32_16x16x32_bf16 v[80:83], v[128:131], v[72:75], v[236:239]
LX_c 70,84,85
LX_c 71,86,87
LX_aa 160,96,240
buffer_load_dwordx4 v29, s[28:31], s35 offen offset:128  lds
v_mfma_f32_16x16x32_bf16 v[84:87], v[136:139], v[72:75], v[232:235]
LX_aa 160,112,244
v_max3_f32 v154, v88, v89, v90
LX_aa 164,100,240
v_max3_f32 v155, v91, v92, v93
v_max3_f32 v154, v154, v94, v95
LX_aa 164,116,244
LX_t 224,13,16384
v_max3_f32 v152, v80, v81, v82
LX_aa 168,104,240
LX_t 226,13,18432
v_max3_f32 v153, v83, v84, v85
LX_aa 168,120,244
LX_t 228,13,20480
v_max3_f32 v152, v152, v86, v87
LX_aa 172,108,240
LX_t 230,13,22528
v_max_f32 v152, v152, v153
v_max_f32 v154, v154, v155
LX_aa 172,124,244
LX_w 4
v_mov_b32 v153, v152
v_mov_b32 v155, v154
LX_aa 176,96,248
s_add_u32 s35, s60, 1441792
s_add_u32 m0, s48, 16384
v_permlane16_swap_b32 v152, v153
LX_aa 176,112,252
buffer_load_dwordx4 v28, s[28:31], s35 offen offset:0  lds
v_permlane16_swap_b32 v154, v155
LX_aa 180,100,248
v_max_f32 v152, v152, v153
v_max_f32 v154, v154, v155
LX_aa 180,116,252
LX_t 232,14,16384
v_mov_b32 v153, v152
v_mov_b32 v155, v154
LX_aa 184,104,248
LX_t 234,14,18432
v_permlane32_swap_b32 v152, v153
LX_aa 184,120,252
LX_t 236,14,20480
v_permlane32_swap_b32 v154, v155
LX_aa 188,108,248
LX_t 238,14,22528
v_max_f32 v152, v152, v153
LX_aa 188,124,252
LX_w 4
v_max_f32 v154, v154, v155
v_mul_f32 v152, s54, v152
LX_aa 160,128,224
s_add_u32 m0, s48, 18304
v_mul_f32 v154, s54, v154
v_fma_f32 v80, v80, s54, -v152
v_fma_f32 v81, v81, s54, -v152
LX_aa 160,144,228
buffer_load_dwordx4 v28, s[28:31], s35 offen offset:128  lds
v_fma_f32 v82, v82, s54, -v152
LX_aa 164,132,224
v_exp_f32 v80, v80
LX_aa 164,148,228
LX_t 240,13,24576
v_fma_f32 v83, v83, s54, -v152
LX_aa 168,136,224
LX_t 242,13,26624
v_fma_f32 v84, v84, s54, -v152
LX_aa 168,152,228
LX_t 244,13,28672
v_exp_f32 v81, v81
LX_aa 172,140,224
LX_t 246,13,30720
v_fma_f32 v85, v85, s54, -v152
LX_aa 172,156,228
LX_w 4
v_exp_f32 v82, v82
LX_aa 176,128,232
v_exp_f32 v83, v83
LX_aa 176,144,236
s_add_u32 m0, s48, 17408
v_exp_f32 v84, v84
LX_aa 180,132,232
buffer_load_dwordx4 v29, s[28:31], s35 offen offset:0  lds
v_exp_f32 v85, v85
LX_aa 180,148,236
LX_t 248,14,24576
v_fma_f32 v86, v86, s54, -v152
LX_aa 184,136,232
LX_t 250,14,26624
v_fma_f32 v87, v87, s54, -v152
LX_aa 184,152,236
LX_t 252,14,28672
LX_c 72,80,81
LX_aa 188,140,232
LX_t 254,14,30720
v_fma_f32 v88, v88, s54, -v154
LX_aa 188,156,236
LX_w 4
v_exp_f32 v86, v86
v_fma_f32 v89, v89, s54, -v154
LX_aa 160,0,240
v_exp_f32 v87, v87
LX_aa 160,16,244
s_add_u32 m0, s48, 19328
v_exp_f32 v88, v88
LX_aa 164,4,240
buffer_load_dwordx4 v29, s[28:31], s35 offen offset:128  lds
LX_c 73,82,83
LX_aa 164,20,244
LX_t 224,15,0
v_fma_f32 v90, v90, s54, -v154
LX_aa 168,8,240
LX_t 226,15,2048
v_exp_f32 v89, v89
LX_aa 168,24,244
LX_t 228,15,4096
v_fma_f32 v91, v91, s54, -v154
LX_aa 172,12,240
LX_t 230,15,6144
LX_c 74,84,85
LX_aa 172,28,244
LX_w 4
v_exp_f32 v90, v90
LX_aa 176,0,248
v_exp_f32 v91, v91
LX_aa 176,16,252
v_fma_f32 v92, v92, s54, -v154
v_exp_f32 v92, v92
LX_aa 180,4,248
v_fma_f32 v93, v93, s54, -v154
LX_c 75,86,87
LX_aa 180,20,252
LX_t 232,16,0
v_fma_f32 v94, v94, s54, -v154
v_mfma_f32_16x16x32_bf16 v[80:83], v[248:251], v[72:75], 0
v_exp_f32 v93, v93
LX_aa 184,8,248
LX_t 234,16,2048
v_fma_f32 v95, v95, s54, -v154
v_mfma_f32_16x16x32_bf16 v[84:87], v[128:131], v[64:67], v[232:235]
v_exp_f32 v94, v94
LX_aa 184,24,252
LX_t 236,16,4096
LX_c 76,88,89
LX_aa 188,12,248
s_add_u32 s35, s60, 131072
s_add_u32 m0, s48, 32768
LX_t 238,16,6144
LX_c 77,90,91
LX_aa 188,28,252
LX_w 4
buffer_load_dwordx4 v28, s[28:31], s35 offen offset:0  lds
LX_c 78,92,93
LX_aa 192,64,224
v_exp_f32 v95, v95
LX_aa 192,80,228
v_rcp_f32 v158, v80
LX_aa 196,68,224
LX_c 79,94,95
v_mfma_f32_16x16x32_bf16 v[80:83], v[144:147], v[64:67], v[240:243]
LX_aa 196,84,228
LX_t 240,15,8192
v_mfma_f32_16x16x32_bf16 v[88:91], v[248:251], v[76:79], 0
LX_aa 200,72,224
LX_t 242,15,10240
v_mfma_f32_16x16x32_bf16 v[92:95], v[132:135], v[68:71], v[232:235]
LX_aa 200,88,228
LX_t 244,15,12288
LX_aa 204,76,224
LX_t 246,15,14336
v_max3_f32 v152, v80, v81, v82
LX_aa 204,92,228
LX_w 4
s_add_u32 m0, s48, 34688
v_max3_f32 v153, v83, v84, v85
LX_aa 208,64,232
buffer_load_dwordx4 v28, s[28:31], s35 offen offset:128  lds
v_rcp_f32 v159, v88
LX_aa 208,80,236
v_max3_f32 v152, v152, v86, v87
v_max_f32 v152, v152, v153
v_mfma_f32_16x16x32_bf16 v[88:91], v[148:151], v[68:71], v[244:247]
v_mov_b32 v153, v152
LX_aa 212,68,232
s_nop 0
v_permlane16_swap_b32 v152, v153
v_max_f32 v152, v152, v153
LX_aa 212,84,236
LX_t 248,16,8192
v_mov_b32 v153, v152
LX_aa 216,72,232
LX_t 250,16,10240
v_permlane32_swap_b32 v152, v153
LX_aa 216,88,236
LX_t 252,16,12288
v_max3_f32 v154, v88, v89, v90
v_max3_f32 v155, v91, v92, v93
LX_aa 220,76,232
LX_t 254,16,14336
v_max3_f32 v154, v154, v94, v95
LX_aa 220,92,236
LX_w 4
s_add_u32 m0, s48, 33792
v_max_f32 v154, v154, v155
LX_aa 192,96,240
buffer_load_dwordx4 v29, s[28:31], s35 offen offset:0  lds
v_mov_b32 v155, v154
LX_aa 192,112,244
s_nop 0
v_permlane16_swap_b32 v154, v155
v_max_f32 v154, v154, v155
LX_aa 196,100,240
v_mov_b32 v155, v154
v_max_f32 v152, v152, v153
v_mul_f32 v152, s54, v152
LX_aa 196,116,244
LX_t 224,15,16384
v_permlane32_swap_b32 v154, v155
LX_aa 200,104,240
LX_t 226,15,18432
v_max_f32 v154, v154, v155
LX_aa 200,120,244
LX_t 228,15,20480
v_mul_f32 v154, s54, v154
LX_aa 204,108,240
LX_t 230,15,22528
v_fma_f32 v80, v80, s54, -v152
LX_aa 204,124,244
LX_w 4
v_exp_f32 v80, v80
v_fma_f32 v81, v81, s54, -v152
LX_aa 208,96,248
s_add_u32 m0, s48, 35712
v_exp_f32 v81, v81
LX_aa 208,112,252
buffer_load_dwordx4 v29, s[28:31], s35 offen offset:128  lds
v_fma_f32 v82, v82, s54, -v152
LX_aa 212,100,248
v_exp_f32 v82, v82
LX_aa 212,116,252
LX_t 232,16,16384
v_fma_f32 v83, v83, s54, -v152
LX_aa 216,104,248
LX_t 234,16,18432
v_exp_f32 v83, v83
LX_aa 216,120,252
LX_t 236,16,20480
v_fma_f32 v84, v84, s54, -v152
LX_aa 220,108,248
LX_t 238,16,22528
v_fma_f32 v85, v85, s54, -v152
LX_aa 220,124,252
LX_w 4
v_exp_f32 v84, v84
LX_aa 192,128,224
s_add_u32 s35, s60, 655360
s_add_u32 m0, s48, 49152
v_fma_f32 v86, v86, s54, -v152
LX_aa 192,144,228
buffer_load_dwordx4 v28, s[28:31], s35 offen offset:0  lds
v_exp_f32 v85, v85
LX_aa 196,132,224
v_exp_f32 v86, v86
LX_aa 196,148,228
LX_t 240,15,24576
v_fma_f32 v87, v87, s54, -v152
LX_aa 200,136,224
LX_t 242,15,26624
LX_c 64,80,81
LX_aa 200,152,228
LX_t 244,15,28672
v_fma_f32 v88, v88, s54, -v154
LX_aa 204,140,224
LX_t 246,15,30720
v_exp_f32 v87, v87
LX_aa 204,156,228
LX_w 4
v_exp_f32 v88, v88
LX_aa 208,128,232
v_fma_f32 v89, v89, s54, -v154
LX_c 65,82,83
LX_aa 208,144,236
s_add_u32 m0, s48, 51072
v_exp_f32 v89, v89
LX_aa 212,132,232
buffer_load_dwordx4 v28, s[28:31], s35 offen offset:128  lds
v_fma_f32 v90, v90, s54, -v154
LX_aa 212,148,236
LX_t 248,16,24576
v_exp_f32 v90, v90
LX_aa 216,136,232
LX_t 250,16,26624
v_fma_f32 v91, v91, s54, -v154
LX_aa 216,152,236
LX_t 252,16,28672
LX_c 66,84,85
LX_aa 220,140,232
LX_t 254,16,30720
v_fma_f32 v92, v92, s54, -v154
LX_aa 220,156,236
LX_w 4
v_exp_f32 v91, v91
LX_aa 192,0,240
v_exp_f32 v92, v92
v_fma_f32 v93, v93, s54, -v154
LX_aa 192,16,244
s_add_u32 m0, s48, 50176
v_exp_f32 v93, v93
LX_aa 196,4,240
buffer_load_dwordx4 v29, s[28:31], s35 offen offset:0  lds
LX_c 67,86,87
LX_aa 196,20,244
v_fma_f32 v94, v94, s54, -v154
v_fma_f32 v95, v95, s54, -v154
v_mfma_f32_16x16x32_bf16 v[80:83], v[248:251], v[64:67], 0
v_exp_f32 v94, v94
LX_aa 200,8,240
v_exp_f32 v95, v95
LX_aa 200,24,244
LX_c 68,88,89
LX_c 69,90,91
LX_c 70,92,93
LX_aa 204,12,240
s_add_u32 m0, s48, 52096
LX_c 71,94,95
LX_c 228,228,229
LX_aa 204,28,244
LX_w 0
buffer_load_dwordx4 v29, s[28:31], s35 offen offset:128  lds
LX_c 229,230,231
v_mfma_f32_16x16x32_bf16 v[88:91], v[248:251], v[68:71], 0
v_rcp_f32 v156, v80
LX_aa 208,0,248
ds_write_b64 v6, v[228:229] offset:0
LX_c 252,164,165
LX_aa 208,16,252
ds_write_b64 v6, v[228:229] offset:2048
LX_c 253,166,167
LX_aa 212,4,248
LX_c 164,168,169
LX_c 165,170,171
LX_c 172,172,173
LX_aa 212,20,252
s_mov_b64 exec, s[52:53]
ds_write_b64 v5, v[164:165] offset:4096
s_mov_b64 exec, -1
LX_aa 216,8,248
v_rcp_f32 v157, v88
LX_aa 216,24,252
LX_c 173,174,175
LX_c 163,162,163
LX_aa 220,12,248
s_mov_b64 exec, s[52:53]
ds_write_b64 v5, v[172:173] offset:6144
s_mov_b64 exec, -1
LX_aa 220,28,252
LX_c 162,160,161
v_mov_b32 v170, v252
v_mov_b32 v171, v253
LX_c 252,180,181
LX_c 253,182,183
LX_c 180,184,185
LX_c 181,186,187
LX_c 188,188,189
LX_c 189,190,191
s_mov_b64 exec, s[52:53]
ds_write_b64 v5, v[180:181] offset:4608
s_mov_b64 exec, -1
s_mov_b64 exec, s[52:53]
ds_write_b64 v5, v[188:189] offset:6656
s_mov_b64 exec, -1
LX_c 179,178,179
LX_c 178,176,177
)ASM"
R"ASM(v_mov_b32 v186, v252
v_mov_b32 v187, v253
LX_c 252,196,197
LX_c 253,198,199
LX_c 196,200,201
LX_c 197,202,203
LX_c 204,204,205
LX_c 205,206,207
s_mov_b64 exec, s[52:53]
ds_write_b64 v5, v[196:197] offset:5120
s_mov_b64 exec, -1
s_mov_b64 exec, s[52:53]
ds_write_b64 v5, v[204:205] offset:7168
s_mov_b64 exec, -1
LX_c 195,194,195
LX_c 194,192,193
v_mov_b32 v202, v252
v_mov_b32 v203, v253
LX_c 252,212,213
LX_c 253,214,215
LX_c 212,216,217
LX_c 213,218,219
LX_c 220,220,221
LX_c 221,222,223
s_mov_b64 exec, s[52:53]
ds_write_b64 v5, v[212:213] offset:5632
s_mov_b64 exec, -1
s_mov_b64 exec, s[52:53]
ds_write_b64 v5, v[220:221] offset:7680
s_mov_b64 exec, -1
LX_c 211,210,211
LX_c 210,208,209
v_mov_b32 v218, v252
v_mov_b32 v219, v253
buffer_load_dwordx2 v[18:19], v24, s[20:23], s80 offen sc1
LX_w 0
s_barrier
ds_read_b64 v[160:161], v5 offset:0
ds_read_b64 v[176:177], v5 offset:512
ds_read_b64 v[192:193], v5 offset:1024
ds_read_b64 v[208:209], v5 offset:1536
ds_read_b64 v[168:169], v5 offset:2048
ds_read_b64 v[184:185], v5 offset:2560
ds_read_b64 v[200:201], v5 offset:3072
ds_read_b64 v[216:217], v5 offset:3584
v_mfma_f32_16x16x32_bf16 v[48:51], v[162:165], v[40:43], 0
v_mfma_f32_16x16x32_bf16 v[52:55], v[178:181], v[40:43], 0
v_mfma_f32_16x16x32_bf16 v[56:59], v[194:197], v[72:75], 0
v_mfma_f32_16x16x32_bf16 v[60:63], v[210:213], v[72:75], 0
v_mfma_f32_16x16x32_bf16 v[80:83], v[170:173], v[44:47], 0
v_mfma_f32_16x16x32_bf16 v[84:87], v[186:189], v[44:47], 0
v_mfma_f32_16x16x32_bf16 v[88:91], v[202:205], v[76:79], 0
s_nop 1
v_pk_mul_f32 v[48:49], v[48:49], v[126:127] op_sel:[0,0] op_sel_hi:[1,0]
v_pk_mul_f32 v[50:51], v[50:51], v[126:127] op_sel:[0,0] op_sel_hi:[1,0]
v_mfma_f32_16x16x32_bf16 v[92:95], v[218:221], v[76:79], 0
v_pk_mul_f32 v[52:53], v[52:53], v[126:127] op_sel:[0,0] op_sel_hi:[1,0]
v_pk_mul_f32 v[54:55], v[54:55], v[126:127] op_sel:[0,0] op_sel_hi:[1,0]
LX_w 0
v_mfma_f32_16x16x32_bf16 v[96:99], v[160:163], v[32:35], 0
LX_c 40,48,49
LX_c 41,50,51
v_mfma_f32_16x16x32_bf16 v[100:103], v[176:179], v[32:35], 0
LX_c 42,52,53
LX_c 43,54,55
v_mfma_f32_16x16x32_bf16 v[104:107], v[192:195], v[64:67], 0
v_pk_mul_f32 v[56:57], v[56:57], v[158:159] op_sel:[0,0] op_sel_hi:[1,0]
v_pk_mul_f32 v[58:59], v[58:59], v[158:159] op_sel:[0,0] op_sel_hi:[1,0]
v_mfma_f32_16x16x32_bf16 v[108:111], v[208:211], v[64:67], 0
v_pk_mul_f32 v[60:61], v[60:61], v[158:159] op_sel:[0,0] op_sel_hi:[1,0]
v_pk_mul_f32 v[62:63], v[62:63], v[158:159] op_sel:[0,0] op_sel_hi:[1,0]
v_mfma_f32_16x16x32_bf16 v[128:131], v[168:171], v[36:39], 0
LX_c 72,56,57
LX_c 73,58,59
v_mfma_f32_16x16x32_bf16 v[132:135], v[184:187], v[36:39], 0
LX_c 74,60,61
LX_c 75,62,63
v_mfma_f32_16x16x32_bf16 v[136:139], v[200:203], v[68:71], 0
v_pk_mul_f32 v[80:81], v[80:81], v[126:127] op_sel:[0,1] op_sel_hi:[1,1]
v_pk_mul_f32 v[82:83], v[82:83], v[126:127] op_sel:[0,1] op_sel_hi:[1,1]
v_mfma_f32_16x16x32_bf16 v[140:143], v[216:219], v[68:71], 0
v_pk_mul_f32 v[84:85], v[84:85], v[126:127] op_sel:[0,1] op_sel_hi:[1,1]
v_pk_mul_f32 v[86:87], v[86:87], v[126:127] op_sel:[0,1] op_sel_hi:[1,1]
s_waitcnt vmcnt(0)
v_cmp_ne_u64 vcc, s[56:57], v[18:19]
LX_c 44,80,81
LX_c 45,82,83
LX_c 46,84,85
LX_c 47,86,87
s_cbranch_vccnz LX_c10
LX_c10r:
v_pk_mul_f32 v[88:89], v[88:89], v[158:159] op_sel:[0,1] op_sel_hi:[1,1]
s_add_u32 s33, s34, 32768
LX_l 0,1,0
v_pk_mul_f32 v[90:91], v[90:91], v[158:159] op_sel:[0,1] op_sel_hi:[1,1]
v_pk_mul_f32 v[92:93], v[92:93], v[158:159] op_sel:[0,1] op_sel_hi:[1,1]
v_pk_mul_f32 v[94:95], v[94:95], v[158:159] op_sel:[0,1] op_sel_hi:[1,1]
LX_c 76,88,89
LX_l 4,1,1024
LX_c 77,90,91
LX_c 78,92,93
LX_c 79,94,95
v_pk_mul_f32 v[96:97], v[96:97], v[124:125] op_sel:[0,0] op_sel_hi:[1,0]
LX_l 8,1,2048
v_pk_mul_f32 v[98:99], v[98:99], v[124:125] op_sel:[0,0] op_sel_hi:[1,0]
v_pk_mul_f32 v[100:101], v[100:101], v[124:125] op_sel:[0,0] op_sel_hi:[1,0]
v_pk_mul_f32 v[102:103], v[102:103], v[124:125] op_sel:[0,0] op_sel_hi:[1,0]
LX_c 32,96,97
LX_l 12,1,3072
LX_c 33,98,99
LX_c 34,100,101
LX_c 35,102,103
v_pk_mul_f32 v[104:105], v[104:105], v[156:157] op_sel:[0,0] op_sel_hi:[1,0]
LX_l 16,23,0
v_pk_mul_f32 v[106:107], v[106:107], v[156:157] op_sel:[0,0] op_sel_hi:[1,0]
v_pk_mul_f32 v[108:109], v[108:109], v[156:157] op_sel:[0,0] op_sel_hi:[1,0]
v_pk_mul_f32 v[110:111], v[110:111], v[156:157] op_sel:[0,0] op_sel_hi:[1,0]
LX_c 64,104,105
LX_l 20,23,1024
LX_c 65,106,107
LX_c 66,108,109
LX_c 67,110,111
v_pk_mul_f32 v[128:129], v[128:129], v[124:125] op_sel:[0,1] op_sel_hi:[1,1]
LX_l 24,23,2048
v_pk_mul_f32 v[130:131], v[130:131], v[124:125] op_sel:[0,1] op_sel_hi:[1,1]
v_pk_mul_f32 v[132:133], v[132:133], v[124:125] op_sel:[0,1] op_sel_hi:[1,1]
v_pk_mul_f32 v[134:135], v[134:135], v[124:125] op_sel:[0,1] op_sel_hi:[1,1]
LX_c 36,128,129
LX_l 28,23,3072
LX_c 37,130,131
LX_c 38,132,133
LX_c 39,134,135
v_pk_mul_f32 v[136:137], v[136:137], v[156:157] op_sel:[0,1] op_sel_hi:[1,1]
v_pk_mul_f32 v[138:139], v[138:139], v[156:157] op_sel:[0,1] op_sel_hi:[1,1]
v_pk_mul_f32 v[140:141], v[140:141], v[156:157] op_sel:[0,1] op_sel_hi:[1,1]
v_pk_mul_f32 v[142:143], v[142:143], v[156:157] op_sel:[0,1] op_sel_hi:[1,1]
LX_c 68,136,137
LX_c 69,138,139
LX_c 70,140,141
LX_c 71,142,143
s_lshr_b32 s50, %2, 1
s_lshl_b32 s50, s50, 11
s_and_b32 s51, %2, 1
s_lshl_b32 s51, s51, 6
s_or_b32 s50, s50, s51
s_or_b32 s50, s50, 114688
v_xor_b32 v2, s50, v9
v_xor_b32 v3, 32, v2
v_xor_b32 v4, 64, v2
v_xor_b32 v5, 96, v2
v_xor_b32 v6, 2048, v2
v_xor_b32 v7, 2080, v2
v_xor_b32 v8, 2112, v2
v_xor_b32 v21, 2144, v2
s_barrier
LX_t 224,4,0
LX_t 226,4,4096
LX_t 228,4,8192
LX_t 230,4,12288
LX_t 232,5,0
LX_Q0_pad8:
s_cbranch_execz LX_Q0_pad11
LX_t 234,5,4096
LX_t 236,5,8192
LX_t 238,5,12288
LX_w 4
v_mfma_f32_16x16x32_bf16 v[48:51], a[224:227], v[32:35], 0
s_add_u32 s33, s34, 65536
LX_av 48,228,64
LX_l 32,1,0
v_mfma_f32_16x16x32_bf16 v[52:55], a[224:227], v[36:39], 0
LX_l 36,1,1024
LX_av 52,228,68
LX_t 240,6,0
v_mfma_f32_16x16x32_bf16 v[56:59], a[224:227], v[40:43], 0
LX_t 242,6,4096
LX_av 56,228,72
LX_t 244,6,8192
v_mfma_f32_16x16x32_bf16 v[60:63], a[224:227], v[44:47], 0
LX_t 246,6,12288
LX_av 60,228,76
LX_w 4
v_mfma_f32_16x16x32_bf16 v[80:83], a[232:235], v[32:35], 0
LX_l 40,1,2048
LX_av 80,236,64
v_mfma_f32_16x16x32_bf16 v[84:87], a[232:235], v[36:39], 0
LX_l 44,1,3072
LX_av 84,236,68
LX_t 248,7,0
v_mfma_f32_16x16x32_bf16 v[88:91], a[232:235], v[40:43], 0
LX_t 250,7,4096
LX_av 88,236,72
LX_t 252,7,8192
v_mfma_f32_16x16x32_bf16 v[92:95], a[232:235], v[44:47], 0
LX_t 254,7,12288
LX_av 92,236,76
LX_w 4
v_mfma_f32_16x16x32_bf16 v[112:115], a[240:243], v[32:35], 0
LX_l 48,23,0
LX_av 112,244,64
v_mfma_f32_16x16x32_bf16 v[116:119], a[240:243], v[36:39], 0
LX_l 52,23,1024
LX_av 116,244,68
LX_t 224,8,0
v_mfma_f32_16x16x32_bf16 v[120:123], a[240:243], v[40:43], 0
LX_t 226,8,4096
LX_av 120,244,72
LX_t 228,8,8192
v_mfma_f32_16x16x32_bf16 v[124:127], a[240:243], v[44:47], 0
LX_t 230,8,12288
LX_av 124,244,76
LX_w 4
v_mfma_f32_16x16x32_bf16 v[144:147], a[248:251], v[32:35], 0
LX_l 56,23,2048
LX_av 144,252,64
v_mfma_f32_16x16x32_bf16 v[148:151], a[248:251], v[36:39], 0
LX_l 60,23,3072
LX_av 148,252,68
LX_t 232,21,0
v_mfma_f32_16x16x32_bf16 v[152:155], a[248:251], v[40:43], 0
LX_t 234,21,4096
LX_av 152,252,72
LX_t 236,21,8192
v_mfma_f32_16x16x32_bf16 v[156:159], a[248:251], v[44:47], 0
LX_t 238,21,12288
LX_av 156,252,76
LX_w 4
s_add_u32 s33, s34, 90112
v_mfma_f32_16x16x32_bf16 v[176:179], a[224:227], v[32:35], 0
LX_l 64,1,0
LX_av 176,228,64
LX_l 68,1,1024
v_mfma_f32_16x16x32_bf16 v[180:183], a[224:227], v[36:39], 0
LX_av 180,228,68
LX_t 240,2,0
v_mfma_f32_16x16x32_bf16 v[184:187], a[224:227], v[40:43], 0
LX_t 242,2,4096
LX_av 184,228,72
LX_t 244,2,8192
v_mfma_f32_16x16x32_bf16 v[188:191], a[224:227], v[44:47], 0
LX_t 246,2,12288
LX_av 188,228,76
LX_w 4
LX_l 72,1,2048
v_mfma_f32_16x16x32_bf16 v[208:211], a[232:235], v[32:35], 0
LX_av 208,236,64
LX_l 76,1,3072
v_mfma_f32_16x16x32_bf16 v[212:215], a[232:235], v[36:39], 0
LX_av 212,236,68
LX_t 248,3,0
v_mfma_f32_16x16x32_bf16 v[216:219], a[232:235], v[40:43], 0
LX_t 250,3,4096
LX_av 216,236,72
LX_t 252,3,8192
v_mfma_f32_16x16x32_bf16 v[220:223], a[232:235], v[44:47], 0
LX_t 254,3,12288
LX_av 220,236,76
LX_w 4
LX_l 80,23,0
v_mfma_f32_16x16x32_bf16 v[240:243], a[240:243], v[32:35], 0
LX_av 240,244,64
LX_l 84,23,1024
v_mfma_f32_16x16x32_bf16 v[244:247], a[240:243], v[36:39], 0
LX_av 244,244,68
LX_t 224,4,16384
v_mfma_f32_16x16x32_bf16 v[248:251], a[240:243], v[40:43], 0
LX_t 226,4,20480
LX_av 248,244,72
LX_t 228,4,24576
v_mfma_f32_16x16x32_bf16 v[252:255], a[240:243], v[44:47], 0
LX_t 230,4,28672
LX_av 252,244,76
LX_w 4
LX_l 88,23,2048
v_mfma_f32_16x16x32_bf16 v[224:227], a[248:251], v[32:35], 0
LX_av 224,252,64
LX_l 92,23,3072
v_mfma_f32_16x16x32_bf16 v[228:231], a[248:251], v[36:39], 0
LX_av 228,252,68
LX_t 232,5,16384
v_mfma_f32_16x16x32_bf16 v[232:235], a[248:251], v[40:43], 0
LX_t 234,5,20480
LX_av 232,252,72
LX_t 236,5,24576
v_mfma_f32_16x16x32_bf16 v[236:239], a[248:251], v[44:47], 0
s_add_u32 s49, s88, 0
LX_t 238,5,28672
LX_av 236,252,76
LX_w 4
s_waitcnt vmcnt(16)
buffer_store_dwordx4 v[32:35], v1, s[20:23], s49 offen offset:0 sc1
LX_aa 48,224,0
LX_aa 48,228,16
buffer_store_dwordx4 v[36:39], v1, s[20:23], s49 offen offset:1024 sc1
LX_aa 52,224,4
LX_aa 52,228,20
LX_t 240,6,16384
LX_aa 56,224,8
LX_t 242,6,20480
LX_aa 56,228,24
LX_t 244,6,24576
LX_aa 60,224,12
LX_t 246,6,28672
LX_aa 60,228,28
LX_w 4
buffer_store_dwordx4 v[40:43], v1, s[20:23], s49 offen offset:2048 sc1
LX_aa 80,232,0
LX_aa 80,236,16
buffer_store_dwordx4 v[44:47], v1, s[20:23], s49 offen offset:3072 sc1
LX_aa 84,232,4
LX_aa 84,236,20
LX_t 248,7,16384
LX_aa 88,232,8
LX_t 250,7,20480
LX_aa 88,236,24
LX_t 252,7,24576
LX_aa 92,232,12
LX_t 254,7,28672
LX_aa 92,236,28
LX_w 4
s_add_u32 s49, s88, 4096
LX_aa 112,240,0
buffer_store_dwordx4 v[64:67], v1, s[20:23], s49 offen offset:0 sc1
LX_aa 112,244,16
LX_aa 116,240,4
buffer_store_dwordx4 v[68:71], v1, s[20:23], s49 offen offset:1024 sc1
LX_aa 116,244,20
LX_t 224,8,16384
LX_aa 120,240,8
LX_t 226,8,20480
LX_aa 120,244,24
LX_t 228,8,24576
LX_aa 124,240,12
LX_t 230,8,28672
LX_aa 124,244,28
LX_w 4
LX_aa 144,248,0
buffer_store_dwordx4 v[72:75], v1, s[20:23], s49 offen offset:2048 sc1
LX_aa 144,252,16
LX_aa 148,248,4
buffer_store_dwordx4 v[76:79], v1, s[20:23], s49 offen offset:3072 sc1
LX_aa 148,252,20
LX_t 232,21,16384
LX_aa 152,248,8
LX_t 234,21,20480
LX_aa 152,252,24
LX_t 236,21,24576
LX_aa 156,248,12
s_add_u32 s33, s34, 122880
LX_t 238,21,28672
LX_aa 156,252,28
LX_w 4
LX_l 96,1,0
LX_aa 176,224,0
LX_aa 176,228,16
LX_l 100,1,1024
LX_aa 180,224,4
buffer_load_dwordx2 v[18:19], v25, s[20:23], s80 offen sc1
LX_aa 180,228,20
LX_t 240,2,16384
LX_aa 184,224,8
LX_t 242,2,20480
LX_aa 184,228,24
LX_t 244,2,24576
LX_aa 188,224,12
LX_t 246,2,28672
LX_aa 188,228,28
LX_w 4
LX_l 104,1,2048
LX_aa 208,232,0
LX_aa 208,236,16
LX_l 108,1,3072
LX_aa 212,232,4
LX_aa 212,236,20
LX_t 248,3,16384
LX_aa 216,232,8
LX_t 250,3,20480
LX_aa 216,236,24
LX_t 252,3,24576
LX_aa 220,232,12
LX_t 254,3,28672
LX_aa 220,236,28
LX_w 4
LX_l 112,23,0
LX_aa 240,240,0
LX_aa 240,244,16
LX_aa 244,240,4
LX_l 116,23,1024
LX_aa 244,244,20
LX_t 224,4,32768
LX_aa 248,240,8
LX_t 226,4,36864
LX_aa 248,244,24
LX_t 228,4,40960
LX_aa 252,240,12
LX_t 230,4,45056
LX_aa 252,244,28
LX_w 4
LX_l 120,23,2048
LX_aa 224,248,0
LX_aa 224,252,16
LX_aa 228,248,4
LX_aa 228,252,20
LX_t 232,5,32768
LX_aa 232,248,8
LX_t 234,5,36864
LX_aa 232,252,24
LX_t 236,5,40960
LX_aa 236,248,12
LX_t 238,5,45056
LX_aa 236,252,28
LX_w 4
s_waitcnt vmcnt(24)
LX_l 124,23,3072
LX_aa 48,224,32
s_add_u32 s35, s60, 1048576
s_add_u32 m0, s48, 65536
LX_aa 48,228,48
buffer_load_dwordx4 v28, s[28:31], s35 offen offset:0  lds
LX_aa 52,224,36
LX_aa 52,228,52
LX_t 240,6,32768
LX_aa 56,224,40
LX_t 242,6,36864
LX_aa 56,228,56
LX_t 244,6,40960
LX_aa 60,224,44
s_add_u32 m0, s48, 67456
LX_t 246,6,45056
LX_aa 60,228,60
LX_w 4
buffer_load_dwordx4 v28, s[28:31], s35 offen offset:128  lds
LX_aa 80,232,32
LX_aa 80,236,48
LX_aa 84,232,36
LX_aa 84,236,52
LX_t 248,7,32768
)ASM"
R"ASM(LX_aa 88,232,40
LX_t 250,7,36864
LX_aa 88,236,56
LX_t 252,7,40960
LX_aa 92,232,44
s_add_u32 m0, s48, 66560
LX_t 254,7,45056
LX_aa 92,236,60
LX_w 4
buffer_load_dwordx4 v29, s[28:31], s35 offen offset:0  lds
LX_aa 112,240,32
LX_aa 112,244,48
LX_aa 116,240,36
LX_aa 116,244,52
LX_t 224,8,32768
LX_aa 120,240,40
LX_t 226,8,36864
LX_aa 120,244,56
LX_t 228,8,40960
LX_aa 124,240,44
s_add_u32 m0, s48, 68480
LX_t 230,8,45056
LX_aa 124,244,60
LX_w 4
buffer_load_dwordx4 v29, s[28:31], s35 offen offset:128  lds
LX_aa 144,248,32
LX_aa 144,252,48
LX_aa 148,248,36
LX_aa 148,252,52
LX_t 232,21,32768
LX_aa 152,248,40
LX_t 234,21,36864
LX_aa 152,252,56
LX_t 236,21,40960
LX_aa 156,248,44
s_add_u32 s35, s60, 1572864
s_add_u32 m0, s48, 81920
LX_t 238,21,45056
LX_aa 156,252,60
LX_w 4
buffer_load_dwordx4 v28, s[28:31], s35 offen offset:0  lds
LX_aa 176,224,32
LX_aa 176,228,48
LX_aa 180,224,36
LX_aa 180,228,52
LX_t 240,2,32768
LX_aa 184,224,40
LX_t 242,2,36864
LX_aa 184,228,56
LX_t 244,2,40960
LX_aa 188,224,44
LX_t 246,2,45056
LX_aa 188,228,60
LX_w 4
s_add_u32 m0, s48, 83840
LX_aa 208,232,32
buffer_load_dwordx4 v28, s[28:31], s35 offen offset:128  lds
LX_aa 208,236,48
LX_aa 212,232,36
LX_aa 212,236,52
LX_t 248,3,32768
LX_aa 216,232,40
LX_t 250,3,36864
LX_aa 216,236,56
LX_t 252,3,40960
LX_aa 220,232,44
LX_t 254,3,45056
LX_aa 220,236,60
LX_w 4
LX_aa 240,240,32
s_add_u32 m0, s48, 82944
LX_aa 240,244,48
buffer_load_dwordx4 v29, s[28:31], s35 offen offset:0  lds
LX_aa 244,240,36
v_add_u32 v4, 4294852608, v4
LX_aa 244,244,52
s_waitcnt vmcnt(53) lgkmcnt(0)
s_barrier
LX_t 224,4,0
LX_aa 248,240,40
LX_t 226,4,4096
LX_aa 248,244,56
LX_t 228,4,8192
LX_aa 252,240,44
LX_t 230,4,12288
LX_aa 252,244,60
LX_aa 224,248,32
LX_aa 224,252,48
s_add_u32 m0, s48, 84864
LX_aa 228,248,36
buffer_load_dwordx4 v29, s[28:31], s35 offen offset:128  lds
v_add_u32 v5, 4294852608, v5
LX_aa 228,252,52
LX_t 232,5,0
LX_aa 232,248,40
LX_t 234,5,4096
LX_aa 232,252,56
LX_t 236,5,8192
LX_aa 236,248,44
LX_t 238,5,12288
LX_aa 236,252,60
LX_w 4
s_waitcnt vmcnt(25)
LX_aa 48,224,64
LX_aa 48,228,80
LX_aa 52,224,68
v_add_u32 v6, 4294852608, v6
LX_aa 52,228,84
LX_t 240,6,0
LX_aa 56,224,72
LX_t 242,6,4096
LX_aa 56,228,88
LX_t 244,6,8192
LX_aa 60,224,76
s_add_u32 s35, s60, 262144
s_add_u32 m0, s48, 98304
LX_t 246,6,12288
LX_aa 60,228,92
LX_w 4
s_waitcnt vmcnt(14)
buffer_load_dwordx4 v28, s[28:31], s35 offen offset:0  lds
v_cmp_ne_u64 vcc, s[56:57], v[18:19]
LX_aa 80,232,64
LX_aa 80,236,80
s_cbranch_vccnz LX_c4
LX_c4r:
LX_aa 84,232,68
v_add_u32 v7, 4294852608, v7
LX_aa 84,236,84
LX_t 248,7,0
LX_aa 88,232,72
LX_t 250,7,4096
LX_aa 88,236,88
LX_t 252,7,8192
LX_aa 92,232,76
s_add_u32 s33, s34, 40960
LX_t 254,7,12288
LX_aa 92,236,92
LX_w 4
LX_l 128,1,0
LX_aa 112,240,64
LX_aa 112,244,80
LX_l 132,1,1024
LX_aa 116,240,68
v_add_u32 v8, 4294852608, v8
LX_aa 116,244,84
LX_t 224,8,0
LX_aa 120,240,72
LX_t 226,8,4096
LX_aa 120,244,88
LX_t 228,8,8192
LX_aa 124,240,76
LX_t 230,8,12288
LX_aa 124,244,92
LX_w 4
LX_l 136,1,2048
LX_aa 144,248,64
LX_aa 144,252,80
LX_l 140,1,3072
LX_aa 148,248,68
v_add_u32 v21, 4294852608, v21
LX_aa 148,252,84
LX_t 232,21,0
LX_aa 152,248,72
LX_t 234,21,4096
LX_aa 152,252,88
LX_t 236,21,8192
LX_aa 156,248,76
LX_t 238,21,12288
LX_aa 156,252,92
LX_w 4
LX_l 144,23,0
LX_aa 176,224,64
LX_aa 176,228,80
LX_l 148,23,1024
LX_aa 180,224,68
v_add_u32 v2, 4294852608, v2
LX_aa 180,228,84
LX_t 240,2,0
LX_aa 184,224,72
LX_t 242,2,4096
LX_aa 184,228,88
LX_t 244,2,8192
LX_aa 188,224,76
LX_t 246,2,12288
LX_aa 188,228,92
LX_w 4
LX_l 152,23,2048
LX_aa 208,232,64
LX_aa 208,236,80
LX_l 156,23,3072
LX_aa 212,232,68
v_add_u32 v3, 4294852608, v3
LX_aa 212,236,84
LX_t 248,3,0
LX_aa 216,232,72
LX_t 250,3,4096
LX_aa 216,236,88
LX_t 252,3,8192
LX_aa 220,232,76
s_add_u32 s33, s34, 73728
LX_t 254,3,12288
LX_aa 220,236,92
LX_w 4
LX_l 160,1,0
LX_aa 240,240,64
s_waitcnt vmcnt(27)
buffer_store_dwordx2 v[26:27], v30, s[20:23], s83 offen offset:0 sc1
LX_aa 240,244,80
LX_l 164,1,1024
LX_aa 244,240,68
LX_aa 244,244,84
LX_t 224,4,16384
LX_aa 248,240,72
LX_t 226,4,20480
LX_aa 248,244,88
LX_t 228,4,24576
LX_aa 252,240,76
LX_t 230,4,28672
LX_aa 252,244,92
LX_w 4
LX_l 168,1,2048
LX_aa 224,248,64
LX_aa 224,252,80
LX_l 172,1,3072
LX_aa 228,248,68
LX_aa 228,252,84
LX_t 232,5,16384
LX_aa 232,248,72
LX_t 234,5,20480
LX_aa 232,252,88
LX_t 236,5,24576
LX_aa 236,248,76
LX_t 238,5,28672
LX_aa 236,252,92
LX_w 4
s_waitcnt vmcnt(22)
LX_l 176,23,0
LX_aa 48,224,96
LX_aa 48,228,112
LX_l 180,23,1024
LX_aa 52,224,100
LX_aa 52,228,116
LX_t 240,6,16384
LX_aa 56,224,104
LX_t 242,6,20480
LX_aa 56,228,120
LX_t 244,6,24576
LX_aa 60,224,108
LX_t 246,6,28672
LX_aa 60,228,124
LX_w 4
LX_l 184,23,2048
LX_aa 80,232,96
LX_Q0_pad9:
s_cbranch_execz LX_Q0_pad12
LX_aa 80,236,112
LX_aa 84,232,100
LX_l 188,23,3072
LX_aa 84,236,116
LX_t 248,7,16384
LX_aa 88,232,104
LX_t 250,7,20480
LX_aa 88,236,120
LX_t 252,7,24576
LX_aa 92,232,108
s_add_u32 s33, s34, 98304
LX_t 254,7,28672
LX_aa 92,236,124
LX_w 4
LX_l 192,1,0
LX_aa 112,240,96
LX_aa 112,244,112
LX_aa 116,240,100
LX_aa 116,244,116
LX_t 224,8,16384
LX_aa 120,240,104
LX_t 226,8,20480
LX_aa 120,244,120
LX_t 228,8,24576
LX_aa 124,240,108
LX_t 230,8,28672
LX_aa 124,244,124
LX_w 4
LX_l 196,1,1024
LX_aa 144,248,96
LX_aa 144,252,112
LX_l 200,1,2048
LX_aa 148,248,100
LX_aa 148,252,116
LX_t 232,21,16384
LX_aa 152,248,104
LX_t 234,21,20480
LX_aa 152,252,120
LX_t 236,21,24576
LX_aa 156,248,108
LX_t 238,21,28672
LX_aa 156,252,124
LX_w 4
LX_l 204,1,3072
LX_aa 176,224,96
LX_aa 176,228,112
LX_l 208,23,0
LX_aa 180,224,100
LX_aa 180,228,116
LX_t 240,2,16384
LX_aa 184,224,104
LX_t 242,2,20480
LX_aa 184,228,120
LX_t 244,2,24576
LX_aa 188,224,108
LX_t 246,2,28672
LX_aa 188,228,124
LX_w 4
LX_l 212,23,1024
LX_aa 208,232,96
LX_aa 208,236,112
LX_l 216,23,2048
LX_aa 212,232,100
LX_aa 212,236,116
LX_t 248,3,16384
LX_aa 216,232,104
LX_t 250,3,20480
LX_aa 216,236,120
LX_t 252,3,24576
LX_aa 220,232,108
LX_t 254,3,28672
LX_aa 220,236,124
LX_w 4
LX_l 220,23,3072
LX_aa 240,240,96
s_add_u32 s33, s34, 131072
LX_aa 240,244,112
buffer_load_dwordx4 v[96:99], v1, s[20:23], s33 offen offset:0 sc1
LX_aa 244,240,100
LX_aa 244,244,116
LX_t 224,4,32768
LX_aa 248,240,104
LX_t 226,4,36864
LX_aa 248,244,120
LX_t 228,4,40960
LX_aa 252,240,108
LX_t 230,4,45056
LX_aa 252,244,124
LX_w 4
buffer_load_dwordx4 v[100:103], v1, s[20:23], s33 offen offset:1024 sc1
LX_aa 224,248,96
LX_aa 224,252,112
buffer_load_dwordx4 v[104:107], v1, s[20:23], s33 offen offset:2048 sc1
LX_aa 228,248,100
LX_aa 228,252,116
LX_t 232,5,32768
LX_aa 232,248,104
LX_t 234,5,36864
LX_aa 232,252,120
LX_t 236,5,40960
LX_aa 236,248,108
LX_t 238,5,45056
LX_aa 236,252,124
LX_w 4
s_waitcnt vmcnt(20)
buffer_load_dwordx4 v[108:111], v1, s[20:23], s33 offen offset:3072 sc1
LX_aa 48,224,128
LX_aa 48,228,144
buffer_load_dwordx2 v[18:19], v22, s[20:23], s80 offen sc1
LX_aa 52,224,132
buffer_load_dwordx4 v[128:131], v23, s[20:23], s33 offen offset:0 sc1
LX_aa 52,228,148
LX_t 240,6,32768
LX_aa 56,224,136
LX_t 242,6,36864
LX_aa 56,228,152
LX_t 244,6,40960
LX_aa 60,224,140
LX_t 246,6,45056
LX_aa 60,228,156
LX_w 4
buffer_load_dwordx4 v[132:135], v23, s[20:23], s33 offen offset:1024 sc1
LX_aa 80,232,128
LX_aa 80,236,144
LX_aa 84,232,132
LX_aa 84,236,148
LX_t 248,7,32768
LX_aa 88,232,136
LX_t 250,7,36864
LX_aa 88,236,152
LX_t 252,7,40960
LX_aa 92,232,140
LX_t 254,7,45056
LX_aa 92,236,156
LX_w 4
buffer_load_dwordx4 v[136:139], v23, s[20:23], s33 offen offset:2048 sc1
LX_aa 112,240,128
LX_aa 112,244,144
buffer_load_dwordx4 v[140:143], v23, s[20:23], s33 offen offset:3072 sc1
LX_aa 116,240,132
LX_aa 116,244,148
LX_t 224,8,32768
LX_aa 120,240,136
LX_t 226,8,36864
LX_aa 120,244,152
LX_t 228,8,40960
LX_aa 124,240,140
s_add_u32 m0, s48, 100224
LX_t 230,8,45056
LX_aa 124,244,156
LX_w 4
buffer_load_dwordx4 v28, s[28:31], s35 offen offset:128  lds
LX_aa 144,248,128
LX_aa 144,252,144
s_add_u32 m0, s48, 99328
LX_aa 148,248,132
buffer_load_dwordx4 v29, s[28:31], s35 offen offset:0  lds
LX_aa 148,252,148
LX_t 232,21,32768
LX_aa 152,248,136
LX_t 234,21,36864
LX_aa 152,252,152
LX_t 236,21,40960
LX_aa 156,248,140
LX_t 238,21,45056
LX_aa 156,252,156
LX_w 4
LX_aa 176,224,128
LX_aa 176,228,144
LX_aa 180,224,132
LX_aa 180,228,148
LX_t 240,2,32768
LX_aa 184,224,136
LX_t 242,2,36864
LX_aa 184,228,152
LX_t 244,2,40960
LX_aa 188,224,140
s_add_u32 m0, s48, 101248
LX_t 246,2,45056
LX_aa 188,228,156
LX_w 4
buffer_load_dwordx4 v29, s[28:31], s35 offen offset:128  lds
LX_aa 208,232,128
LX_aa 208,236,144
LX_aa 212,232,132
LX_aa 212,236,148
LX_t 248,3,32768
LX_aa 216,232,136
LX_t 250,3,36864
LX_aa 216,236,152
LX_t 252,3,40960
LX_aa 220,232,140
s_add_u32 s35, s60, 786432
s_add_u32 m0, s48, 114688
LX_t 254,3,45056
LX_aa 220,236,156
LX_w 4
buffer_load_dwordx4 v28, s[28:31], s35 offen offset:0  lds
LX_aa 240,240,128
LX_aa 240,244,144
LX_aa 244,240,132
LX_aa 244,244,148
s_waitcnt vmcnt(39) lgkmcnt(0)
s_barrier
LX_t 224,4,49152
LX_aa 248,240,136
LX_t 226,4,53248
LX_aa 248,244,152
LX_t 228,4,57344
LX_aa 252,240,140
s_add_u32 m0, s48, 116608
LX_t 230,4,61440
LX_aa 252,244,156
buffer_load_dwordx4 v28, s[28:31], s35 offen offset:128  lds
LX_aa 224,248,128
LX_aa 224,252,144
LX_aa 228,248,132
LX_aa 228,252,148
LX_t 232,5,49152
LX_aa 232,248,136
LX_t 234,5,53248
LX_aa 232,252,152
LX_t 236,5,57344
LX_aa 236,248,140
s_add_u32 m0, s48, 115712
LX_t 238,5,61440
LX_aa 236,252,156
LX_w 4
s_waitcnt vmcnt(22)
buffer_load_dwordx4 v29, s[28:31], s35 offen offset:0  lds
LX_aa 48,224,160
LX_aa 48,228,176
LX_aa 52,224,164
LX_aa 52,228,180
LX_t 240,6,49152
LX_aa 56,224,168
LX_t 242,6,53248
LX_aa 56,228,184
LX_t 244,6,57344
LX_aa 60,224,172
s_add_u32 m0, s48, 117632
LX_t 246,6,61440
LX_aa 60,228,188
LX_w 4
buffer_load_dwordx4 v29, s[28:31], s35 offen offset:128  lds
LX_aa 80,232,160
LX_aa 80,236,176
LX_aa 84,232,164
LX_aa 84,236,180
LX_t 248,7,49152
LX_aa 88,232,168
LX_t 250,7,53248
LX_aa 88,236,184
LX_t 252,7,57344
LX_aa 92,232,172
LX_t 254,7,61440
LX_aa 92,236,188
LX_w 4
s_add_u32 s35, s60, 1179648
s_add_u32 m0, s48, 131072
LX_aa 112,240,160
buffer_load_dwordx4 v28, s[28:31], s35 offen offset:0  lds
LX_aa 112,244,176
LX_aa 116,240,164
LX_aa 116,244,180
LX_t 224,8,49152
LX_aa 120,240,168
LX_t 226,8,53248
LX_aa 120,244,184
LX_t 228,8,57344
LX_aa 124,240,172
LX_t 230,8,61440
LX_aa 124,244,188
LX_w 4
LX_aa 144,248,160
s_add_u32 m0, s48, 132992
LX_aa 144,252,176
buffer_load_dwordx4 v28, s[28:31], s35 offen offset:128  lds
LX_aa 148,248,164
LX_aa 148,252,180
LX_t 232,21,49152
LX_aa 152,248,168
LX_t 234,21,53248
LX_aa 152,252,184
LX_t 236,21,57344
LX_aa 156,248,172
LX_t 238,21,61440
LX_aa 156,252,188
LX_w 4
LX_aa 176,224,160
LX_aa 176,228,176
s_add_u32 m0, s48, 132096
LX_aa 180,224,164
buffer_load_dwordx4 v29, s[28:31], s35 offen offset:0  lds
LX_aa 180,228,180
s_waitcnt vmcnt(14)
LX_t 240,2,49152
v_cmp_ne_u64 vcc, s[56:57], v[18:19]
LX_aa 184,224,168
LX_t 242,2,53248
LX_aa 184,228,184
s_cbranch_vccnz LX_c6
LX_c6r:
LX_t 244,2,57344
LX_aa 188,224,172
s_add_u32 s33, s34, 49152
LX_t 246,2,61440
LX_aa 188,228,188
LX_w 4
buffer_load_dwordx4 v[160:163], v1, s[20:23], s33 offen offset:0 sc1
LX_aa 208,232,160
LX_aa 208,236,176
buffer_load_dwordx4 v[164:167], v1, s[20:23], s33 offen offset:1024 sc1
LX_aa 212,232,164
LX_aa 212,236,180
LX_t 248,3,49152
LX_aa 216,232,168
LX_t 250,3,53248
LX_aa 216,236,184
LX_t 252,3,57344
LX_aa 220,232,172
s_add_u32 m0, s48, 134016
LX_t 254,3,61440
LX_aa 220,236,188
LX_w 4
buffer_load_dwordx4 v29, s[28:31], s35 offen offset:128  lds
LX_aa 240,240,160
LX_aa 240,244,176
buffer_load_dwordx4 v[168:171], v1, s[20:23], s33 offen offset:2048 sc1
LX_aa 244,240,164
v_add_u32 v4, 65536, v4
LX_aa 244,244,180
LX_t 224,4,0
LX_aa 248,240,168
LX_t 226,4,4096
LX_aa 248,244,184
LX_t 228,4,8192
LX_aa 252,240,172
LX_t 230,4,12288
LX_aa 252,244,188
LX_w 4
buffer_load_dwordx4 v[172:175], v1, s[20:23], s33 offen offset:3072 sc1
LX_aa 224,248,160
LX_aa 224,252,176
buffer_load_dwordx4 v[192:195], v23, s[20:23], s33 offen offset:0 sc1
LX_aa 228,248,164
v_add_u32 v5, 65536, v5
LX_aa 228,252,180
LX_t 232,5,0
LX_aa 232,248,168
LX_t 234,5,4096
)ASM"
R"ASM(LX_aa 232,252,184
LX_t 236,5,8192
LX_aa 236,248,172
LX_t 238,5,12288
LX_aa 236,252,188
LX_w 4
s_waitcnt vmcnt(25)
buffer_load_dwordx4 v[196:199], v23, s[20:23], s33 offen offset:1024 sc1
LX_aa 48,224,192
LX_aa 48,228,208
buffer_load_dwordx4 v[200:203], v23, s[20:23], s33 offen offset:2048 sc1
LX_aa 52,224,196
v_add_u32 v6, 65536, v6
LX_aa 52,228,212
LX_t 240,6,0
LX_aa 56,224,200
LX_t 242,6,4096
LX_aa 56,228,216
LX_t 244,6,8192
LX_aa 60,224,204
LX_t 246,6,12288
LX_aa 60,228,220
LX_w 4
buffer_load_dwordx4 v[204:207], v23, s[20:23], s33 offen offset:3072 sc1
LX_aa 80,232,192
s_add_u32 s33, s34, 81920
LX_aa 80,236,208
LX_l 0,1,0
LX_aa 84,232,196
v_add_u32 v7, 65536, v7
LX_aa 84,236,212
LX_t 248,7,0
LX_aa 88,232,200
LX_t 250,7,4096
LX_aa 88,236,216
LX_t 252,7,8192
LX_aa 92,232,204
LX_t 254,7,12288
LX_aa 92,236,220
LX_w 4
LX_l 4,1,1024
LX_aa 112,240,192
LX_aa 112,244,208
LX_l 8,1,2048
LX_aa 116,240,196
v_add_u32 v8, 65536, v8
LX_aa 116,244,212
LX_t 224,8,0
LX_aa 120,240,200
LX_t 226,8,4096
LX_aa 120,244,216
LX_t 228,8,8192
LX_aa 124,240,204
LX_t 230,8,12288
LX_aa 124,244,220
LX_w 4
LX_l 12,1,3072
LX_aa 144,248,192
LX_aa 144,252,208
LX_l 16,23,0
LX_aa 148,248,196
v_add_u32 v21, 65536, v21
LX_aa 148,252,212
LX_t 232,21,0
LX_aa 152,248,200
LX_t 234,21,4096
LX_aa 152,252,216
LX_t 236,21,8192
LX_aa 156,248,204
LX_t 238,21,12288
LX_aa 156,252,220
LX_w 4
LX_l 20,23,1024
LX_aa 176,224,192
LX_aa 176,228,208
LX_aa 180,224,196
LX_l 24,23,2048
v_add_u32 v2, 65536, v2
LX_aa 180,228,212
LX_t 240,2,0
LX_aa 184,224,200
LX_t 242,2,4096
LX_aa 184,228,216
LX_t 244,2,8192
LX_aa 188,224,204
LX_t 246,2,12288
LX_aa 188,228,220
LX_w 4
LX_l 28,23,3072
LX_aa 208,232,192
LX_aa 208,236,208
LX_aa 212,232,196
v_add_u32 v3, 65536, v3
LX_aa 212,236,212
LX_t 248,3,0
LX_aa 216,232,200
LX_t 250,3,4096
LX_aa 216,236,216
LX_t 252,3,8192
LX_aa 220,232,204
s_add_u32 s33, s34, 106496
LX_t 254,3,12288
LX_aa 220,236,220
LX_w 4
LX_l 32,1,0
LX_aa 240,240,192
LX_aa 240,244,208
LX_l 36,1,1024
LX_aa 244,240,196
buffer_load_dwordx2 v[18:19], v31, s[20:23], s80 offen sc1
LX_aa 244,244,212
LX_t 224,4,16384
LX_aa 248,240,200
LX_t 226,4,20480
LX_aa 248,244,216
LX_t 228,4,24576
LX_aa 252,240,204
LX_t 230,4,28672
LX_aa 252,244,220
LX_w 4
LX_l 40,1,2048
LX_aa 224,248,192
LX_aa 224,252,208
LX_l 44,1,3072
LX_aa 228,248,196
LX_aa 228,252,212
LX_t 232,5,16384
LX_aa 232,248,200
LX_t 234,5,20480
LX_aa 232,252,216
LX_t 236,5,24576
LX_aa 236,248,204
LX_t 238,5,28672
LX_aa 236,252,220
LX_w 4
s_waitcnt vmcnt(32)
LX_l 48,23,0
LX_av 48,224,96
LX_av 48,228,128
LX_l 52,23,1024
LX_av 52,224,100
LX_av 52,228,132
LX_t 240,6,16384
LX_av 56,224,104
LX_t 242,6,20480
LX_av 56,228,136
LX_t 244,6,24576
LX_av 60,224,108
LX_t 246,6,28672
LX_av 60,228,140
LX_w 4
LX_l 56,23,2048
LX_av 80,232,96
LX_av 80,236,128
LX_l 60,23,3072
LX_av 84,232,100
LX_av 84,236,132
LX_t 248,7,16384
LX_av 88,232,104
LX_t 250,7,20480
LX_av 88,236,136
LX_t 252,7,24576
LX_av 92,232,108
s_add_u32 s35, s60, 1703936
s_add_u32 m0, s48, 147456
LX_t 254,7,28672
LX_av 92,236,140
LX_w 4
buffer_load_dwordx4 v28, s[28:31], s35 offen offset:0  lds
LX_av 112,240,96
s_add_u32 s33, s34, 139264
LX_av 112,244,128
LX_l 64,1,0
LX_av 116,240,100
LX_av 116,244,132
LX_t 224,8,16384
LX_av 120,240,104
LX_t 226,8,20480
LX_av 120,244,136
LX_t 228,8,24576
LX_av 124,240,108
s_add_u32 m0, s48, 149376
LX_t 230,8,28672
LX_av 124,244,140
LX_Q0_pad10:
s_cbranch_execz LX_Q0_padret
LX_w 4
buffer_load_dwordx4 v28, s[28:31], s35 offen offset:128  lds
LX_av 144,248,96
LX_av 144,252,128
LX_av 148,248,100
LX_l 68,1,1024
LX_av 148,252,132
LX_t 232,21,16384
LX_av 152,248,104
LX_t 234,21,20480
LX_av 152,252,136
LX_t 236,21,24576
LX_av 156,248,108
s_add_u32 m0, s48, 148480
LX_t 238,21,28672
LX_av 156,252,140
LX_w 4
buffer_load_dwordx4 v29, s[28:31], s35 offen offset:0  lds
LX_av 176,224,96
LX_av 176,228,128
LX_av 180,224,100
LX_l 72,1,2048
LX_av 180,228,132
LX_t 240,2,16384
LX_av 184,224,104
LX_t 242,2,20480
LX_av 184,228,136
LX_t 244,2,24576
LX_av 188,224,108
s_add_u32 m0, s48, 150400
LX_t 246,2,28672
LX_av 188,228,140
LX_w 4
buffer_load_dwordx4 v29, s[28:31], s35 offen offset:128  lds
LX_av 208,232,96
LX_av 208,236,128
LX_av 212,232,100
LX_av 212,236,132
LX_t 248,3,16384
LX_av 216,232,104
LX_t 250,3,20480
LX_av 216,236,136
LX_t 252,3,24576
LX_av 220,232,108
s_add_u32 s35, s60, 393216
s_add_u32 m0, s48, 0
LX_t 254,3,28672
LX_av 220,236,140
LX_w 4
buffer_load_dwordx4 v28, s[28:31], s35 offen offset:0  lds
LX_av 240,240,96
LX_av 240,244,128
LX_l 76,1,3072
LX_av 244,240,100
LX_av 244,244,132
s_waitcnt vmcnt(32) lgkmcnt(0)
s_barrier
LX_t 224,4,32768
LX_av 248,240,104
LX_t 226,4,36864
LX_av 248,244,136
LX_t 228,4,40960
LX_av 252,240,108
s_add_u32 m0, s48, 1920
LX_t 230,4,45056
LX_av 252,244,140
buffer_load_dwordx4 v28, s[28:31], s35 offen offset:128  lds
LX_av 224,248,96
LX_av 224,252,128
LX_l 80,23,0
LX_av 228,248,100
LX_av 228,252,132
LX_t 232,5,32768
LX_av 232,248,104
LX_t 234,5,36864
LX_av 232,252,136
LX_t 236,5,40960
LX_av 236,248,108
s_add_u32 m0, s48, 1024
LX_t 238,5,45056
LX_av 236,252,140
LX_w 4
s_waitcnt vmcnt(28)
buffer_load_dwordx4 v29, s[28:31], s35 offen offset:0  lds
LX_av 48,224,160
LX_av 48,228,192
LX_l 84,23,1024
LX_av 52,224,164
LX_av 52,228,196
LX_t 240,6,32768
LX_av 56,224,168
LX_t 242,6,36864
LX_av 56,228,200
LX_t 244,6,40960
LX_av 60,224,172
LX_t 246,6,45056
LX_av 60,228,204
LX_w 4
LX_l 88,23,2048
LX_av 80,232,160
s_add_u32 m0, s48, 2944
LX_av 80,236,192
buffer_load_dwordx4 v29, s[28:31], s35 offen offset:128  lds
LX_av 84,232,164
LX_av 84,236,196
LX_t 248,7,32768
LX_av 88,232,168
LX_t 250,7,36864
LX_av 88,236,200
LX_t 252,7,40960
LX_av 92,232,172
LX_t 254,7,45056
LX_av 92,236,204
LX_w 4
LX_l 92,23,3072
LX_av 112,240,160
LX_av 112,244,192
s_add_u32 s35, s60, 1310720
s_add_u32 m0, s48, 16384
LX_av 116,240,164
buffer_load_dwordx4 v28, s[28:31], s35 offen offset:0  lds
LX_av 116,244,196
LX_t 224,8,32768
LX_av 120,240,168
LX_t 226,8,36864
LX_av 120,244,200
LX_t 228,8,40960
LX_av 124,240,172
s_add_u32 s33, s34, 57344
LX_t 230,8,45056
LX_av 124,244,204
LX_w 4
s_waitcnt vmcnt(23)
LX_l 96,1,0
v_cmp_ne_u64 vcc, s[56:57], v[18:19]
LX_av 144,248,160
LX_av 144,252,192
s_cbranch_vccnz LX_c8
LX_c8r:
s_add_u32 m0, s48, 18304
LX_av 148,248,164
buffer_load_dwordx4 v28, s[28:31], s35 offen offset:128  lds
LX_av 148,252,196
s_lshl_b32 s53, %3, 9
s_add_u32 s53, s53, 0
LX_t 232,21,32768
LX_av 152,248,168
s_lshl_b32 s54, %2, 6
s_add_u32 s53, s53, s54
LX_t 234,21,36864
LX_av 152,252,200
s_lshl_b32 s54, s41, 17
s_add_u32 s54, s54, s53
LX_t 236,21,40960
LX_av 156,248,172
s_add_u32 s55, s54, 65536
LX_t 238,21,45056
v_and_b32 v9, 63, %4
LX_av 156,252,204
LX_w 4
LX_l 100,1,1024
v_and_b32 v10, 15, v9
LX_av 176,224,160
v_lshrrev_b32 v11, 4, v9
v_lshlrev_b32 v14, 3, v11
LX_av 176,228,192
s_mov_b64 s[16:17], s[96:97]
s_and_b32 s17, s17, 0xffff
s_mov_b32 s18, 2048
LX_av 180,224,164
buffer_load_dwordx2 v[64:65], v14, s[16:19], s53 offen offset:0
LX_av 180,228,196
LX_t 240,4,49152
LX_av 184,224,168
LX_t 242,4,53248
LX_av 184,228,200
LX_t 244,4,57344
LX_av 188,224,172
s_add_u32 m0, s48, 17408
LX_t 246,4,61440
LX_av 188,228,204
LX_w 4
buffer_load_dwordx4 v29, s[28:31], s35 offen offset:0  lds
LX_av 208,232,160
s_add_u32 m0, s48, 19328
buffer_load_dwordx2 v[66:67], v14, s[16:19], s53 offen offset:32
s_add_u32 s49, s48, 81920
LX_av 208,236,192
buffer_load_dwordx4 v29, s[28:31], s35 offen offset:128  lds
v_lshl_add_u32 v12, v10, 6, v14
LX_av 212,232,164
v_add_u32 v12, s49, v12
v_lshl_add_u32 v13, v9, 4, s49
LX_av 212,236,196
LX_t 248,5,49152
v_lshrrev_b32 v15, 2, v9
LX_av 216,232,168
LX_t 250,5,53248
v_lshlrev_b32 v15, 12, v15
LX_av 216,236,200
LX_t 252,5,57344
v_and_b32 v14, 48, v13
LX_av 220,232,172
LX_t 254,5,61440
v_add_u32 v15, v15, v14
LX_av 220,236,204
LX_w 4
s_waitcnt vmcnt(33)
LX_l 104,1,2048
LX_aa 48,240,0
v_lshlrev_b32 v11, 2, v11
v_sub_u32 v11, v10, v11
LX_aa 48,244,16
LX_l 108,1,3072
v_mov_b32 v16, 0x3f80
LX_aa 52,240,4
v_mov_b32 v17, 1.0
v_cmp_eq_u32 vcc, 0, v11
LX_aa 52,244,20
LX_t 224,6,49152
v_mov_b32 v70, 0
LX_aa 56,240,8
LX_t 226,6,53248
v_mov_b32 v72, 0
LX_aa 56,244,24
LX_t 228,6,57344
v_cndmask_b32 v68, 0, v16, vcc
LX_aa 60,240,12
LX_t 230,6,61440
v_cmp_eq_u32 vcc, 1, v11
LX_aa 60,244,28
LX_w 4
s_nop 0
LX_l 112,23,0
LX_aa 80,248,0
s_nop 0
v_cndmask_b32 v68, v68, v17, vcc
v_mov_b32 v74, v68
LX_aa 80,252,16
LX_l 116,23,1024
v_cmp_eq_u32 vcc, 2, v11
LX_aa 84,248,4
v_mov_b32 v71, 0
v_mov_b32 v73, 0
LX_aa 84,252,20
LX_t 232,7,49152
v_cndmask_b32 v69, 0, v16, vcc
LX_aa 88,248,8
LX_t 234,7,53248
v_cmp_eq_u32 vcc, 3, v11
LX_aa 88,252,24
s_nop 0
s_nop 0
LX_t 236,7,57344
LX_aa 92,248,12
LX_t 238,7,61440
v_cndmask_b32 v69, v69, v17, vcc
LX_aa 92,252,28
LX_w 4
LX_l 120,23,2048
v_mov_b32 v75, v69
LX_aa 112,224,0
s_mov_b64 s[16:17], s[98:99]
s_and_b32 s17, s17, 0xffff
s_mov_b32 s18, 4194304
LX_aa 112,228,16
LX_l 124,23,3072
v_lshrrev_b32 v20, 4, v9
LX_aa 116,224,4
v_min_u32 v22, 1, v20
v_min_u32 v20, 2, v20
LX_aa 116,228,20
LX_t 240,8,49152
v_lshlrev_b32 v20, 7, v20
LX_aa 120,224,8
LX_t 242,8,53248
v_lshlrev_b32 v22, 7, v22
LX_aa 120,228,24
s_lshl_b32 s52, s81, 9
s_add_u32 s52, s52, 36175872
LX_t 244,8,57344
LX_aa 124,224,12
s_add_u32 s33, s34, 114688
LX_t 246,8,61440
LX_aa 124,228,28
LX_w 4
LX_l 128,1,0
LX_aa 144,232,0
s_xor_b32 s50, s81, 4
s_lshl_b32 s96, s50, 9
LX_aa 144,236,16
s_add_u32 s96, s96, 36175872
s_bfe_u32 s50, s50, 0x50002
LX_aa 148,232,4
s_mul_i32 s50, s50, 12288
s_add_u32 s50, s50, s38
LX_l 132,1,1024
LX_aa 148,236,20
s_add_u32 s42, s50, 720896
LX_t 248,21,49152
LX_aa 152,232,8
s_xor_b32 s50, s81, 8
s_lshl_b32 s97, s50, 9
LX_t 250,21,53248
LX_aa 152,236,24
s_add_u32 s97, s97, 36175872
s_bfe_u32 s50, s50, 0x50002
LX_t 252,21,57344
LX_aa 156,232,12
s_mul_i32 s50, s50, 12288
s_add_u32 s50, s50, s38
LX_t 254,21,61440
LX_aa 156,236,28
LX_w 4
s_add_u32 s43, s50, 724992
LX_l 136,1,2048
LX_aa 176,240,0
s_xor_b32 s50, s81, 12
s_lshl_b32 s98, s50, 9
LX_aa 176,244,16
s_add_u32 s98, s98, 36175872
s_bfe_u32 s50, s50, 0x50002
LX_aa 180,240,4
LX_l 140,1,3072
v_add_u32 v4, 65536, v4
LX_aa 180,244,20
s_mul_i32 s50, s50, 12288
s_add_u32 s50, s50, s38
LX_t 224,4,0
LX_aa 184,240,8
s_add_u32 s44, s50, 729088
LX_t 226,4,4096
LX_aa 184,244,24
s_bfe_u32 s51, s81, 0x50002
s_mul_i32 s51, s51, 12288
LX_t 228,4,8192
LX_aa 188,240,12
s_add_u32 s51, s51, s38
s_add_u32 s51, s51, 720896
LX_t 230,4,12288
LX_aa 188,244,28
LX_w 4
s_add_u32 s50, s51, 8192
LX_l 144,23,0
LX_aa 208,248,0
LX_aa 208,252,16
LX_aa 212,248,4
v_add_u32 v5, 65536, v5
LX_aa 212,252,20
LX_t 232,5,0
LX_aa 216,248,8
LX_t 234,5,4096
LX_aa 216,252,24
LX_t 236,5,8192
LX_aa 220,248,12
LX_t 238,5,12288
LX_aa 220,252,28
LX_w 4
s_waitcnt vmcnt(35)
LX_l 148,23,1024
LX_aa 48,224,32
LX_aa 48,228,48
LX_l 152,23,2048
LX_aa 52,224,36
v_add_u32 v6, 65536, v6
LX_aa 52,228,52
LX_t 240,6,0
LX_aa 56,224,40
LX_t 242,6,4096
LX_aa 56,228,56
LX_t 244,6,8192
LX_aa 60,224,44
LX_t 246,6,12288
LX_aa 60,228,60
LX_w 4
LX_l 156,23,3072
LX_aa 80,232,32
s_add_u32 s35, s93, 0
s_add_u32 m0, s48, 32768
LX_aa 80,236,48
buffer_load_dwordx4 v28, s[84:87], s35 offen offset:0  lds
LX_aa 84,232,36
v_add_u32 v7, 65536, v7
LX_aa 84,236,52
LX_t 248,7,0
LX_aa 88,232,40
LX_t 250,7,4096
LX_aa 88,236,56
LX_t 252,7,8192
LX_aa 92,232,44
s_add_u32 m0, s48, 34688
LX_t 254,7,12288
LX_aa 92,236,60
LX_w 4
buffer_load_dwordx4 v28, s[84:87], s35 offen offset:128  lds
LX_aa 112,240,32
s_add_u32 s33, s89, 0
LX_aa 112,244,48
LX_l 160,1,0
LX_aa 116,240,36
v_add_u32 v8, 65536, v8
LX_aa 116,244,52
LX_t 224,8,0
LX_aa 120,240,40
LX_t 226,8,4096
LX_aa 120,244,56
LX_t 228,8,8192
LX_aa 124,240,44
s_add_u32 m0, s48, 33792
LX_t 230,8,12288
LX_aa 124,244,60
LX_w 4
buffer_load_dwordx4 v29, s[84:87], s35 offen offset:0  lds
LX_aa 144,248,32
LX_aa 144,252,48
LX_l 164,1,1024
LX_aa 148,248,36
v_add_u32 v21, 65536, v21
LX_aa 148,252,52
LX_t 232,21,0
LX_aa 152,248,40
LX_t 234,21,4096
LX_aa 152,252,56
LX_t 236,21,8192
LX_aa 156,248,44
s_add_u32 m0, s48, 35712
LX_t 238,21,12288
LX_aa 156,252,60
LX_w 4
buffer_load_dwordx4 v29, s[84:87], s35 offen offset:128  lds
LX_aa 176,224,32
)ASM"
R"ASM(LX_aa 176,228,48
LX_aa 180,224,36
LX_l 168,1,2048
LX_aa 180,228,52
s_waitcnt vmcnt(21) lgkmcnt(0)
s_barrier
LX_t 240,4,16384
LX_aa 184,224,40
LX_t 242,4,20480
LX_aa 184,228,56
LX_t 244,4,24576
LX_aa 188,224,44
s_add_u32 s35, s94, 0
s_add_u32 m0, s48, 49152
LX_t 246,4,28672
LX_aa 188,228,60
buffer_load_dwordx4 v28, s[84:87], s35 offen offset:0  lds
LX_aa 208,232,32
LX_aa 208,236,48
LX_aa 212,232,36
LX_l 172,1,3072
LX_aa 212,236,52
LX_t 248,5,16384
LX_aa 216,232,40
LX_t 250,5,20480
LX_aa 216,236,56
LX_t 252,5,24576
LX_aa 220,232,44
LX_t 254,5,28672
LX_aa 220,236,60
LX_w 4
s_waitcnt vmcnt(31)
LX_l 176,23,0
LX_aa 48,240,64
LX_aa 48,244,80
LX_aa 52,240,68
LX_aa 52,244,84
LX_t 224,6,16384
LX_aa 56,240,72
LX_t 226,6,20480
LX_aa 56,244,88
LX_t 228,6,24576
LX_aa 60,240,76
s_add_u32 m0, s48, 51072
LX_t 230,6,28672
LX_aa 60,244,92
LX_w 4
buffer_load_dwordx4 v28, s[84:87], s35 offen offset:128  lds
LX_aa 80,248,64
LX_aa 80,252,80
LX_l 180,23,1024
LX_aa 84,248,68
LX_aa 84,252,84
LX_t 232,7,16384
LX_aa 88,248,72
LX_t 234,7,20480
LX_aa 88,252,88
LX_t 236,7,24576
LX_aa 92,248,76
s_add_u32 m0, s48, 50176
LX_t 238,7,28672
LX_aa 92,252,92
LX_w 4
buffer_load_dwordx4 v29, s[84:87], s35 offen offset:0  lds
LX_aa 112,224,64
LX_aa 112,228,80
LX_l 184,23,2048
LX_aa 116,224,68
LX_aa 116,228,84
LX_t 240,8,16384
LX_aa 120,224,72
LX_t 242,8,20480
LX_aa 120,228,88
LX_t 244,8,24576
LX_aa 124,224,76
s_add_u32 m0, s48, 52096
LX_t 246,8,28672
LX_aa 124,228,92
LX_w 4
buffer_load_dwordx4 v29, s[84:87], s35 offen offset:128  lds
LX_aa 144,232,64
LX_aa 144,236,80
LX_l 188,23,3072
LX_aa 148,232,68
LX_aa 148,236,84
LX_t 248,21,16384
LX_aa 152,232,72
LX_t 250,21,20480
LX_aa 152,236,88
LX_t 252,21,24576
LX_aa 156,232,76
s_add_u32 s35, s95, 0
s_add_u32 m0, s48, 65536
LX_t 254,21,28672
LX_aa 156,236,92
LX_w 4
buffer_load_dwordx4 v28, s[84:87], s35 offen offset:0  lds
LX_aa 176,240,64
s_add_u32 m0, s48, 67456
LX_aa 176,244,80
buffer_load_dwordx4 v28, s[84:87], s35 offen offset:128  lds
LX_aa 180,240,68
v_add_u32 v4, 4294836224, v4
LX_aa 180,244,84
LX_t 224,4,0
LX_aa 184,240,72
LX_t 226,4,4096
LX_aa 184,244,88
LX_t 228,4,8192
LX_aa 188,240,76
s_add_u32 m0, s48, 66560
LX_t 230,4,12288
LX_aa 188,244,92
LX_w 4
buffer_load_dwordx4 v29, s[84:87], s35 offen offset:0  lds
LX_aa 208,248,64
s_add_u32 m0, s48, 68480
LX_aa 208,252,80
buffer_load_dwordx4 v29, s[84:87], s35 offen offset:128  lds
LX_aa 212,248,68
v_add_u32 v5, 4294836224, v5
LX_aa 212,252,84
LX_t 232,5,0
LX_aa 216,248,72
LX_t 234,5,4096
LX_aa 216,252,88
LX_t 236,5,8192
LX_aa 220,248,76
LX_Q0_pad11:
s_cbranch_execz LX_Q0_padret
s_add_u32 s33, s90, 0
LX_t 238,5,12288
LX_aa 220,252,92
LX_w 4
s_waitcnt vmcnt(28)
LX_l 192,1,0
LX_aa 48,224,96
LX_aa 48,228,112
LX_aa 52,224,100
LX_l 196,1,1024
v_add_u32 v6, 4294836224, v6
LX_aa 52,228,116
LX_t 240,6,0
LX_aa 56,224,104
LX_t 242,6,4096
LX_aa 56,228,120
LX_t 244,6,8192
LX_aa 60,224,108
LX_t 246,6,12288
LX_aa 60,228,124
LX_w 4
LX_l 200,1,2048
LX_aa 80,232,96
LX_aa 80,236,112
LX_aa 84,232,100
v_add_u32 v7, 4294836224, v7
LX_aa 84,236,116
LX_t 248,7,0
LX_aa 88,232,104
LX_t 250,7,4096
LX_aa 88,236,120
LX_t 252,7,8192
LX_aa 92,232,108
LX_t 254,7,12288
LX_aa 92,236,124
LX_w 4
LX_l 204,1,3072
LX_aa 112,240,96
LX_aa 112,244,112
LX_l 208,23,0
LX_aa 116,240,100
v_add_u32 v8, 4294836224, v8
LX_aa 116,244,116
LX_t 224,8,0
LX_aa 120,240,104
LX_t 226,8,4096
LX_aa 120,244,120
LX_t 228,8,8192
LX_aa 124,240,108
LX_t 230,8,12288
LX_aa 124,244,124
LX_w 4
LX_l 212,23,1024
LX_aa 144,248,96
LX_aa 144,252,112
LX_l 216,23,2048
LX_aa 148,248,100
v_add_u32 v21, 4294836224, v21
LX_aa 148,252,116
LX_t 232,21,0
LX_aa 152,248,104
LX_t 234,21,4096
LX_aa 152,252,120
LX_t 236,21,8192
LX_aa 156,248,108
LX_t 238,21,12288
LX_aa 156,252,124
LX_w 4
LX_l 220,23,3072
LX_aa 176,224,96
s_add_u32 s33, s91, 0
LX_aa 176,228,112
buffer_load_dwordx4 v[96:99], v1, s[20:23], s33 offen offset:0 sc1
LX_aa 180,224,100
LX_aa 180,228,116
LX_t 240,4,16384
LX_aa 184,224,104
LX_t 242,4,20480
LX_aa 184,228,120
LX_t 244,4,24576
LX_aa 188,224,108
LX_t 246,4,28672
LX_aa 188,228,124
LX_w 4
buffer_load_dwordx4 v[100:103], v1, s[20:23], s33 offen offset:1024 sc1
LX_aa 208,232,96
LX_aa 208,236,112
buffer_load_dwordx4 v[104:107], v1, s[20:23], s33 offen offset:2048 sc1
LX_aa 212,232,100
LX_aa 212,236,116
LX_t 248,5,16384
LX_aa 216,232,104
LX_t 250,5,20480
LX_aa 216,236,120
LX_t 252,5,24576
LX_aa 220,232,108
LX_t 254,5,28672
LX_aa 220,236,124
LX_w 4
s_waitcnt vmcnt(31)
buffer_load_dwordx4 v[108:111], v1, s[20:23], s33 offen offset:3072 sc1
LX_aa 48,240,128
LX_aa 48,244,144
buffer_load_dwordx4 v[128:131], v23, s[20:23], s33 offen offset:0 sc1
LX_aa 52,240,132
LX_aa 52,244,148
LX_t 224,6,16384
LX_aa 56,240,136
LX_t 226,6,20480
LX_aa 56,244,152
LX_t 228,6,24576
LX_aa 60,240,140
LX_t 230,6,28672
LX_aa 60,244,156
LX_w 4
buffer_load_dwordx4 v[132:135], v23, s[20:23], s33 offen offset:1024 sc1
LX_aa 80,248,128
LX_aa 80,252,144
LX_aa 84,248,132
buffer_load_dwordx4 v[136:139], v23, s[20:23], s33 offen offset:2048 sc1
LX_aa 84,252,148
LX_t 232,7,16384
LX_aa 88,248,136
LX_t 234,7,20480
LX_aa 88,252,152
LX_t 236,7,24576
LX_aa 92,248,140
LX_t 238,7,28672
LX_aa 92,252,156
LX_w 4
buffer_load_dwordx4 v[140:143], v23, s[20:23], s33 offen offset:3072 sc1
LX_aa 112,224,128
LX_aa 112,228,144
LX_aa 116,224,132
LX_aa 116,228,148
LX_t 240,8,16384
LX_aa 120,224,136
LX_t 242,8,20480
LX_aa 120,228,152
LX_t 244,8,24576
LX_aa 124,224,140
s_add_u32 m0, s49, 0
LX_t 246,8,28672
LX_aa 124,228,156
LX_w 4
buffer_load_dwordx4 v15, s[24:27], s54 offen offset:0 lds
LX_aa 144,232,128
LX_aa 144,236,144
s_add_u32 m0, s49, 4294966272
LX_aa 148,232,132
buffer_load_dwordx4 v15, s[24:27], s54 offen offset:2048 lds
LX_aa 148,236,148
LX_t 248,21,16384
LX_aa 152,232,136
LX_t 250,21,20480
LX_aa 152,236,152
LX_t 252,21,24576
LX_aa 156,232,140
LX_t 254,21,28672
LX_aa 156,236,156
LX_w 4
LX_aa 176,240,128
s_add_u32 m0, s49, 2048
LX_aa 176,244,144
buffer_load_dwordx4 v15, s[24:27], s55 offen offset:0 lds
LX_aa 180,240,132
LX_aa 180,244,148
s_waitcnt vmcnt(19) lgkmcnt(0)
s_barrier
LX_t 224,4,32768
LX_aa 184,240,136
LX_t 226,4,36864
LX_aa 184,244,152
LX_t 228,4,40960
LX_aa 188,240,140
LX_t 230,4,45056
LX_aa 188,244,156
s_add_u32 m0, s49, 1024
LX_aa 208,248,128
buffer_load_dwordx4 v15, s[24:27], s55 offen offset:2048 lds
LX_aa 208,252,144
LX_aa 212,248,132
LX_aa 212,252,148
LX_t 232,4,49152
LX_aa 216,248,136
LX_t 234,4,53248
LX_aa 216,252,152
LX_t 236,4,57344
LX_aa 220,248,140
LX_t 238,4,61440
LX_aa 220,252,156
LX_w 4
s_waitcnt vmcnt(24)
LX_aa 48,224,160
LX_aa 48,228,176
LX_aa 52,224,164
v_add_u32 v4, 65536, v4
LX_aa 52,228,180
LX_t 240,4,0
LX_aa 56,224,168
LX_t 242,4,4096
LX_aa 56,228,184
LX_t 244,4,8192
LX_aa 60,224,172
LX_t 246,4,12288
LX_aa 60,228,188
LX_w 4
s_waitcnt vmcnt(12)
LX_aa 48,232,192
LX_aa 48,236,208
LX_aa 52,232,196
LX_aa 52,236,212
LX_t 248,5,32768
LX_aa 56,232,200
LX_t 250,5,36864
LX_aa 56,236,216
LX_t 252,5,40960
LX_aa 60,232,204
LX_t 254,5,45056
LX_aa 60,236,220
LX_w 4
s_waitcnt vmcnt(4)
LX_av 48,240,96
LX_av 48,244,128
LX_av 52,240,100
LX_av 52,244,132
LX_t 224,5,49152
LX_av 56,240,104
LX_t 226,5,53248
LX_av 56,244,136
LX_t 228,5,57344
LX_av 60,240,108
LX_t 230,5,61440
LX_av 60,244,140
LX_w 4
LX_aa 80,248,160
LX_aa 80,252,176
LX_aa 84,248,164
v_add_u32 v5, 65536, v5
LX_aa 84,252,180
LX_t 232,5,0
LX_aa 88,248,168
LX_t 234,5,4096
LX_aa 88,252,184
LX_t 236,5,8192
LX_c 48,48,49
LX_aa 92,248,172
LX_t 238,5,12288
LX_c 49,50,51
LX_aa 92,252,188
LX_w 4
LX_c 50,52,53
LX_c 51,54,55
LX_aa 80,224,192
buffer_store_dwordx4 v[48:51], v1, s[20:23], s42 offen offset:0 sc1
LX_c 56,56,57
LX_aa 80,228,208
LX_c 57,58,59
LX_c 58,60,61
LX_aa 84,224,196
LX_c 59,62,63
LX_aa 84,228,212
LX_t 240,6,32768
LX_aa 88,224,200
LX_t 242,6,36864
LX_aa 88,228,216
LX_t 244,6,40960
LX_aa 92,224,204
LX_t 246,6,45056
LX_aa 92,228,220
LX_w 4
buffer_store_dwordx4 v[56:59], v1, s[20:23], s42 offen offset:1024 sc1
LX_av 80,232,96
LX_av 80,236,128
LX_av 84,232,100
LX_av 84,236,132
LX_t 248,6,49152
LX_av 88,232,104
LX_t 250,6,53248
LX_av 88,236,136
LX_t 252,6,57344
LX_av 92,232,108
LX_t 254,6,61440
LX_av 92,236,140
LX_w 4
LX_aa 112,240,160
LX_aa 112,244,176
LX_aa 116,240,164
v_add_u32 v6, 65536, v6
LX_aa 116,244,180
LX_t 224,6,0
LX_aa 120,240,168
LX_t 226,6,4096
LX_aa 120,244,184
LX_t 228,6,8192
LX_c 80,80,81
LX_aa 124,240,172
LX_t 230,6,12288
LX_c 81,82,83
LX_aa 124,244,188
LX_w 4
LX_c 82,84,85
LX_c 83,86,87
LX_aa 112,248,192
buffer_store_dwordx4 v[80:83], v1, s[20:23], s42 offen offset:2048 sc1
LX_c 88,88,89
LX_aa 112,252,208
LX_c 89,90,91
LX_c 90,92,93
LX_aa 116,248,196
LX_c 91,94,95
LX_aa 116,252,212
LX_t 232,7,32768
LX_aa 120,248,200
LX_t 234,7,36864
LX_aa 120,252,216
LX_t 236,7,40960
LX_aa 124,248,204
LX_t 238,7,45056
LX_aa 124,252,220
LX_w 4
buffer_store_dwordx4 v[88:91], v1, s[20:23], s42 offen offset:3072 sc1
LX_av 112,224,96
LX_av 112,228,128
LX_av 116,224,100
LX_av 116,228,132
LX_t 240,7,49152
LX_av 120,224,104
LX_t 242,7,53248
LX_av 120,228,136
LX_t 244,7,57344
LX_av 124,224,108
LX_t 246,7,61440
LX_av 124,228,140
LX_w 4
LX_aa 144,232,160
LX_aa 144,236,176
LX_aa 148,232,164
v_add_u32 v7, 65536, v7
LX_aa 148,236,180
LX_t 248,7,0
LX_aa 152,232,168
LX_t 250,7,4096
LX_aa 152,236,184
LX_t 252,7,8192
LX_c 112,112,113
LX_aa 156,232,172
LX_t 254,7,12288
LX_c 113,114,115
LX_aa 156,236,188
LX_w 4
LX_c 114,116,117
LX_c 115,118,119
LX_aa 144,240,192
buffer_store_dwordx4 v[112:115], v1, s[20:23], s43 offen offset:0 sc1
LX_c 120,120,121
LX_aa 144,244,208
LX_c 121,122,123
LX_c 122,124,125
LX_aa 148,240,196
LX_c 123,126,127
LX_aa 148,244,212
LX_t 224,8,32768
LX_aa 152,240,200
LX_t 226,8,36864
LX_aa 152,244,216
LX_t 228,8,40960
LX_aa 156,240,204
LX_t 230,8,45056
LX_aa 156,244,220
LX_w 4
buffer_store_dwordx4 v[120:123], v1, s[20:23], s43 offen offset:1024 sc1
LX_av 144,248,96
LX_av 144,252,128
LX_av 148,248,100
LX_av 148,252,132
LX_t 232,8,49152
LX_av 152,248,104
LX_t 234,8,53248
LX_av 152,252,136
LX_t 236,8,57344
LX_av 156,248,108
LX_t 238,8,61440
LX_av 156,252,140
LX_w 4
LX_aa 176,224,160
LX_aa 176,228,176
LX_aa 180,224,164
v_add_u32 v8, 65536, v8
LX_aa 180,228,180
LX_t 240,8,0
LX_aa 184,224,168
LX_t 242,8,4096
LX_aa 184,228,184
LX_t 244,8,8192
LX_c 144,144,145
LX_aa 188,224,172
LX_t 246,8,12288
LX_c 145,146,147
LX_aa 188,228,188
LX_w 4
LX_c 146,148,149
LX_c 147,150,151
LX_aa 176,232,192
buffer_store_dwordx4 v[144:147], v1, s[20:23], s43 offen offset:2048 sc1
LX_c 152,152,153
LX_aa 176,236,208
LX_c 153,154,155
LX_c 154,156,157
LX_aa 180,232,196
LX_c 155,158,159
LX_aa 180,236,212
LX_t 248,21,32768
LX_aa 184,232,200
LX_t 250,21,36864
LX_aa 184,236,216
LX_t 252,21,40960
LX_aa 188,232,204
LX_t 254,21,45056
LX_aa 188,236,220
LX_w 4
buffer_store_dwordx4 v[152:155], v1, s[20:23], s43 offen offset:3072 sc1
LX_av 176,240,96
LX_av 176,244,128
LX_av 180,240,100
LX_av 180,244,132
LX_t 224,21,49152
LX_av 184,240,104
LX_t 226,21,53248
LX_av 184,244,136
LX_t 228,21,57344
LX_av 188,240,108
LX_t 230,21,61440
LX_av 188,244,140
LX_w 4
LX_aa 208,248,160
LX_aa 208,252,176
LX_aa 212,248,164
v_add_u32 v21, 65536, v21
LX_aa 212,252,180
LX_t 232,21,0
LX_aa 216,248,168
LX_t 234,21,4096
LX_aa 216,252,184
LX_t 236,21,8192
LX_c 176,176,177
LX_aa 220,248,172
LX_t 238,21,12288
LX_c 177,178,179
LX_aa 220,252,188
LX_w 4
LX_c 178,180,181
LX_c 179,182,183
LX_aa 208,224,192
buffer_store_dwordx4 v[176:179], v1, s[20:23], s44 offen offset:0 sc1
LX_c 184,184,185
LX_aa 208,228,208
LX_c 185,186,187
LX_c 186,188,189
LX_aa 212,224,196
LX_c 187,190,191
LX_aa 212,228,212
LX_t 240,2,32768
LX_aa 216,224,200
LX_t 242,2,36864
LX_aa 216,228,216
LX_t 244,2,40960
LX_aa 220,224,204
LX_t 246,2,45056
LX_aa 220,228,220
LX_w 4
s_waitcnt vmcnt(5)
buffer_store_dwordx2 v[26:27], v30, s[20:23], s96 offen offset:0 sc1
LX_av 208,232,96
buffer_store_dwordx4 v[184:187], v1, s[20:23], s44 offen offset:1024 sc1
LX_av 208,236,128
LX_av 212,232,100
LX_av 212,236,132
LX_t 248,3,32768
LX_av 216,232,104
LX_t 250,3,36864
LX_av 216,236,136
LX_t 252,3,40960
LX_av 220,232,108
LX_t 254,3,45056
LX_av 220,236,140
LX_w 4
LX_av 240,240,160
LX_av 240,244,192
)ASM"
R"ASM(LX_av 244,240,164
LX_av 244,244,196
LX_t 224,2,49152
LX_av 248,240,168
LX_t 226,2,53248
LX_av 248,244,200
LX_t 228,2,57344
LX_c 208,208,209
LX_av 252,240,172
LX_t 230,2,61440
LX_c 209,210,211
LX_av 252,244,204
LX_w 4
LX_c 210,212,213
LX_c 211,214,215
LX_av 224,248,160
buffer_store_dwordx4 v[208:211], v1, s[20:23], s44 offen offset:2048 sc1
LX_c 216,216,217
LX_av 224,252,192
LX_c 217,218,219
LX_c 218,220,221
LX_av 228,248,164
LX_c 219,222,223
LX_av 228,252,196
LX_t 232,3,49152
LX_av 232,248,168
LX_t 234,3,53248
LX_av 232,252,200
LX_t 236,3,57344
LX_av 236,248,172
LX_t 238,3,61440
LX_av 236,252,204
LX_w 4
buffer_store_dwordx4 v[216:219], v1, s[20:23], s44 offen offset:3072 sc1
LX_aa 240,224,0
LX_aa 240,228,16
buffer_load_dwordx2 v[18:19], v20, s[20:23], s52 offen sc1
LX_aa 244,224,4
v_add_u32 v2, 65536, v2
LX_aa 244,228,20
LX_t 240,2,0
LX_aa 248,224,8
LX_t 242,2,4096
LX_aa 248,228,24
LX_Q0_pad12:
s_cbranch_execz LX_Q0_padret
LX_t 244,2,8192
LX_aa 252,224,12
LX_t 246,2,12288
LX_aa 252,228,28
LX_w 4
LX_aa 224,232,0
LX_aa 224,236,16
LX_aa 228,232,4
v_add_u32 v3, 65536, v3
LX_aa 228,236,20
LX_t 248,3,0
LX_aa 232,232,8
LX_t 250,3,4096
LX_aa 232,236,24
LX_t 252,3,8192
LX_aa 236,232,12
LX_t 254,3,12288
LX_aa 236,236,28
LX_w 4
LX_aa 240,240,32
LX_aa 240,244,48
LX_aa 244,240,36
LX_aa 244,244,52
LX_t 224,2,16384
LX_aa 248,240,40
LX_t 226,2,20480
LX_aa 248,244,56
LX_t 228,2,24576
LX_aa 252,240,44
LX_t 230,2,28672
LX_aa 252,244,60
LX_w 4
s_waitcnt vmcnt(6)
buffer_store_dwordx2 v[26:27], v30, s[20:23], s97 offen offset:128 sc1
LX_aa 224,248,32
LX_aa 224,252,48
LX_aa 228,248,36
LX_aa 228,252,52
LX_t 232,3,16384
LX_aa 232,248,40
LX_t 234,3,20480
LX_aa 232,252,56
LX_t 236,3,24576
LX_aa 236,248,44
LX_t 238,3,28672
LX_aa 236,252,60
LX_w 4
LX_aa 240,224,64
LX_aa 240,228,80
LX_aa 244,224,68
v_add_u32 v2, 4294836224, v2
LX_aa 244,228,84
LX_t 240,2,0
LX_aa 248,224,72
LX_t 242,2,4096
LX_aa 248,228,88
LX_t 244,2,8192
LX_aa 252,224,76
LX_t 246,2,12288
LX_aa 252,228,92
LX_w 4
LX_aa 224,232,64
LX_aa 224,236,80
LX_aa 228,232,68
v_add_u32 v3, 4294836224, v3
LX_aa 228,236,84
LX_t 248,3,0
LX_aa 232,232,72
LX_t 250,3,4096
LX_aa 232,236,88
LX_t 252,3,8192
LX_aa 236,232,76
LX_t 254,3,12288
LX_aa 236,236,92
LX_w 4
LX_aa 240,240,96
LX_aa 240,244,112
LX_aa 244,240,100
LX_aa 244,244,116
LX_t 224,2,16384
LX_aa 248,240,104
LX_t 226,2,20480
LX_aa 248,244,120
LX_t 228,2,24576
LX_aa 252,240,108
LX_t 230,2,28672
LX_aa 252,244,124
LX_w 4
LX_aa 224,248,96
LX_aa 224,252,112
LX_aa 228,248,100
LX_aa 228,252,116
LX_t 232,3,16384
LX_aa 232,248,104
LX_t 234,3,20480
LX_aa 232,252,120
LX_t 236,3,24576
LX_aa 236,248,108
LX_t 238,3,28672
LX_aa 236,252,124
LX_w 4
s_waitcnt vmcnt(2)
buffer_store_dwordx2 v[26:27], v30, s[20:23], s98 offen offset:256 sc1
LX_aa 240,224,128
LX_aa 240,228,144
s_waitcnt vmcnt(2)
v_cmp_eq_u64 s[36:37], s[56:57], v[18:19]
LX_aa 244,224,132
s_nop 0
s_bitcmp1_b64 s[36:37], 0
s_cselect_b32 s53, 0, 1073737728
v_add_u32 v16, s53, v1
LX_aa 244,228,148
LX_t 240,2,32768
LX_aa 248,224,136
LX_t 242,2,36864
LX_aa 248,228,152
LX_t 244,2,40960
LX_aa 252,224,140
LX_t 246,2,45056
LX_aa 252,228,156
LX_w 4
buffer_load_dwordx4 v[48:51], v16, s[20:23], s51 offen offset:0 sc1
LX_aa 224,232,128
buffer_load_dwordx4 v[52:55], v16, s[20:23], s51 offen offset:1024 sc1
LX_aa 224,236,144
buffer_load_dwordx4 v[56:59], v16, s[20:23], s51 offen offset:2048 sc1
LX_aa 228,232,132
buffer_load_dwordx4 v[60:63], v16, s[20:23], s51 offen offset:3072 sc1
LX_aa 228,236,148
s_bitcmp1_b64 s[36:37], 16
s_cselect_b32 s53, 0, 1073737728
LX_t 248,3,32768
LX_aa 232,232,136
LX_t 250,3,36864
v_add_u32 v17, s53, v23
LX_aa 232,236,152
LX_t 252,3,40960
LX_aa 236,232,140
LX_t 254,3,45056
LX_aa 236,236,156
LX_w 4
buffer_load_dwordx4 v[80:83], v17, s[20:23], s51 offen offset:0 sc1
LX_aa 240,240,160
buffer_load_dwordx4 v[84:87], v17, s[20:23], s51 offen offset:1024 sc1
LX_aa 240,244,176
buffer_load_dwordx4 v[88:91], v17, s[20:23], s51 offen offset:2048 sc1
LX_aa 244,240,164
buffer_load_dwordx4 v[92:95], v17, s[20:23], s51 offen offset:3072 sc1
LX_aa 244,244,180
s_bitcmp1_b64 s[36:37], 32
s_cselect_b32 s53, 0, 1073737728
LX_t 224,2,49152
LX_aa 248,240,168
LX_t 226,2,53248
v_add_u32 v16, s53, v1
LX_aa 248,244,184
LX_t 228,2,57344
LX_aa 252,240,172
LX_t 230,2,61440
LX_aa 252,244,188
LX_w 4
buffer_load_dwordx4 v[112:115], v16, s[20:23], s50 offen offset:0 sc1
LX_aa 224,248,160
buffer_load_dwordx4 v[116:119], v16, s[20:23], s50 offen offset:1024 sc1
LX_aa 224,252,176
buffer_load_dwordx4 v[120:123], v16, s[20:23], s50 offen offset:2048 sc1
LX_aa 228,248,164
buffer_load_dwordx4 v[124:127], v16, s[20:23], s50 offen offset:3072 sc1
LX_aa 228,252,180
LX_t 232,3,49152
LX_aa 232,248,168
LX_t 234,3,53248
LX_aa 232,252,184
LX_t 236,3,57344
LX_aa 236,248,172
LX_t 238,3,61440
LX_aa 236,252,188
LX_w 4
LX_aa 240,224,192
LX_aa 240,228,208
LX_aa 244,224,196
v_add_u32 v2, 65536, v2
LX_aa 244,228,212
LX_t 240,2,0
LX_aa 248,224,200
LX_t 242,2,4096
LX_aa 248,228,216
LX_t 244,2,8192
LX_aa 252,224,204
LX_t 246,2,12288
LX_aa 252,228,220
LX_w 4
LX_aa 224,232,192
LX_aa 224,236,208
LX_aa 228,232,196
v_add_u32 v3, 65536, v3
LX_aa 228,236,212
LX_t 248,3,0
LX_aa 232,232,200
LX_t 250,3,4096
LX_aa 232,236,216
LX_t 252,3,8192
LX_aa 236,232,204
LX_t 254,3,12288
LX_aa 236,236,220
LX_w 4
LX_av 240,240,96
LX_av 240,244,128
LX_av 244,240,100
LX_av 244,244,132
LX_av 248,240,104
LX_av 248,244,136
LX_av 252,240,108
LX_av 252,244,140
LX_w 0
LX_av 224,248,96
LX_av 224,252,128
LX_av 228,248,100
LX_av 228,252,132
LX_av 232,248,104
LX_av 232,252,136
LX_av 236,248,108
LX_av 236,252,140
s_waitcnt vmcnt(28)
ds_read_b64 v[32:33], v12 offset:0
ds_read_b64 v[34:35], v12 offset:32
ds_read_b64 v[36:37], v12 offset:1024
ds_read_b64 v[38:39], v12 offset:1056
ds_read_b64 v[40:41], v12 offset:2048
ds_read_b64 v[42:43], v12 offset:2080
ds_read_b64 v[44:45], v12 offset:3072
ds_read_b64 v[46:47], v12 offset:3104
LX_vv 240,68,64
LX_vv 224,72,64
LX_vv 244,68,64
LX_vv 228,72,64
LX_vv 248,68,64
LX_vv 232,72,64
LX_vv 252,68,64
LX_vv 236,72,64
LX_w 0
LX_vv 240,68,32
LX_vv 224,72,32
LX_vv 244,68,36
LX_vv 228,72,36
LX_vv 248,68,40
LX_vv 232,72,40
LX_vv 252,68,44
LX_vv 236,72,44
s_mov_b32 s53, 0
s_bitcmp1_b64 s[36:37], 0
s_cbranch_scc0 LX_c12w
s_waitcnt vmcnt(8)
LX_c12a0:
LX_vv 240,68,48
LX_vv 244,72,48
LX_vv 224,68,56
LX_vv 228,72,56
LX_vv 248,68,52
LX_vv 252,72,52
LX_vv 232,68,60
LX_vv 236,72,60
s_mov_b32 s53, 1
s_bitcmp1_b64 s[36:37], 16
s_cbranch_scc0 LX_c12w
s_waitcnt vmcnt(4)
LX_c12a1:
LX_vv 240,68,80
LX_vv 244,72,80
LX_vv 224,68,88
LX_vv 228,72,88
LX_vv 248,68,84
LX_vv 252,72,84
LX_vv 232,68,92
LX_vv 236,72,92
s_mov_b32 s53, 2
s_bitcmp1_b64 s[36:37], 32
s_cbranch_scc0 LX_c12w
s_waitcnt vmcnt(0)
LX_c12a2:
LX_vv 240,68,112
LX_vv 244,72,112
LX_vv 224,68,120
LX_vv 228,72,120
LX_vv 248,68,116
LX_vv 252,72,116
LX_vv 232,68,124
LX_vv 236,72,124
s_branch LX_c12d
LX_c12w:
s_mov_b32 s40, 262144
LX_c12:
buffer_load_dwordx2 v[18:19], v20, s[20:23], s52 offen sc1
s_waitcnt vmcnt(0)
v_cmp_eq_u64 vcc, s[56:57], v[18:19]
s_sub_u32 s40, s40, 1
s_cbranch_scc1 LX_c12x
s_andn2_b64 s[46:47], vcc, s[36:37]
s_cbranch_scc0 LX_c12
s_or_b64 s[36:37], s[36:37], vcc
s_bitcmp1_b64 s[46:47], 0
s_cbranch_scc0 LX_c12n0
buffer_load_dwordx4 v[48:51], v1, s[20:23], s51 offen offset:0 sc1
buffer_load_dwordx4 v[52:55], v1, s[20:23], s51 offen offset:1024 sc1
buffer_load_dwordx4 v[56:59], v1, s[20:23], s51 offen offset:2048 sc1
buffer_load_dwordx4 v[60:63], v1, s[20:23], s51 offen offset:3072 sc1
LX_c12n0:
s_bitcmp1_b64 s[46:47], 16
s_cbranch_scc0 LX_c12n1
buffer_load_dwordx4 v[80:83], v23, s[20:23], s51 offen offset:0 sc1
buffer_load_dwordx4 v[84:87], v23, s[20:23], s51 offen offset:1024 sc1
buffer_load_dwordx4 v[88:91], v23, s[20:23], s51 offen offset:2048 sc1
buffer_load_dwordx4 v[92:95], v23, s[20:23], s51 offen offset:3072 sc1
LX_c12n1:
s_bitcmp1_b64 s[46:47], 32
s_cbranch_scc0 LX_c12n2
buffer_load_dwordx4 v[112:115], v1, s[20:23], s50 offen offset:0 sc1
buffer_load_dwordx4 v[116:119], v1, s[20:23], s50 offen offset:1024 sc1
buffer_load_dwordx4 v[120:123], v1, s[20:23], s50 offen offset:2048 sc1
buffer_load_dwordx4 v[124:127], v1, s[20:23], s50 offen offset:3072 sc1
LX_c12n2:
s_cmp_eq_u64 s[36:37], -1
s_cbranch_scc0 LX_c12
LX_c12x:
s_mov_b64 s[36:37], -1
s_cmp_eq_u32 s53, 2
s_waitcnt vmcnt(0)
s_cbranch_scc1 LX_c12a2
s_cmp_eq_u32 s53, 1
s_cbranch_scc1 LX_c12a1
s_branch LX_c12a0
LX_c12d:
s_nop 6
LX_c 240,240,241
LX_c 241,242,243
LX_c 242,224,225
LX_c 243,226,227
ds_write_b64 v12, v[240:241] offset:0
ds_write_b64 v12, v[242:243] offset:32
ds_read_b128 v[32:35], v13 offset:0
LX_c 244,244,245
LX_c 245,246,247
LX_c 246,228,229
LX_c 247,230,231
ds_write_b64 v12, v[244:245] offset:1024
ds_write_b64 v12, v[246:247] offset:1056
ds_read_b128 v[36:39], v13 offset:1024
LX_c 248,248,249
LX_c 249,250,251
LX_c 250,232,233
LX_c 251,234,235
ds_write_b64 v12, v[248:249] offset:2048
ds_write_b64 v12, v[250:251] offset:2080
ds_read_b128 v[40:43], v13 offset:2048
LX_c 252,252,253
LX_c 253,254,255
LX_c 254,236,237
LX_c 255,238,239
ds_write_b64 v12, v[252:253] offset:3072
ds_write_b64 v12, v[254:255] offset:3104
ds_read_b128 v[44:47], v13 offset:3072
LX_w 9
buffer_store_dwordx4 v[32:35], v15, s[16:19], s54 offen offset:0 sc1
LX_w 6
buffer_store_dwordx4 v[36:39], v15, s[16:19], s54 offen offset:2048 sc1
LX_w 3
buffer_store_dwordx4 v[40:43], v15, s[16:19], s55 offen offset:0 sc1
LX_w 0
buffer_store_dwordx4 v[44:47], v15, s[16:19], s55 offen offset:2048 sc1
s_endpgm
LX_c2:
s_mov_b32 s40, 262144
LX_3:
buffer_load_dwordx2 v[18:19], v24, s[20:23], 0 offen sc1
s_waitcnt vmcnt(0)
v_cmp_ne_u64 vcc, s[56:57], v[18:19]
s_sub_u32 s40, s40, 1
s_cbranch_scc1 LX_3d
s_nop 2
s_cbranch_vccnz LX_3
LX_3d:
s_branch LX_c2r
LX_c4:
s_mov_b32 s40, 262144
LX_5:
buffer_load_dwordx2 v[18:19], v25, s[20:23], s80 offen sc1
s_waitcnt vmcnt(0)
v_cmp_ne_u64 vcc, s[56:57], v[18:19]
s_sub_u32 s40, s40, 1
s_cbranch_scc1 LX_5d
s_nop 2
s_cbranch_vccnz LX_5
LX_5d:
s_branch LX_c4r
LX_c6:
s_mov_b32 s40, 262144
LX_7:
buffer_load_dwordx2 v[18:19], v22, s[20:23], s80 offen sc1
s_waitcnt vmcnt(0)
v_cmp_ne_u64 vcc, s[56:57], v[18:19]
s_sub_u32 s40, s40, 1
s_cbranch_scc1 LX_7d
s_nop 2
s_cbranch_vccnz LX_7
LX_7d:
s_branch LX_c6r
LX_c8:
s_mov_b32 s40, 262144
LX_9:
buffer_load_dwordx2 v[18:19], v31, s[20:23], s80 offen sc1
s_waitcnt vmcnt(0)
v_cmp_ne_u64 vcc, s[56:57], v[18:19]
s_sub_u32 s40, s40, 1
s_cbranch_scc1 LX_9d
s_nop 2
s_cbranch_vccnz LX_9
LX_9d:
s_branch LX_c8r
LX_c10:
s_mov_b32 s40, 262144
LX_11:
buffer_load_dwordx2 v[18:19], v24, s[20:23], s80 offen sc1
s_waitcnt vmcnt(0)
v_cmp_ne_u64 vcc, s[56:57], v[18:19]
s_sub_u32 s40, s40, 1
s_cbranch_scc1 LX_11d
s_nop 2
s_cbranch_vccnz LX_11
LX_11d:
s_branch LX_c10r
LX_Q0_end:
.fill (LX_Q0_begin+61440-LX_Q0_end)/4, 4, 0xbf800000
LX_F0_begin:
s_load_dwordx2 s[16:17], %0, 24
s_load_dwordx2 s[28:29], %0, 32
s_load_dwordx2 s[84:85], %0, 40
s_load_dwordx2 s[58:59], %0, 48
s_load_dwordx2 s[96:97], %0, 56
s_load_dwordx2 s[98:99], %0, 64
v_lshrrev_b32 v25, 6, %4
v_lshlrev_b32 v1, 4, v24
v_readfirstlane_b32 s61, v25
s_lshl_b32 s55, %2, 2
s_add_u32 s55, s55, %3
s_and_b32 s51, s55, 1
s_mul_i32 s50, s55, 448
s_lshl_b32 s54, s51, 6
s_add_u32 s43, s50, s54
s_xor_b32 s51, s51, 1
s_mul_i32 s51, s51, 384
s_add_u32 s44, s50, s51
s_lshl_b32 s45, s61, 10
s_lshl_b32 s39, s61, 11
s_lshl_b32 s42, s61, 4
v_lshrrev_b32 v25, 3, v24
v_add_u32 v25, s42, v25
v_mul_u32_u24 v25, 7168, v25
v_lshrrev_b32 v26, 4, v24
v_lshlrev_b32 v26, 1, v26
v_and_b32 v27, 7, v24
v_xor_b32 v26, v26, v27
v_lshlrev_b32 v26, 4, v26
v_add3_u32 v2, v25, v26, s43
v_add_u32 v3, 57344, v2
v_add_u32 v4, 128, v2
v_add_u32 v5, 57472, v2
v_add_u32 v6, 256, v2
v_add_u32 v7, 57600, v2
v_lshrrev_b32 v25, 2, v24
v_add_u32 v25, s42, v25
v_mul_u32_u24 v25, 7168, v25
v_bfe_u32 v26, v24, 4, 1
v_lshlrev_b32 v26, 1, v26
v_and_b32 v27, 3, v24
v_xor_b32 v26, v26, v27
v_lshlrev_b32 v26, 4, v26
v_add3_u32 v8, v25, v26, s44
s_and_b32 s55, s55, 7
s_lshl_b32 s54, %1, 3
s_add_u32 s55, s54, s55
s_lshl_b32 s55, s55, 2
s_add_u32 s55, s55, s61
s_lshl_b32 s33, s55, 11
)ASM"
R"ASM(s_cmp_lt_u32 %2, 2
s_cselect_b32 s54, 1024, 0
s_add_u32 s33, s33, s54
s_cmp_lt_u32 s55, 224
s_cselect_b32 s55, s33, 1073737728
v_lshrrev_b32 v25, 3, v24
v_add_u32 v25, 1, v25
v_mul_u32_u24 v25, 458752, v25
v_and_b32 v28, 7, v24
v_lshlrev_b32 v28, 7, v28
v_cmp_gt_u32 vcc, 16, v24
v_add3_u32 v28, v28, v25, s55
v_mov_b32 v25, 1073737728
v_cndmask_b32 v28, v25, v28, vcc
LX_w 0
s_and_b32 s17, s17, 0xffff
s_mov_b32 s18, 7340032
s_mov_b32 s19, 0x20000
s_mov_b32 s35, 0
s_add_u32 m0, s39, 0
s_nop 0
buffer_load_dwordx4 v2, s[16:19], s35 offen  lds
s_add_u32 m0, s39, 1024
s_nop 0
buffer_load_dwordx4 v3, s[16:19], s35 offen  lds
s_add_u32 m0, s39, 32768
s_nop 0
buffer_load_dwordx4 v4, s[16:19], s35 offen  lds
s_add_u32 m0, s39, 33792
s_nop 0
buffer_load_dwordx4 v5, s[16:19], s35 offen  lds
s_add_u32 m0, s39, 65536
s_nop 0
buffer_load_dwordx4 v6, s[16:19], s35 offen  lds
s_add_u32 m0, s39, 66560
s_nop 0
buffer_load_dwordx4 v7, s[16:19], s35 offen  lds
s_add_u32 m0, s45, 98304
s_nop 0
buffer_load_dwordx4 v8, s[16:19], s35 offen  lds
buffer_load_dword a223, v28, s[16:19], 0 offen
s_and_b32 s29, s29, 0xffff
s_mov_b32 s30, 7168
s_mov_b32 s31, 0x20000
v_and_b32 v25, 15, v24
v_lshrrev_b32 v28, 4, v24
v_lshlrev_b32 v28, 3, v28
v_lshl_add_u32 v28, v25, 5, v28
s_sub_u32 s50, s44, 384
v_mov_b32 v26, s50
v_mov_b32 v27, s43
v_cmp_gt_u32 vcc, 12, v25
s_nop 1
v_cndmask_b32 v26, v26, v27, vcc
v_add_u32 v28, v28, v26
buffer_load_dwordx2 v[96:97], v28, s[28:31], 0 offen
s_mov_b32 s47, 0xffff0000
s_lshl_b32 s52, %3, 1
s_add_u32 s52, s52, 1
s_lshl_b32 s53, %1, 2
s_add_u32 s53, s53, %2
s_lshl_b32 s50, s53, 12
s_lshl_b32 s51, s61, 10
s_add_u32 s50, s50, s51
s_lshl_b32 s51, s52, 7
s_add_u32 s50, s50, s51
s_add_u32 s50, s50, 35782656
v_mov_b32 v18, 1073737728
v_writelane_b32 v18, s50, 0
s_lshl_b32 s50, %1, 11
s_lshl_b32 s51, s61, 8
s_add_u32 s50, s50, s51
s_lshl_b32 s51, %3, 7
s_add_u32 s50, s50, s51
s_add_u32 s50, s50, 35686144
s_lshl_b32 s51, %2, 2
s_add_u32 s51, s51, %3
s_cmp_ge_u32 s51, 14
s_cselect_b32 s50, s50, 1073737728
v_writelane_b32 v18, s50, 1
buffer_load_dword a223, v18, s[20:23], 0 offen sc1
v_and_b32 v25, 7, v24
v_lshrrev_b32 v27, 5, v24
v_bfe_u32 v26, v24, 3, 2
v_lshlrev_b32 v19, 12, v27
v_lshl_add_u32 v19, v26, 8, v19
v_lshl_add_u32 v19, v25, 4, v19
s_mul_i32 s49, s53, 1114112
s_lshl_b32 s50, s61, 15
s_add_u32 s49, s49, s50
s_lshl_b32 s50, %3, 10
s_add_u32 s49, s49, s50
s_add_u32 s49, s49, 128
v_add_u32 v19, s49, v19
s_mul_i32 s50, s53, 37
s_lshr_b32 s50, s50, 8
s_mul_i32 s50, s50, 1113216
s_mul_i32 s54, s53, 1113984
s_sub_u32 s50, s50, s54
s_add_u32 s50, s50, 36700288
v_cmp_eq_u32 vcc, 1, v26
v_add_u32 v25, s50, v19
s_nop 0
v_cndmask_b32 v19, v19, v25, vcc
v_bfe_u32 v25, v24, 2, 3
v_lshlrev_b32 v86, 9, v25
v_and_b32 v26, 1, v24
v_lshl_add_u32 v86, v26, 8, v86
v_bfe_u32 v27, v24, 1, 1
v_lshl_add_u32 v86, v27, 3, v86
v_and_b32 v25, 3, v25
v_lshl_add_u32 v25, v25, 1, v26
v_lshrrev_b32 v27, 5, v24
s_lshl_b32 s50, s61, 1
v_add_u32 v26, s50, v27
v_xor_b32 v25, v25, v26
v_lshl_add_u32 v86, v25, 4, v86
v_add_u32 v86, 114688, v86
v_bfe_u32 v25, v24, 4, 2
v_xor_b32 v25, v25, v24
v_lshlrev_b32 v87, 4, v25
s_lshl_b32 s50, s61, 12
s_add_u32 s50, s50, 114688
v_add_u32 v87, s50, v87
v_xor_b32 v88, 64, v87
s_waitcnt vmcnt(11)
v_lshlrev_b32 v48, 16, v32
v_and_b32 v49, s47, v32
v_lshlrev_b32 v50, 16, v33
v_and_b32 v51, s47, v33
v_lshlrev_b32 v52, 16, v34
v_and_b32 v53, s47, v34
v_lshlrev_b32 v54, 16, v35
v_and_b32 v55, s47, v35
v_lshlrev_b32 v56, 16, v36
v_and_b32 v57, s47, v36
v_lshlrev_b32 v58, 16, v37
v_and_b32 v59, s47, v37
v_lshlrev_b32 v60, 16, v38
v_and_b32 v61, s47, v38
v_lshlrev_b32 v62, 16, v39
v_and_b32 v63, s47, v39
v_lshlrev_b32 v64, 16, v40
v_and_b32 v65, s47, v40
v_lshlrev_b32 v66, 16, v41
v_and_b32 v67, s47, v41
v_lshlrev_b32 v68, 16, v42
v_and_b32 v69, s47, v42
v_lshlrev_b32 v70, 16, v43
v_and_b32 v71, s47, v43
v_lshlrev_b32 v72, 16, v44
v_and_b32 v73, s47, v44
v_lshlrev_b32 v74, 16, v45
v_and_b32 v75, s47, v45
v_lshlrev_b32 v76, 16, v46
v_and_b32 v77, s47, v46
v_lshlrev_b32 v78, 16, v47
v_and_b32 v79, s47, v47
v_pk_add_f32 v[80:81], v[48:49], v[50:51]
v_pk_add_f32 v[82:83], v[52:53], v[54:55]
v_pk_add_f32 v[80:81], v[80:81], v[56:57]
v_pk_add_f32 v[82:83], v[82:83], v[58:59]
v_pk_add_f32 v[80:81], v[80:81], v[60:61]
v_pk_add_f32 v[82:83], v[82:83], v[62:63]
v_pk_add_f32 v[80:81], v[80:81], v[64:65]
v_pk_add_f32 v[82:83], v[82:83], v[66:67]
v_pk_add_f32 v[80:81], v[80:81], v[68:69]
v_pk_add_f32 v[82:83], v[82:83], v[70:71]
v_pk_add_f32 v[80:81], v[80:81], v[72:73]
v_pk_add_f32 v[82:83], v[82:83], v[74:75]
v_pk_add_f32 v[80:81], v[80:81], v[76:77]
v_pk_add_f32 v[82:83], v[82:83], v[78:79]
v_pk_add_f32 v[80:81], v[80:81], v[82:83]
v_add_f32 v80, v80, v81
s_nop 1
v_add_f32_dpp v80, v80, v80 quad_perm:[1,0,3,2] row_mask:0xf bank_mask:0xf
s_nop 1
v_add_f32_dpp v80, v80, v80 quad_perm:[2,3,0,1] row_mask:0xf bank_mask:0xf
s_nop 1
v_add_f32_dpp v80, v80, v80 row_half_mirror row_mask:0xf bank_mask:0xf
s_nop 1
v_add_f32_dpp v80, v80, v80 row_mirror row_mask:0xf bank_mask:0xf
v_mov_b32 v81, v80
s_nop 1
v_permlane16_swap_b32 v80, v81
v_add_f32 v80, v80, v81
v_mul_f32 v84, 0xba800000, v80
v_mul_f32 v85, 0xba800000, v80
v_pk_add_f32 v[48:49], v[48:49], v[84:85]
v_pk_add_f32 v[50:51], v[50:51], v[84:85]
v_pk_add_f32 v[52:53], v[52:53], v[84:85]
v_pk_add_f32 v[54:55], v[54:55], v[84:85]
v_pk_add_f32 v[56:57], v[56:57], v[84:85]
v_pk_add_f32 v[58:59], v[58:59], v[84:85]
v_pk_add_f32 v[60:61], v[60:61], v[84:85]
v_pk_add_f32 v[62:63], v[62:63], v[84:85]
v_pk_add_f32 v[64:65], v[64:65], v[84:85]
v_pk_add_f32 v[66:67], v[66:67], v[84:85]
v_pk_add_f32 v[68:69], v[68:69], v[84:85]
v_pk_add_f32 v[70:71], v[70:71], v[84:85]
v_pk_add_f32 v[72:73], v[72:73], v[84:85]
v_pk_add_f32 v[74:75], v[74:75], v[84:85]
v_pk_add_f32 v[76:77], v[76:77], v[84:85]
v_pk_add_f32 v[78:79], v[78:79], v[84:85]
v_pk_mul_f32 v[80:81], v[48:49], v[48:49]
v_pk_mul_f32 v[82:83], v[50:51], v[50:51]
v_pk_fma_f32 v[80:81], v[52:53], v[52:53], v[80:81]
v_pk_fma_f32 v[82:83], v[54:55], v[54:55], v[82:83]
v_pk_fma_f32 v[80:81], v[56:57], v[56:57], v[80:81]
v_pk_fma_f32 v[82:83], v[58:59], v[58:59], v[82:83]
v_pk_fma_f32 v[80:81], v[60:61], v[60:61], v[80:81]
v_pk_fma_f32 v[82:83], v[62:63], v[62:63], v[82:83]
v_pk_fma_f32 v[80:81], v[64:65], v[64:65], v[80:81]
v_pk_fma_f32 v[82:83], v[66:67], v[66:67], v[82:83]
v_pk_fma_f32 v[80:81], v[68:69], v[68:69], v[80:81]
v_pk_fma_f32 v[82:83], v[70:71], v[70:71], v[82:83]
v_pk_fma_f32 v[80:81], v[72:73], v[72:73], v[80:81]
v_pk_fma_f32 v[82:83], v[74:75], v[74:75], v[82:83]
v_pk_fma_f32 v[80:81], v[76:77], v[76:77], v[80:81]
v_pk_fma_f32 v[82:83], v[78:79], v[78:79], v[82:83]
v_pk_add_f32 v[80:81], v[80:81], v[82:83]
v_add_f32 v82, v80, v81
LX_c 85,85,85
v_and_b32 v85, s47, v85
v_add_f32_dpp v82, v82, v82 quad_perm:[1,0,3,2] row_mask:0xf bank_mask:0xf
s_nop 1
v_add_f32_dpp v82, v82, v82 quad_perm:[2,3,0,1] row_mask:0xf bank_mask:0xf
s_nop 1
v_add_f32_dpp v82, v82, v82 row_half_mirror row_mask:0xf bank_mask:0xf
s_nop 1
v_add_f32_dpp v82, v82, v82 row_mirror row_mask:0xf bank_mask:0xf
v_mov_b32 v81, v82
s_nop 1
v_permlane16_swap_b32 v82, v81
v_add_f32 v82, v82, v81
v_mul_f32 v82, 0x3a800000, v82
LX_c 82,82,82
v_and_b32 v82, s47, v82
v_add_f32 v82, 0x3727c5ac, v82
LX_c 82,82,82
v_and_b32 v82, s47, v82
v_sqrt_f32 v82, v82
v_sub_f32 v80, v85, v84
LX_c 82,82,82
v_and_b32 v82, s47, v82
v_rcp_f32 v82, v82
s_nop 0
v_mul_f32 v80, v80, v82
v_pk_fma_f32 v[48:49], v[48:49], v[82:83], v[80:81] op_sel_hi:[1,0,0]
v_pk_fma_f32 v[50:51], v[50:51], v[82:83], v[80:81] op_sel_hi:[1,0,0]
v_pk_fma_f32 v[52:53], v[52:53], v[82:83], v[80:81] op_sel_hi:[1,0,0]
v_pk_fma_f32 v[54:55], v[54:55], v[82:83], v[80:81] op_sel_hi:[1,0,0]
LX_c 32,48,49
LX_c 33,50,51
LX_c 34,52,53
LX_c 35,54,55
ds_write_b64 v86, v[32:33] offset:0
ds_write_b64 v86, v[34:35] offset:128
v_pk_fma_f32 v[56:57], v[56:57], v[82:83], v[80:81] op_sel_hi:[1,0,0]
v_pk_fma_f32 v[58:59], v[58:59], v[82:83], v[80:81] op_sel_hi:[1,0,0]
v_pk_fma_f32 v[60:61], v[60:61], v[82:83], v[80:81] op_sel_hi:[1,0,0]
v_pk_fma_f32 v[62:63], v[62:63], v[82:83], v[80:81] op_sel_hi:[1,0,0]
LX_c 36,56,57
LX_c 37,58,59
LX_c 38,60,61
LX_c 39,62,63
ds_write_b64 v86, v[36:37] offset:4096
ds_write_b64 v86, v[38:39] offset:4224
v_pk_fma_f32 v[64:65], v[64:65], v[82:83], v[80:81] op_sel_hi:[1,0,0]
v_pk_fma_f32 v[66:67], v[66:67], v[82:83], v[80:81] op_sel_hi:[1,0,0]
v_pk_fma_f32 v[68:69], v[68:69], v[82:83], v[80:81] op_sel_hi:[1,0,0]
v_pk_fma_f32 v[70:71], v[70:71], v[82:83], v[80:81] op_sel_hi:[1,0,0]
LX_c 40,64,65
LX_c 41,66,67
LX_c 42,68,69
LX_c 43,70,71
ds_write_b64 v86, v[40:41] offset:8192
ds_write_b64 v86, v[42:43] offset:8320
v_pk_fma_f32 v[72:73], v[72:73], v[82:83], v[80:81] op_sel_hi:[1,0,0]
v_pk_fma_f32 v[74:75], v[74:75], v[82:83], v[80:81] op_sel_hi:[1,0,0]
v_pk_fma_f32 v[76:77], v[76:77], v[82:83], v[80:81] op_sel_hi:[1,0,0]
v_pk_fma_f32 v[78:79], v[78:79], v[82:83], v[80:81] op_sel_hi:[1,0,0]
LX_c 44,72,73
LX_c 45,74,75
LX_c 46,76,77
LX_c 47,78,79
ds_write_b64 v86, v[44:45] offset:12288
ds_write_b64 v86, v[46:47] offset:12416
v_add_co_u32 v26, vcc, 1, v98
s_nop 1
v_addc_co_u32 v27, vcc, 0, v99, vcc
s_mov_b32 s36, 8192
s_mov_b32 s37, 16384
s_mov_b32 s49, 24576
LX_w 0
s_barrier
ds_read_b128 a[200:203], v87 offset:0
ds_read_b128 a[204:207], v88 offset:1024
ds_read_b128 a[208:211], v87 offset:2048
ds_read_b128 a[212:215], v88 offset:3072
v_readlane_b32 s56, v26, 0
v_readlane_b32 s57, v27, 0
s_cmp_ge_u32 s61, 1
s_cbranch_scc1 LX_F0_nost
LX_w 3
buffer_store_dwordx4 a[200:203], v19, s[20:23], 0 offen sc1
LX_w 2
buffer_store_dwordx4 a[204:207], v19, s[20:23], s36 offen sc1
LX_w 1
buffer_store_dwordx4 a[208:211], v19, s[20:23], s37 offen sc1
LX_w 0
buffer_store_dwordx4 a[212:215], v19, s[20:23], s49 offen sc1
v_mov_b32 v19, 1073737728
LX_F0_nost:
v_lshrrev_b32 v25, 2, v24
v_lshlrev_b32 v25, 7, v25
v_and_b32 v94, 3, v24
v_lshlrev_b32 v94, 3, v94
v_bfe_u32 v95, v24, 3, 2
v_lshlrev_b32 v95, 5, v95
v_add3_u32 v9, v25, v94, v95
v_xor_b32 v10, 32, v9
v_xor_b32 v11, 64, v9
v_xor_b32 v12, 96, v9
v_add_u32 v13, 65536, v9
v_add_u32 v14, 65536, v10
v_add_u32 v15, 65536, v11
v_add_u32 v16, 65536, v12
v_lshrrev_b32 v25, 2, v24
v_lshlrev_b32 v25, 6, v25
v_and_b32 v94, 3, v24
v_lshlrev_b32 v94, 3, v94
v_add_u32 v25, v25, v94
v_bfe_u32 v94, v24, 4, 1
v_lshlrev_b32 v94, 5, v94
v_add_u32 v25, v25, v94
v_add_u32 v17, 98304, v25
v_xor_b32 v20, 32, v17
s_lshl_b32 s41, %1, 2
s_add_u32 s41, s41, s61
s_mul_i32 s38, s41, 1114112
s_mul_i32 s50, s41, 37
s_lshr_b32 s50, s50, 8
s_mul_i32 s50, s50, 1113216
s_mul_i32 s54, s41, 1113984
s_sub_u32 s50, s50, s54
s_add_u32 s50, s50, 36700288
v_lshrrev_b32 v25, 3, v24
v_cmp_eq_u32 vcc, 3, v25
v_add_u32 v25, s50, v1
s_nop 0
v_cndmask_b32 v1, v1, v25, vcc
v_add_u32 v23, 4096, v1
v_lshrrev_b32 v25, 3, v24
v_add_u32 v25, 4, v25
v_mul_u32_u24 v25, 458752, v25
v_and_b32 v21, 7, v24
v_lshlrev_b32 v21, 7, v21
v_cmp_gt_u32 vcc, 8, v24
v_add3_u32 v21, v21, v25, s55
v_mov_b32 v25, 1073737728
v_cndmask_b32 v21, v25, v21, vcc
s_add_u32 s51, s33, 393216
v_lshlrev_b32 v22, 7, v24
v_add_u32 v22, s51, v22
v_cmp_gt_u32 vcc, 8, v24
s_nop 1
v_cndmask_b32 v22, v25, v22, vcc
v_cmp_eq_u32 vcc, 0, v24
s_lshl_b32 s51, %3, 9
s_add_u32 s51, s51, 256
v_cndmask_b32 v30, v25, 0, vcc
v_lshrrev_b32 v25, 3, v24
v_add_u32 v25, s42, v25
v_mul_u32_u24 v25, 2048, v25
s_cmp_lt_u32 s61, 1
s_cbranch_scc1 LX_F0_nob
s_mov_b32 s35, 458752
s_add_u32 m0, s39, 8192
s_nop 0
buffer_load_dwordx4 v2, s[16:19], s35 offen  lds
s_add_u32 m0, s39, 9216
s_nop 0
buffer_load_dwordx4 v3, s[16:19], s35 offen  lds
s_add_u32 m0, s39, 40960
s_nop 0
buffer_load_dwordx4 v4, s[16:19], s35 offen  lds
s_add_u32 m0, s39, 41984
s_nop 0
buffer_load_dwordx4 v5, s[16:19], s35 offen  lds
s_add_u32 m0, s39, 73728
s_nop 0
buffer_load_dwordx4 v6, s[16:19], s35 offen  lds
s_add_u32 m0, s39, 74752
s_nop 0
buffer_load_dwordx4 v7, s[16:19], s35 offen  lds
s_add_u32 m0, s45, 102400
s_nop 0
buffer_load_dwordx4 v8, s[16:19], s35 offen  lds
s_mov_b32 s35, 917504
)ASM"
R"ASM(s_add_u32 m0, s39, 16384
s_nop 0
buffer_load_dwordx4 v2, s[16:19], s35 offen  lds
s_add_u32 m0, s39, 17408
s_nop 0
buffer_load_dwordx4 v3, s[16:19], s35 offen  lds
s_add_u32 m0, s39, 49152
s_nop 0
buffer_load_dwordx4 v4, s[16:19], s35 offen  lds
s_add_u32 m0, s39, 50176
s_nop 0
buffer_load_dwordx4 v5, s[16:19], s35 offen  lds
s_add_u32 m0, s39, 81920
s_nop 0
buffer_load_dwordx4 v6, s[16:19], s35 offen  lds
s_add_u32 m0, s39, 82944
s_nop 0
buffer_load_dwordx4 v7, s[16:19], s35 offen  lds
s_add_u32 m0, s45, 106496
s_nop 0
buffer_load_dwordx4 v8, s[16:19], s35 offen  lds
LX_F0_nob:
v_lshrrev_b32 v94, 4, v24
v_lshlrev_b32 v94, 1, v94
v_and_b32 v95, 7, v24
v_xor_b32 v94, v94, v95
v_lshlrev_b32 v94, 4, v94
v_add3_u32 v28, v25, v94, s51
v_add_u32 v29, 16384, v28
s_mov_b64 s[28:29], s[58:59]
s_and_b32 s29, s29, 0xffff
s_mov_b32 s30, 7340032
s_mov_b32 s31, 0x20000
s_and_b32 s85, s85, 0xffff
s_mov_b32 s86, 2097152
s_mov_b32 s87, 0x20000
s_lshl_b32 s48, s61, 12
s_mul_i32 s60, %2, 1835008
s_and_b32 s54, %1, 0
s_xor_b32 s54, s54, %2
s_lshl_b32 s46, s54, 19
s_mul_i32 s51, %2, 147456
s_add_u32 s34, s38, s51
s_add_u32 s34, s34, 131072
s_mov_b32 s51, %3
s_mul_i32 s51, s51, 7
s_add_u32 s51, s51, 1
s_lshr_b32 s51, s51, 1
s_lshl_b32 s54, s51, 17
s_add_u32 s92, s54, s60
s_add_u32 s51, s51, 4
s_lshl_b32 s51, s51, 13
s_add_u32 s88, s51, s34
s_cmp_le_u32 %3, 0
s_cselect_b32 s51, 1, 0
s_mul_i32 s51, s51, 7
s_add_u32 s51, s51, 1
s_lshr_b32 s51, s51, 1
s_lshl_b32 s54, s51, 17
s_add_u32 s93, s54, s60
s_add_u32 s51, s51, 4
s_lshl_b32 s51, s51, 13
s_add_u32 s89, s51, s34
s_cmp_le_u32 %3, 1
s_cselect_b32 s51, 2, 1
s_mul_i32 s51, s51, 7
s_add_u32 s51, s51, 1
s_lshr_b32 s51, s51, 1
s_lshl_b32 s54, s51, 17
s_add_u32 s94, s54, s60
s_add_u32 s51, s51, 4
s_lshl_b32 s51, s51, 13
s_add_u32 s90, s51, s34
s_cmp_le_u32 %3, 2
s_cselect_b32 s51, 3, 2
s_mul_i32 s51, s51, 7
s_add_u32 s51, s51, 1
s_lshr_b32 s51, s51, 1
s_lshl_b32 s54, s51, 17
s_add_u32 s95, s54, s60
s_add_u32 s51, s51, 4
s_lshl_b32 s51, s51, 13
s_add_u32 s91, s51, s34
s_lshl_b32 s50, s53, 2
s_add_u32 s50, s50, s61
s_lshl_b32 s50, s50, 11
s_add_u32 s80, s50, 35913728
s_lshl_b32 s50, %3, 7
s_add_u32 s82, s50, s80
s_add_u32 s83, s50, s82
s_lshl_b32 s50, %1, 3
s_add_u32 s50, s50, s52
s_lshl_b32 s50, s50, 2
s_add_u32 s50, s50, %2
s_lshl_b32 s50, s50, 2
s_add_u32 s81, s50, s61
s_lshl_b32 s50, s52, 8
s_add_u32 s50, s50, s80
s_lshl_b32 s51, s81, 9
s_add_u32 s51, s51, 36175616
v_mov_b32 v94, s50
v_mov_b32 v95, s51
v_cmp_gt_u32 vcc, 2, v24
v_min_u32 v31, 4, v24
s_nop 0
v_cndmask_b32 v94, v95, v94, vcc
v_lshl_add_u32 v31, v31, 7, v94
s_cmp_ge_u32 s61, 1
s_cbranch_scc1 LX_F0_nofl
s_waitcnt vmcnt(0)
buffer_store_dwordx2 v[26:27], v18, s[20:23], 0 offen sc1
v_mov_b32 v18, 1073737728
s_mov_b32 s35, 458752
s_add_u32 m0, s39, 8192
s_nop 0
buffer_load_dwordx4 v2, s[16:19], s35 offen  lds
s_add_u32 m0, s39, 9216
s_nop 0
buffer_load_dwordx4 v3, s[16:19], s35 offen  lds
s_add_u32 m0, s39, 40960
s_nop 0
buffer_load_dwordx4 v4, s[16:19], s35 offen  lds
s_add_u32 m0, s39, 41984
s_nop 0
buffer_load_dwordx4 v5, s[16:19], s35 offen  lds
s_add_u32 m0, s39, 73728
s_nop 0
buffer_load_dwordx4 v6, s[16:19], s35 offen  lds
s_add_u32 m0, s39, 74752
s_nop 0
buffer_load_dwordx4 v7, s[16:19], s35 offen  lds
s_add_u32 m0, s45, 102400
s_nop 0
buffer_load_dwordx4 v8, s[16:19], s35 offen  lds
s_mov_b32 s35, 917504
s_add_u32 m0, s39, 16384
s_nop 0
buffer_load_dwordx4 v2, s[16:19], s35 offen  lds
s_add_u32 m0, s39, 17408
s_nop 0
buffer_load_dwordx4 v3, s[16:19], s35 offen  lds
s_add_u32 m0, s39, 49152
s_nop 0
buffer_load_dwordx4 v4, s[16:19], s35 offen  lds
s_add_u32 m0, s39, 50176
s_nop 0
buffer_load_dwordx4 v5, s[16:19], s35 offen  lds
s_add_u32 m0, s39, 81920
s_nop 0
buffer_load_dwordx4 v6, s[16:19], s35 offen  lds
s_add_u32 m0, s39, 82944
s_nop 0
buffer_load_dwordx4 v7, s[16:19], s35 offen  lds
s_add_u32 m0, s45, 106496
s_nop 0
buffer_load_dwordx4 v8, s[16:19], s35 offen  lds
s_branch LX_F0_fl0
LX_F0_nofl:
s_waitcnt vmcnt(14)
LX_F0_fl0:
v_and_b32 v94, 3, v24
v_min_u32 v90, 0, v94
v_max_u32 v91, 1, v94
v_bfe_u32 v95, v24, 2, 3
v_lshlrev_b32 v95, 7, v95
s_lshl_b32 s50, s41, 12
s_add_u32 s50, s50, 35782656
v_add_u32 v95, s50, v95
v_lshl_add_u32 v92, v90, 10, v95
v_lshl_add_u32 v93, v91, 10, v95
v_mov_b32 v25, v92
v_mov_b32 v24, v93
LX_F0_fl:
v_lshlrev_b32 v252, 16, v96
v_and_b32 v253, s47, v96
v_lshlrev_b32 v254, 16, v97
v_and_b32 v255, s47, v97
v_mov_b64_dpp v[44:45], v[252:253] row_newbcast:0 row_mask:0xf bank_mask:0xf
s_nop 0
v_mov_b64_dpp v[46:47], v[254:255] row_newbcast:0 row_mask:0xf bank_mask:0xf
v_mov_b64_dpp v[60:61], v[252:253] row_newbcast:1 row_mask:0xf bank_mask:0xf
v_mov_b64_dpp v[62:63], v[254:255] row_newbcast:1 row_mask:0xf bank_mask:0xf
v_mov_b64_dpp v[76:77], v[252:253] row_newbcast:2 row_mask:0xf bank_mask:0xf
v_mov_b64_dpp v[78:79], v[254:255] row_newbcast:2 row_mask:0xf bank_mask:0xf
v_mov_b64_dpp v[92:93], v[252:253] row_newbcast:3 row_mask:0xf bank_mask:0xf
v_mov_b64_dpp v[94:95], v[254:255] row_newbcast:3 row_mask:0xf bank_mask:0xf
v_mov_b64_dpp v[108:109], v[252:253] row_newbcast:4 row_mask:0xf bank_mask:0xf
v_mov_b64_dpp v[110:111], v[254:255] row_newbcast:4 row_mask:0xf bank_mask:0xf
v_mov_b64_dpp v[124:125], v[252:253] row_newbcast:5 row_mask:0xf bank_mask:0xf
v_mov_b64_dpp v[126:127], v[254:255] row_newbcast:5 row_mask:0xf bank_mask:0xf
v_mov_b64_dpp v[140:141], v[252:253] row_newbcast:6 row_mask:0xf bank_mask:0xf
v_mov_b64_dpp v[142:143], v[254:255] row_newbcast:6 row_mask:0xf bank_mask:0xf
v_mov_b64_dpp v[156:157], v[252:253] row_newbcast:7 row_mask:0xf bank_mask:0xf
v_mov_b64_dpp v[158:159], v[254:255] row_newbcast:7 row_mask:0xf bank_mask:0xf
v_mov_b64_dpp v[172:173], v[252:253] row_newbcast:8 row_mask:0xf bank_mask:0xf
v_mov_b64_dpp v[174:175], v[254:255] row_newbcast:8 row_mask:0xf bank_mask:0xf
v_mov_b64_dpp v[188:189], v[252:253] row_newbcast:9 row_mask:0xf bank_mask:0xf
v_mov_b64_dpp v[190:191], v[254:255] row_newbcast:9 row_mask:0xf bank_mask:0xf
v_mov_b64_dpp v[204:205], v[252:253] row_newbcast:10 row_mask:0xf bank_mask:0xf
v_mov_b64_dpp v[206:207], v[254:255] row_newbcast:10 row_mask:0xf bank_mask:0xf
v_mov_b64_dpp v[220:221], v[252:253] row_newbcast:11 row_mask:0xf bank_mask:0xf
v_mov_b64_dpp v[222:223], v[254:255] row_newbcast:11 row_mask:0xf bank_mask:0xf
v_mov_b64_dpp v[236:237], v[252:253] row_newbcast:12 row_mask:0xf bank_mask:0xf
v_mov_b64_dpp v[238:239], v[254:255] row_newbcast:12 row_mask:0xf bank_mask:0xf
v_mov_b64_dpp v[252:253], v[252:253] row_newbcast:13 row_mask:0xf bank_mask:0xf
v_mov_b64_dpp v[254:255], v[254:255] row_newbcast:13 row_mask:0xf bank_mask:0xf
s_mov_b64 exec, 0
s_cmp_eq_u32 s61, 1
s_cbranch_scc1 LX_F0_pad1
s_cmp_eq_u32 s61, 2
s_cbranch_scc1 LX_F0_pad2
s_cmp_eq_u32 s61, 3
s_cbranch_scc1 LX_F0_pad3
LX_F0_padret:
s_mov_b64 exec, -1
LX_w 0
s_barrier
LX_t 224,9,0
LX_t 226,9,2048
LX_t 228,9,4096
LX_t 230,9,6144
LX_t 232,10,0
LX_t 234,10,2048
LX_t 236,10,4096
LX_t 238,10,6144
s_mov_b32 s40, 262144
LX_13:
buffer_load_dwordx2 v[14:15], v25, s[20:23], 0 offen sc1
s_waitcnt vmcnt(0)
v_cmp_ne_u64 vcc, s[56:57], v[14:15]
s_sub_u32 s40, s40, 1
s_cbranch_scc1 LX_13d
s_nop 2
s_cbranch_vccnz LX_13
LX_13d:
v_add_u32 v14, 65536, v10
v_add_u32 v15, 65536, v11
s_add_u32 s33, s38, 0
LX_l 0,1,0
LX_l 16,23,0
LX_l 4,1,1024
LX_l 20,23,1024
LX_l 8,1,2048
LX_l 24,23,2048
LX_l 12,1,3072
LX_l 28,23,3072
s_add_u32 s33, s38, 8192
LX_l 32,1,0
LX_l 36,1,1024
LX_l 40,1,2048
LX_F0_pad1:
s_cbranch_execz LX_F0_pad4
LX_l 44,1,3072
LX_l 48,23,0
LX_l 52,23,1024
LX_l 56,23,2048
LX_l 60,23,3072
s_waitcnt vmcnt(14)
LX_w 4
v_mfma_f32_16x16x32_bf16 v[32:35], a[224:227], a[0:3], v[44:47]
s_mov_b32 s53, 0
LX_aa 32,228,16
s_waitcnt vmcnt(12)
buffer_store_dwordx4 a[200:203], v19, s[20:23], s53 offen sc1
v_mfma_f32_16x16x32_bf16 v[36:39], a[224:227], a[4:7], v[44:47]
LX_aa 36,228,20
s_waitcnt vmcnt(11)
LX_t 240,11,0
v_mfma_f32_16x16x32_bf16 v[40:43], a[224:227], a[8:11], v[44:47]
LX_t 242,11,2048
LX_aa 40,228,24
s_waitcnt vmcnt(9)
LX_t 244,11,4096
LX_aa 44,224,12
s_mov_b32 s53, 8192
LX_t 246,11,6144
LX_aa 44,228,28
LX_w 4
buffer_store_dwordx4 a[204:207], v19, s[20:23], s53 offen sc1
v_mfma_f32_16x16x32_bf16 v[48:51], a[232:235], a[0:3], v[60:63]
LX_aa 48,236,16
s_mov_b32 s53, 16384
v_mfma_f32_16x16x32_bf16 v[52:55], a[232:235], a[4:7], v[60:63]
buffer_store_dwordx4 a[208:211], v19, s[20:23], s53 offen sc1
LX_aa 52,236,20
LX_t 248,12,0
v_mfma_f32_16x16x32_bf16 v[56:59], a[232:235], a[8:11], v[60:63]
LX_t 250,12,2048
LX_aa 56,236,24
LX_t 252,12,4096
LX_aa 60,232,12
LX_t 254,12,6144
LX_aa 60,236,28
LX_w 4
v_mfma_f32_16x16x32_bf16 v[64:67], a[240:243], a[0:3], v[76:79]
s_mov_b32 s53, 24576
LX_aa 64,244,16
buffer_store_dwordx4 a[212:215], v19, s[20:23], s53 offen sc1
v_mfma_f32_16x16x32_bf16 v[68:71], a[240:243], a[4:7], v[76:79]
LX_aa 68,244,20
LX_t 224,9,32768
v_mfma_f32_16x16x32_bf16 v[72:75], a[240:243], a[8:11], v[76:79]
LX_t 226,9,34816
LX_aa 72,244,24
LX_t 228,9,36864
LX_aa 76,240,12
s_add_u32 s33, s38, 16384
LX_t 230,9,38912
LX_aa 76,244,28
LX_w 4
LX_l 64,1,0
v_mfma_f32_16x16x32_bf16 v[80:83], a[248:251], a[0:3], v[92:95]
LX_aa 80,252,16
LX_l 68,1,1024
v_mfma_f32_16x16x32_bf16 v[84:87], a[248:251], a[4:7], v[92:95]
LX_aa 84,252,20
LX_t 232,10,32768
v_mfma_f32_16x16x32_bf16 v[88:91], a[248:251], a[8:11], v[92:95]
LX_t 234,10,34816
LX_aa 88,252,24
LX_t 236,10,36864
LX_aa 92,248,12
LX_t 238,10,38912
LX_aa 92,252,28
LX_w 4
v_mfma_f32_16x16x32_bf16 v[96:99], a[224:227], a[0:3], v[108:111]
LX_l 72,1,2048
LX_aa 96,228,16
v_mfma_f32_16x16x32_bf16 v[100:103], a[224:227], a[4:7], v[108:111]
LX_l 76,1,3072
LX_aa 100,228,20
LX_t 240,11,32768
v_mfma_f32_16x16x32_bf16 v[104:107], a[224:227], a[8:11], v[108:111]
LX_t 242,11,34816
LX_aa 104,228,24
LX_t 244,11,36864
LX_aa 108,224,12
LX_t 246,11,38912
LX_aa 108,228,28
LX_w 4
v_mfma_f32_16x16x32_bf16 v[112:115], a[232:235], a[0:3], v[124:127]
LX_aa 112,236,16
LX_l 80,23,0
v_mfma_f32_16x16x32_bf16 v[116:119], a[232:235], a[4:7], v[124:127]
LX_aa 116,236,20
LX_t 248,12,32768
v_mfma_f32_16x16x32_bf16 v[120:123], a[232:235], a[8:11], v[124:127]
LX_t 250,12,34816
LX_aa 120,236,24
LX_t 252,12,36864
LX_aa 124,232,12
LX_t 254,12,38912
LX_aa 124,236,28
LX_w 4
v_mfma_f32_16x16x32_bf16 v[128:131], a[240:243], a[0:3], v[140:143]
LX_l 84,23,1024
LX_aa 128,244,16
v_mfma_f32_16x16x32_bf16 v[132:135], a[240:243], a[4:7], v[140:143]
LX_l 88,23,2048
LX_aa 132,244,20
LX_t 224,13,0
v_mfma_f32_16x16x32_bf16 v[136:139], a[240:243], a[8:11], v[140:143]
LX_t 226,13,2048
LX_aa 136,244,24
LX_t 228,13,4096
LX_aa 140,240,12
LX_t 230,13,6144
LX_aa 140,244,28
LX_w 4
v_mfma_f32_16x16x32_bf16 v[144:147], a[248:251], a[0:3], v[156:159]
LX_aa 144,252,16
LX_l 92,23,3072
v_mfma_f32_16x16x32_bf16 v[148:151], a[248:251], a[4:7], v[156:159]
LX_aa 148,252,20
LX_t 232,14,0
v_mfma_f32_16x16x32_bf16 v[152:155], a[248:251], a[8:11], v[156:159]
LX_t 234,14,2048
LX_aa 152,252,24
LX_t 236,14,4096
LX_aa 156,248,12
s_mov_b32 s35, 1376256
s_add_u32 m0, s39, 24576
LX_t 238,14,6144
LX_aa 156,252,28
LX_w 4
buffer_load_dwordx4 v2, s[16:19], s35 offen  lds
v_mfma_f32_16x16x32_bf16 v[160:163], a[224:227], a[0:3], v[172:175]
LX_aa 160,228,16
s_add_u32 m0, s39, 25600
v_mfma_f32_16x16x32_bf16 v[164:167], a[224:227], a[4:7], v[172:175]
buffer_load_dwordx4 v3, s[16:19], s35 offen  lds
LX_aa 164,228,20
LX_t 240,15,0
v_mfma_f32_16x16x32_bf16 v[168:171], a[224:227], a[8:11], v[172:175]
LX_t 242,15,2048
LX_aa 168,228,24
LX_t 244,15,4096
LX_aa 172,224,12
LX_t 246,15,6144
LX_aa 172,228,28
LX_w 4
v_mfma_f32_16x16x32_bf16 v[176:179], a[232:235], a[0:3], v[188:191]
s_add_u32 m0, s39, 57344
LX_aa 176,236,16
buffer_load_dwordx4 v4, s[16:19], s35 offen  lds
v_mfma_f32_16x16x32_bf16 v[180:183], a[232:235], a[4:7], v[188:191]
LX_aa 180,236,20
LX_t 248,16,0
v_mfma_f32_16x16x32_bf16 v[184:187], a[232:235], a[8:11], v[188:191]
LX_t 250,16,2048
LX_aa 184,236,24
LX_t 252,16,4096
LX_aa 188,232,12
s_add_u32 m0, s39, 58368
LX_t 254,16,6144
LX_aa 188,236,28
LX_w 4
buffer_load_dwordx4 v5, s[16:19], s35 offen  lds
v_mfma_f32_16x16x32_bf16 v[192:195], a[240:243], a[0:3], v[204:207]
)ASM"
R"ASM(LX_aa 192,244,16
s_add_u32 m0, s39, 90112
v_mfma_f32_16x16x32_bf16 v[196:199], a[240:243], a[4:7], v[204:207]
buffer_load_dwordx4 v6, s[16:19], s35 offen  lds
LX_aa 196,244,20
LX_t 224,17,0
v_mfma_f32_16x16x32_bf16 v[200:203], a[240:243], a[8:11], v[204:207]
LX_t 226,17,1024
LX_aa 200,244,24
LX_t 228,17,2048
LX_aa 204,240,12
LX_t 230,17,3072
LX_aa 204,244,28
LX_w 4
s_add_u32 m0, s39, 91136
v_mfma_f32_16x16x32_bf16 v[208:211], a[248:251], a[0:3], v[220:223]
buffer_load_dwordx4 v7, s[16:19], s35 offen  lds
LX_aa 208,252,16
v_mfma_f32_16x16x32_bf16 v[212:215], a[248:251], a[4:7], v[220:223]
LX_aa 212,252,20
LX_t 232,20,0
v_mfma_f32_16x16x32_bf16 v[216:219], a[248:251], a[8:11], v[220:223]
LX_t 234,20,1024
LX_aa 216,252,24
LX_t 236,20,2048
LX_aa 220,248,12
s_add_u32 m0, s45, 110592
LX_t 238,20,3072
LX_aa 220,252,28
LX_w 4
buffer_load_dwordx4 v8, s[16:19], s35 offen  lds
v_mfma_f32_16x16x32_bf16 v[224:227], a[224:227], a[0:3], v[236:239]
LX_aa 224,228,16
v_mfma_f32_16x16x32_bf16 v[228:231], a[224:227], a[4:7], v[236:239]
LX_aa 228,228,20
s_waitcnt vmcnt(42) lgkmcnt(0)
s_barrier
LX_t 240,9,8192
v_mfma_f32_16x16x32_bf16 v[232:235], a[224:227], a[8:11], v[236:239]
LX_t 242,9,10240
LX_aa 232,228,24
LX_t 244,9,12288
LX_aa 236,224,12
LX_t 246,9,14336
LX_aa 236,228,28
v_mfma_f32_16x16x32_bf16 v[240:243], a[232:235], a[0:3], v[252:255]
LX_aa 240,236,16
v_mfma_f32_16x16x32_bf16 v[244:247], a[232:235], a[4:7], v[252:255]
LX_aa 244,236,20
LX_t 248,10,8192
v_mfma_f32_16x16x32_bf16 v[248:251], a[232:235], a[8:11], v[252:255]
LX_t 250,10,10240
LX_aa 248,236,24
LX_t 252,10,12288
LX_aa 252,232,12
LX_t 254,10,14336
LX_aa 252,236,28
LX_w 4
s_waitcnt vmcnt(19)
LX_aa 32,240,32
s_add_u32 s33, s38, 24576
LX_aa 32,244,48
LX_l 96,1,0
LX_aa 36,240,36
LX_aa 36,244,52
LX_t 224,11,8192
LX_aa 40,240,40
LX_t 226,11,10240
LX_aa 40,244,56
LX_t 228,11,12288
LX_aa 44,240,44
LX_t 230,11,14336
LX_aa 44,244,60
LX_w 4
LX_aa 48,248,32
LX_l 100,1,1024
LX_aa 48,252,48
LX_aa 52,248,36
LX_aa 52,252,52
LX_t 232,12,8192
LX_aa 56,248,40
LX_t 234,12,10240
LX_aa 56,252,56
LX_t 236,12,12288
LX_aa 60,248,44
LX_t 238,12,14336
LX_aa 60,252,60
LX_w 4
LX_l 104,1,2048
LX_aa 64,224,32
LX_aa 64,228,48
LX_aa 68,224,36
LX_l 108,1,3072
LX_aa 68,228,52
LX_t 240,9,40960
LX_aa 72,224,40
LX_t 242,9,43008
LX_aa 72,228,56
LX_t 244,9,45056
LX_aa 76,224,44
LX_t 246,9,47104
LX_aa 76,228,60
LX_w 4
LX_aa 80,232,32
LX_aa 80,236,48
LX_l 112,23,0
LX_aa 84,232,36
LX_aa 84,236,52
LX_t 248,10,40960
LX_aa 88,232,40
LX_t 250,10,43008
LX_aa 88,236,56
LX_t 252,10,45056
LX_aa 92,232,44
LX_t 254,10,47104
LX_aa 92,236,60
LX_w 4
LX_aa 96,240,32
LX_l 116,23,1024
LX_aa 96,244,48
LX_aa 100,240,36
LX_aa 100,244,52
LX_t 224,11,40960
LX_aa 104,240,40
LX_t 226,11,43008
LX_aa 104,244,56
LX_t 228,11,45056
LX_aa 108,240,44
LX_t 230,11,47104
LX_aa 108,244,60
LX_w 4
s_waitcnt vmcnt(21)
buffer_store_dwordx2 v[26:27], v18, s[20:23], 0 offen sc1
LX_aa 112,248,32
LX_l 120,23,2048
LX_aa 112,252,48
LX_aa 116,248,36
LX_aa 116,252,52
LX_t 232,12,40960
LX_aa 120,248,40
LX_t 234,12,43008
LX_aa 120,252,56
LX_t 236,12,45056
LX_aa 124,248,44
LX_t 238,12,47104
LX_aa 124,252,60
LX_w 4
LX_l 124,23,3072
LX_aa 128,224,32
LX_aa 128,228,48
LX_aa 132,224,36
buffer_load_dword a223, v22, s[28:31], 0 offen
LX_aa 132,228,52
LX_t 240,13,8192
v_add_u32 v22, 917504, v22
LX_aa 136,224,40
LX_t 242,13,10240
LX_aa 136,228,56
LX_t 244,13,12288
LX_aa 140,224,44
LX_t 246,13,14336
LX_aa 140,228,60
LX_w 4
LX_aa 144,232,32
s_mov_b32 s35, 1835008
s_add_u32 m0, s39, 0
LX_aa 144,236,48
buffer_load_dwordx4 v2, s[16:19], s35 offen  lds
LX_aa 148,232,36
LX_aa 148,236,52
LX_t 248,14,8192
LX_aa 152,232,40
LX_t 250,14,10240
LX_aa 152,236,56
LX_t 252,14,12288
LX_aa 156,232,44
LX_t 254,14,14336
LX_aa 156,236,60
LX_w 4
s_add_u32 m0, s39, 1024
LX_aa 160,240,32
buffer_load_dwordx4 v3, s[16:19], s35 offen  lds
LX_aa 160,244,48
LX_aa 164,240,36
LX_aa 164,244,52
LX_t 224,15,8192
LX_aa 168,240,40
LX_t 226,15,10240
LX_aa 168,244,56
LX_t 228,15,12288
LX_aa 172,240,44
s_add_u32 m0, s39, 32768
LX_t 230,15,14336
LX_aa 172,244,60
LX_w 4
buffer_load_dwordx4 v4, s[16:19], s35 offen  lds
LX_aa 176,248,32
LX_aa 176,252,48
s_add_u32 m0, s39, 33792
LX_aa 180,248,36
buffer_load_dwordx4 v5, s[16:19], s35 offen  lds
LX_aa 180,252,52
LX_t 232,16,8192
LX_aa 184,248,40
LX_t 234,16,10240
LX_aa 184,252,56
LX_t 236,16,12288
LX_aa 188,248,44
LX_t 238,16,14336
LX_aa 188,252,60
LX_w 4
LX_aa 192,224,32
s_add_u32 m0, s39, 65536
LX_aa 192,228,48
buffer_load_dwordx4 v6, s[16:19], s35 offen  lds
LX_aa 196,224,36
LX_aa 196,228,52
LX_t 240,17,4096
LX_aa 200,224,40
LX_t 242,17,5120
LX_aa 200,228,56
LX_t 244,17,6144
LX_aa 204,224,44
LX_t 246,17,7168
LX_aa 204,228,60
LX_w 4
s_add_u32 m0, s39, 66560
LX_aa 208,232,32
buffer_load_dwordx4 v7, s[16:19], s35 offen  lds
LX_aa 208,236,48
LX_aa 212,232,36
LX_aa 212,236,52
LX_t 248,20,4096
LX_aa 216,232,40
LX_t 250,20,5120
LX_aa 216,236,56
LX_t 252,20,6144
LX_aa 220,232,44
s_add_u32 m0, s45, 98304
LX_t 254,20,7168
LX_aa 220,236,60
LX_w 4
buffer_load_dwordx4 v8, s[16:19], s35 offen  lds
LX_aa 224,240,32
LX_aa 224,244,48
LX_aa 228,240,36
LX_aa 228,244,52
s_waitcnt vmcnt(52) lgkmcnt(0)
s_barrier
LX_t 224,9,16384
LX_aa 232,240,40
LX_t 226,9,18432
LX_aa 232,244,56
LX_t 228,9,20480
LX_aa 236,240,44
LX_t 230,9,22528
LX_aa 236,244,60
LX_aa 240,248,32
LX_aa 240,252,48
LX_aa 244,248,36
LX_aa 244,252,52
LX_t 232,10,16384
LX_aa 248,248,40
LX_t 234,10,18432
LX_aa 248,252,56
LX_t 236,10,20480
LX_aa 252,248,44
LX_t 238,10,22528
LX_aa 252,252,60
LX_w 4
s_waitcnt vmcnt(24)
LX_aa 32,224,64
LX_aa 32,228,80
buffer_load_dword a223, v22, s[28:31], 0 offen
LX_aa 36,224,68
v_add_u32 v22, 917504, v22
LX_aa 36,228,84
LX_t 240,11,16384
LX_aa 40,224,72
LX_t 242,11,18432
LX_aa 40,228,88
LX_t 244,11,20480
LX_aa 44,224,76
LX_t 246,11,22528
LX_aa 44,228,92
LX_w 4
LX_aa 48,232,64
LX_aa 48,236,80
LX_aa 52,232,68
LX_aa 52,236,84
LX_t 248,12,16384
LX_aa 56,232,72
LX_t 250,12,18432
LX_aa 56,236,88
LX_t 252,12,20480
LX_aa 60,232,76
s_mov_b32 s35, 2293760
s_add_u32 m0, s39, 8192
LX_t 254,12,22528
LX_aa 60,236,92
LX_w 4
buffer_load_dwordx4 v2, s[16:19], s35 offen  lds
LX_aa 64,240,64
LX_aa 64,244,80
LX_aa 68,240,68
LX_aa 68,244,84
LX_t 224,9,49152
LX_aa 72,240,72
LX_t 226,9,51200
LX_aa 72,244,88
LX_t 228,9,53248
LX_aa 76,240,76
LX_t 230,9,55296
LX_aa 76,244,92
LX_w 4
LX_aa 80,248,64
s_add_u32 m0, s39, 9216
LX_aa 80,252,80
buffer_load_dwordx4 v3, s[16:19], s35 offen  lds
LX_aa 84,248,68
LX_aa 84,252,84
LX_t 232,10,49152
LX_aa 88,248,72
LX_t 234,10,51200
LX_aa 88,252,88
LX_t 236,10,53248
LX_aa 92,248,76
LX_t 238,10,55296
LX_aa 92,252,92
LX_w 4
LX_aa 96,224,64
LX_aa 96,228,80
LX_aa 100,224,68
buffer_load_dwordx2 v[18:19], v24, s[20:23], 0 offen sc1
LX_aa 100,228,84
LX_t 240,11,49152
LX_aa 104,224,72
LX_t 242,11,51200
LX_aa 104,228,88
LX_t 244,11,53248
LX_aa 108,224,76
LX_t 246,11,55296
LX_aa 108,228,92
LX_w 4
s_add_u32 m0, s39, 40960
LX_aa 112,232,64
buffer_load_dwordx4 v4, s[16:19], s35 offen  lds
LX_aa 112,236,80
LX_aa 116,232,68
LX_aa 116,236,84
LX_t 248,12,49152
LX_aa 120,232,72
LX_t 250,12,51200
LX_aa 120,236,88
LX_t 252,12,53248
LX_aa 124,232,76
LX_t 254,12,55296
LX_aa 124,236,92
LX_w 4
LX_aa 128,240,64
LX_aa 128,244,80
s_add_u32 m0, s39, 41984
LX_aa 132,240,68
LX_F0_pad2:
s_cbranch_execz LX_F0_pad5
buffer_load_dwordx4 v5, s[16:19], s35 offen  lds
LX_aa 132,244,84
LX_t 224,13,16384
LX_aa 136,240,72
LX_t 226,13,18432
LX_aa 136,244,88
LX_t 228,13,20480
LX_aa 140,240,76
LX_t 230,13,22528
LX_aa 140,244,92
LX_w 4
LX_aa 144,248,64
LX_aa 144,252,80
LX_aa 148,248,68
LX_aa 148,252,84
LX_t 232,14,16384
LX_aa 152,248,72
LX_t 234,14,18432
LX_aa 152,252,88
LX_t 236,14,20480
LX_aa 156,248,76
LX_t 238,14,22528
LX_aa 156,252,92
LX_w 4
s_add_u32 m0, s39, 73728
LX_aa 160,224,64
buffer_load_dwordx4 v6, s[16:19], s35 offen  lds
LX_aa 160,228,80
LX_aa 164,224,68
LX_aa 164,228,84
LX_t 240,15,16384
LX_aa 168,224,72
LX_t 242,15,18432
LX_aa 168,228,88
LX_t 244,15,20480
LX_aa 172,224,76
LX_t 246,15,22528
LX_aa 172,228,92
LX_w 4
LX_aa 176,232,64
LX_aa 176,236,80
s_add_u32 m0, s39, 74752
LX_aa 180,232,68
buffer_load_dwordx4 v7, s[16:19], s35 offen  lds
LX_aa 180,236,84
LX_t 248,16,16384
LX_aa 184,232,72
LX_t 250,16,18432
LX_aa 184,236,88
LX_t 252,16,20480
LX_aa 188,232,76
LX_t 254,16,22528
LX_aa 188,236,92
LX_w 4
LX_aa 192,240,64
LX_aa 192,244,80
LX_aa 196,240,68
LX_aa 196,244,84
LX_t 224,17,8192
LX_aa 200,240,72
LX_t 226,17,9216
LX_aa 200,244,88
LX_t 228,17,10240
LX_aa 204,240,76
LX_t 230,17,11264
LX_aa 204,244,92
LX_w 4
s_add_u32 m0, s45, 102400
LX_aa 208,248,64
buffer_load_dwordx4 v8, s[16:19], s35 offen  lds
LX_aa 208,252,80
LX_aa 212,248,68
LX_aa 212,252,84
LX_t 232,20,8192
LX_aa 216,248,72
LX_t 234,20,9216
LX_aa 216,252,88
LX_t 236,20,10240
LX_aa 220,248,76
LX_t 238,20,11264
LX_aa 220,252,92
LX_w 4
LX_aa 224,224,64
LX_aa 224,228,80
LX_aa 228,224,68
LX_aa 228,228,84
s_waitcnt vmcnt(26) lgkmcnt(0)
s_barrier
LX_t 240,9,24576
LX_aa 232,224,72
LX_t 242,9,26624
LX_aa 232,228,88
LX_t 244,9,28672
LX_aa 236,224,76
LX_t 246,9,30720
LX_aa 236,228,92
LX_aa 240,232,64
LX_aa 240,236,80
LX_aa 244,232,68
s_waitcnt vmcnt(5)
v_cmp_ne_u64 vcc, s[56:57], v[18:19]
LX_aa 244,236,84
LX_t 248,10,24576
LX_aa 248,232,72
s_cbranch_vccnz LX_c14
LX_c14r:
LX_t 250,10,26624
LX_aa 248,236,88
LX_t 252,10,28672
LX_aa 252,232,76
LX_t 254,10,30720
LX_aa 252,236,92
LX_w 4
s_waitcnt vmcnt(17)
LX_aa 32,240,96
s_add_u32 s33, s38, 32768
LX_aa 32,244,112
LX_l 128,1,0
LX_aa 36,240,100
LX_aa 36,244,116
LX_t 224,11,24576
LX_aa 40,240,104
LX_t 226,11,26624
LX_aa 40,244,120
LX_t 228,11,28672
LX_aa 44,240,108
LX_t 230,11,30720
LX_aa 44,244,124
LX_w 4
LX_l 132,1,1024
LX_aa 48,248,96
LX_aa 48,252,112
LX_l 136,1,2048
LX_aa 52,248,100
LX_aa 52,252,116
LX_t 232,12,24576
LX_aa 56,248,104
LX_t 234,12,26624
LX_aa 56,252,120
LX_t 236,12,28672
LX_aa 60,248,108
LX_t 238,12,30720
LX_aa 60,252,124
LX_w 4
LX_aa 64,224,96
LX_l 140,1,3072
LX_aa 64,228,112
LX_aa 68,224,100
LX_l 144,23,0
LX_aa 68,228,116
LX_t 240,9,57344
LX_aa 72,224,104
LX_t 242,9,59392
LX_aa 72,228,120
LX_t 244,9,61440
LX_aa 76,224,108
LX_t 246,9,63488
LX_aa 76,228,124
LX_w 4
LX_aa 80,232,96
LX_aa 80,236,112
LX_l 148,23,1024
LX_aa 84,232,100
LX_aa 84,236,116
LX_t 248,10,57344
LX_aa 88,232,104
LX_t 250,10,59392
LX_aa 88,236,120
LX_t 252,10,61440
LX_aa 92,232,108
LX_t 254,10,63488
LX_aa 92,236,124
LX_w 4
LX_l 152,23,2048
LX_aa 96,240,96
LX_aa 96,244,112
LX_aa 100,240,100
LX_l 156,23,3072
LX_aa 100,244,116
LX_t 224,11,57344
LX_aa 104,240,104
LX_t 226,11,59392
LX_aa 104,244,120
LX_t 228,11,61440
LX_aa 108,240,108
LX_t 230,11,63488
LX_aa 108,244,124
LX_w 4
s_add_u32 s33, s38, 40960
LX_aa 112,248,96
LX_l 160,1,0
LX_aa 112,252,112
LX_aa 116,248,100
LX_aa 116,252,116
LX_t 232,12,57344
LX_aa 120,248,104
LX_t 234,12,59392
LX_aa 120,252,120
LX_t 236,12,61440
LX_aa 124,248,108
LX_t 238,12,63488
LX_aa 124,252,124
LX_w 4
LX_l 164,1,1024
LX_aa 128,224,96
LX_aa 128,228,112
LX_l 168,1,2048
LX_aa 132,224,100
LX_aa 132,228,116
LX_t 240,13,24576
LX_aa 136,224,104
LX_t 242,13,26624
LX_aa 136,228,120
LX_t 244,13,28672
LX_aa 140,224,108
LX_t 246,13,30720
LX_aa 140,228,124
LX_w 4
LX_l 172,1,3072
LX_aa 144,232,96
LX_aa 144,236,112
LX_aa 148,232,100
buffer_load_dword a223, v22, s[28:31], 0 offen
LX_aa 148,236,116
LX_t 248,14,24576
v_add_u32 v22, 917504, v22
LX_aa 152,232,104
LX_t 250,14,26624
LX_aa 152,236,120
LX_t 252,14,28672
LX_aa 156,232,108
LX_t 254,14,30720
LX_aa 156,236,124
LX_w 4
s_mov_b32 s35, 2752512
s_add_u32 m0, s39, 16384
LX_aa 160,240,96
buffer_load_dwordx4 v2, s[16:19], s35 offen  lds
LX_aa 160,244,112
LX_aa 164,240,100
LX_aa 164,244,116
LX_t 224,15,24576
LX_aa 168,240,104
LX_t 226,15,26624
LX_aa 168,244,120
LX_t 228,15,28672
LX_aa 172,240,108
s_add_u32 m0, s39, 17408
LX_t 230,15,30720
LX_aa 172,244,124
LX_w 4
buffer_load_dwordx4 v3, s[16:19], s35 offen  lds
LX_aa 176,248,96
s_add_u32 m0, s39, 49152
LX_aa 176,252,112
buffer_load_dwordx4 v4, s[16:19], s35 offen  lds
LX_aa 180,248,100
LX_aa 180,252,116
LX_t 232,16,24576
LX_aa 184,248,104
LX_t 234,16,26624
LX_aa 184,252,120
LX_t 236,16,28672
)ASM"
R"ASM(LX_aa 188,248,108
LX_t 238,16,30720
LX_aa 188,252,124
LX_w 4
s_add_u32 m0, s39, 50176
LX_aa 192,224,96
buffer_load_dwordx4 v5, s[16:19], s35 offen  lds
LX_aa 192,228,112
s_add_u32 m0, s39, 81920
LX_aa 196,224,100
buffer_load_dwordx4 v6, s[16:19], s35 offen  lds
LX_aa 196,228,116
LX_t 240,17,12288
LX_aa 200,224,104
LX_t 242,17,13312
LX_aa 200,228,120
LX_t 244,17,14336
LX_aa 204,224,108
LX_t 246,17,15360
LX_aa 204,228,124
LX_w 4
LX_aa 208,232,96
s_add_u32 m0, s39, 82944
LX_aa 208,236,112
buffer_load_dwordx4 v7, s[16:19], s35 offen  lds
LX_aa 212,232,100
LX_aa 212,236,116
LX_t 248,20,12288
LX_aa 216,232,104
LX_t 250,20,13312
LX_aa 216,236,120
LX_t 252,20,14336
LX_aa 220,232,108
s_add_u32 m0, s45, 106496
LX_t 254,20,15360
LX_aa 220,236,124
LX_w 4
buffer_load_dwordx4 v8, s[16:19], s35 offen  lds
LX_aa 224,240,96
LX_aa 224,244,112
LX_aa 228,240,100
LX_aa 228,244,116
s_waitcnt vmcnt(29) lgkmcnt(0)
s_barrier
LX_t 224,9,0
LX_aa 232,240,104
LX_t 226,9,2048
LX_aa 232,244,120
LX_t 228,9,4096
LX_aa 236,240,108
LX_t 230,9,6144
LX_aa 236,244,124
LX_aa 240,248,96
LX_aa 240,252,112
LX_aa 244,248,100
LX_aa 244,252,116
LX_t 232,10,0
LX_aa 248,248,104
LX_t 234,10,2048
LX_aa 248,252,120
LX_t 236,10,4096
LX_aa 252,248,108
LX_t 238,10,6144
LX_aa 252,252,124
LX_w 4
s_waitcnt vmcnt(12)
LX_aa 32,224,128
LX_aa 32,228,144
LX_l 176,23,0
LX_aa 36,224,132
LX_aa 36,228,148
LX_t 240,11,0
LX_aa 40,224,136
LX_t 242,11,2048
LX_aa 40,228,152
LX_t 244,11,4096
LX_aa 44,224,140
LX_t 246,11,6144
LX_aa 44,228,156
LX_w 4
LX_l 180,23,1024
LX_aa 48,232,128
LX_aa 48,236,144
LX_l 184,23,2048
LX_aa 52,232,132
LX_aa 52,236,148
LX_t 248,12,0
LX_aa 56,232,136
LX_t 250,12,2048
LX_aa 56,236,152
LX_t 252,12,4096
LX_aa 60,232,140
LX_t 254,12,6144
LX_aa 60,236,156
LX_w 4
LX_aa 64,240,128
LX_l 188,23,3072
LX_aa 64,244,144
s_add_u32 s33, s38, 49152
LX_aa 68,240,132
LX_l 0,1,0
LX_aa 68,244,148
LX_t 224,9,32768
LX_aa 72,240,136
LX_t 226,9,34816
LX_aa 72,244,152
LX_t 228,9,36864
LX_aa 76,240,140
LX_t 230,9,38912
LX_aa 76,244,156
LX_w 4
LX_aa 80,248,128
LX_aa 80,252,144
LX_l 4,1,1024
LX_aa 84,248,132
LX_aa 84,252,148
LX_t 232,10,32768
LX_aa 88,248,136
LX_t 234,10,34816
LX_aa 88,252,152
LX_t 236,10,36864
LX_aa 92,248,140
LX_t 238,10,38912
LX_aa 92,252,156
LX_w 4
LX_l 8,1,2048
LX_aa 96,224,128
LX_aa 96,228,144
LX_aa 100,224,132
LX_l 12,1,3072
LX_aa 100,228,148
LX_t 240,11,32768
LX_aa 104,224,136
LX_t 242,11,34816
LX_aa 104,228,152
LX_t 244,11,36864
LX_aa 108,224,140
LX_t 246,11,38912
LX_aa 108,228,156
LX_w 4
LX_aa 112,232,128
LX_l 16,23,0
LX_aa 112,236,144
LX_aa 116,232,132
LX_aa 116,236,148
LX_t 248,12,32768
LX_aa 120,232,136
LX_t 250,12,34816
LX_aa 120,236,152
LX_t 252,12,36864
LX_aa 124,232,140
LX_t 254,12,38912
LX_aa 124,236,156
LX_w 4
LX_l 20,23,1024
LX_aa 128,240,128
LX_aa 128,244,144
LX_l 24,23,2048
LX_aa 132,240,132
LX_aa 132,244,148
LX_t 224,13,0
LX_aa 136,240,136
LX_t 226,13,2048
LX_aa 136,244,152
LX_t 228,13,4096
LX_aa 140,240,140
LX_t 230,13,6144
LX_aa 140,244,156
LX_w 4
LX_l 28,23,3072
LX_aa 144,248,128
LX_aa 144,252,144
LX_aa 148,248,132
buffer_load_dword a223, v22, s[28:31], 0 offen
LX_aa 148,252,148
LX_t 232,14,0
v_add_u32 v22, 917504, v22
LX_aa 152,248,136
LX_t 234,14,2048
LX_aa 152,252,152
LX_t 236,14,4096
LX_aa 156,248,140
LX_t 238,14,6144
LX_aa 156,252,156
LX_w 4
s_mov_b32 s35, 3211264
s_add_u32 m0, s39, 24576
LX_aa 160,224,128
buffer_load_dwordx4 v2, s[16:19], s35 offen  lds
LX_aa 160,228,144
LX_aa 164,224,132
LX_aa 164,228,148
LX_t 240,15,0
LX_aa 168,224,136
LX_t 242,15,2048
LX_aa 168,228,152
LX_t 244,15,4096
LX_aa 172,224,140
s_add_u32 m0, s39, 25600
LX_t 246,15,6144
LX_aa 172,228,156
LX_w 4
buffer_load_dwordx4 v3, s[16:19], s35 offen  lds
LX_aa 176,232,128
s_add_u32 m0, s39, 57344
LX_aa 176,236,144
buffer_load_dwordx4 v4, s[16:19], s35 offen  lds
LX_aa 180,232,132
LX_aa 180,236,148
LX_t 248,16,0
LX_aa 184,232,136
LX_t 250,16,2048
LX_aa 184,236,152
LX_t 252,16,4096
LX_aa 188,232,140
LX_t 254,16,6144
LX_aa 188,236,156
LX_w 4
s_add_u32 m0, s39, 58368
LX_aa 192,240,128
buffer_load_dwordx4 v5, s[16:19], s35 offen  lds
LX_aa 192,244,144
s_add_u32 m0, s39, 90112
LX_aa 196,240,132
buffer_load_dwordx4 v6, s[16:19], s35 offen  lds
LX_aa 196,244,148
LX_t 224,17,0
LX_aa 200,240,136
LX_t 226,17,1024
LX_aa 200,244,152
LX_t 228,17,2048
LX_aa 204,240,140
LX_t 230,17,3072
LX_aa 204,244,156
LX_w 4
LX_aa 208,248,128
s_add_u32 m0, s39, 91136
LX_aa 208,252,144
buffer_load_dwordx4 v7, s[16:19], s35 offen  lds
LX_aa 212,248,132
LX_aa 212,252,148
LX_t 232,20,0
LX_aa 216,248,136
LX_t 234,20,1024
LX_aa 216,252,152
LX_t 236,20,2048
LX_aa 220,248,140
s_add_u32 m0, s45, 110592
LX_t 238,20,3072
LX_aa 220,252,156
LX_w 4
buffer_load_dwordx4 v8, s[16:19], s35 offen  lds
LX_aa 224,224,128
LX_aa 224,228,144
LX_aa 228,224,132
LX_aa 228,228,148
s_waitcnt vmcnt(40) lgkmcnt(0)
s_barrier
LX_t 240,9,8192
LX_aa 232,224,136
LX_t 242,9,10240
LX_aa 232,228,152
LX_t 244,9,12288
LX_aa 236,224,140
LX_t 246,9,14336
LX_aa 236,228,156
LX_aa 240,232,128
LX_aa 240,236,144
LX_aa 244,232,132
LX_aa 244,236,148
LX_F0_pad3:
s_cbranch_execz LX_F0_pad6
LX_t 248,10,8192
LX_aa 248,232,136
LX_t 250,10,10240
LX_aa 248,236,152
LX_t 252,10,12288
LX_aa 252,232,140
LX_t 254,10,14336
LX_aa 252,236,156
LX_w 4
s_waitcnt vmcnt(16)
LX_aa 32,240,160
s_add_u32 s33, s38, 57344
LX_aa 32,244,176
LX_l 32,1,0
LX_aa 36,240,164
LX_aa 36,244,180
LX_t 224,11,8192
LX_aa 40,240,168
LX_t 226,11,10240
LX_aa 40,244,184
LX_t 228,11,12288
LX_aa 44,240,172
LX_t 230,11,14336
LX_aa 44,244,188
LX_w 4
LX_aa 48,248,160
LX_l 36,1,1024
LX_aa 48,252,176
LX_aa 52,248,164
LX_aa 52,252,180
LX_t 232,12,8192
LX_aa 56,248,168
LX_t 234,12,10240
LX_aa 56,252,184
LX_t 236,12,12288
LX_aa 60,248,172
LX_t 238,12,14336
LX_aa 60,252,188
LX_w 4
LX_l 40,1,2048
LX_aa 64,224,160
LX_aa 64,228,176
LX_aa 68,224,164
LX_l 44,1,3072
LX_aa 68,228,180
LX_t 240,9,40960
LX_aa 72,224,168
LX_t 242,9,43008
LX_aa 72,228,184
LX_t 244,9,45056
LX_aa 76,224,172
LX_t 246,9,47104
LX_aa 76,228,188
LX_w 4
LX_aa 80,232,160
LX_aa 80,236,176
LX_l 48,23,0
LX_aa 84,232,164
LX_aa 84,236,180
LX_t 248,10,40960
LX_aa 88,232,168
LX_t 250,10,43008
LX_aa 88,236,184
LX_t 252,10,45056
LX_aa 92,232,172
LX_t 254,10,47104
LX_aa 92,236,188
LX_w 4
LX_aa 96,240,160
LX_l 52,23,1024
LX_aa 96,244,176
LX_aa 100,240,164
LX_aa 100,244,180
LX_t 224,11,40960
LX_aa 104,240,168
LX_t 226,11,43008
LX_aa 104,244,184
LX_t 228,11,45056
LX_aa 108,240,172
LX_t 230,11,47104
LX_aa 108,244,188
LX_w 4
LX_l 56,23,2048
LX_aa 112,248,160
LX_aa 112,252,176
LX_aa 116,248,164
LX_l 60,23,3072
LX_aa 116,252,180
LX_t 232,12,40960
LX_aa 120,248,168
LX_t 234,12,43008
LX_aa 120,252,184
LX_t 236,12,45056
LX_aa 124,248,172
LX_t 238,12,47104
LX_aa 124,252,188
LX_w 4
LX_aa 128,224,160
LX_aa 128,228,176
buffer_load_dword a223, v22, s[28:31], 0 offen
LX_aa 132,224,164
v_add_u32 v22, 917504, v22
LX_aa 132,228,180
LX_t 240,13,8192
LX_aa 136,224,168
LX_t 242,13,10240
LX_aa 136,228,184
LX_t 244,13,12288
LX_aa 140,224,172
LX_t 246,13,14336
LX_aa 140,228,188
LX_w 4
s_mov_b32 s35, 3670016
s_add_u32 m0, s39, 0
LX_aa 144,232,160
buffer_load_dwordx4 v2, s[16:19], s35 offen  lds
LX_aa 144,236,176
LX_aa 148,232,164
LX_aa 148,236,180
LX_t 248,14,8192
LX_aa 152,232,168
LX_t 250,14,10240
LX_aa 152,236,184
LX_t 252,14,12288
LX_aa 156,232,172
s_add_u32 m0, s39, 1024
LX_t 254,14,14336
LX_aa 156,236,188
LX_w 4
buffer_load_dwordx4 v3, s[16:19], s35 offen  lds
LX_aa 160,240,160
LX_aa 160,244,176
s_add_u32 m0, s39, 32768
LX_aa 164,240,164
buffer_load_dwordx4 v4, s[16:19], s35 offen  lds
LX_aa 164,244,180
LX_t 224,15,8192
LX_aa 168,240,168
LX_t 226,15,10240
LX_aa 168,244,184
LX_t 228,15,12288
LX_aa 172,240,172
LX_t 230,15,14336
LX_aa 172,244,188
LX_w 4
LX_aa 176,248,160
s_add_u32 m0, s39, 33792
LX_aa 176,252,176
buffer_load_dwordx4 v5, s[16:19], s35 offen  lds
LX_aa 180,248,164
LX_aa 180,252,180
LX_t 232,16,8192
LX_aa 184,248,168
LX_t 234,16,10240
LX_aa 184,252,184
LX_t 236,16,12288
LX_aa 188,248,172
LX_t 238,16,14336
LX_aa 188,252,188
LX_w 4
s_add_u32 m0, s39, 65536
LX_aa 192,224,160
buffer_load_dwordx4 v6, s[16:19], s35 offen  lds
LX_aa 192,228,176
LX_aa 196,224,164
LX_aa 196,228,180
LX_t 240,17,4096
LX_aa 200,224,168
LX_t 242,17,5120
LX_aa 200,228,184
LX_t 244,17,6144
LX_aa 204,224,172
s_add_u32 m0, s39, 66560
LX_t 246,17,7168
LX_aa 204,228,188
LX_w 4
buffer_load_dwordx4 v7, s[16:19], s35 offen  lds
LX_aa 208,232,160
LX_aa 208,236,176
s_add_u32 m0, s45, 98304
LX_aa 212,232,164
buffer_load_dwordx4 v8, s[16:19], s35 offen  lds
LX_aa 212,236,180
LX_t 248,20,4096
LX_aa 216,232,168
LX_t 250,20,5120
LX_aa 216,236,184
LX_t 252,20,6144
LX_aa 220,232,172
LX_t 254,20,7168
LX_aa 220,236,188
LX_w 4
LX_aa 224,240,160
LX_aa 224,244,176
LX_aa 228,240,164
LX_aa 228,244,180
s_waitcnt vmcnt(36) lgkmcnt(0)
s_barrier
LX_t 224,9,16384
LX_aa 232,240,168
LX_t 226,9,18432
LX_aa 232,244,184
LX_t 228,9,20480
LX_aa 236,240,172
LX_t 230,9,22528
LX_aa 236,244,188
LX_aa 240,248,160
LX_aa 240,252,176
LX_aa 244,248,164
LX_aa 244,252,180
LX_t 232,10,16384
LX_aa 248,248,168
LX_t 234,10,18432
LX_aa 248,252,184
LX_t 236,10,20480
LX_aa 252,248,172
LX_t 238,10,22528
LX_aa 252,252,188
LX_w 4
s_waitcnt vmcnt(24)
LX_aa 32,224,0
s_add_u32 s33, s38, 65536
LX_aa 32,228,16
LX_l 64,1,0
LX_aa 36,224,4
LX_aa 36,228,20
LX_t 240,11,16384
LX_aa 40,224,8
LX_t 242,11,18432
LX_aa 40,228,24
LX_t 244,11,20480
LX_aa 44,224,12
LX_t 246,11,22528
LX_aa 44,228,28
LX_w 4
LX_aa 48,232,0
LX_l 68,1,1024
LX_aa 48,236,16
LX_aa 52,232,4
LX_aa 52,236,20
LX_t 248,12,16384
LX_aa 56,232,8
LX_t 250,12,18432
LX_aa 56,236,24
LX_t 252,12,20480
LX_aa 60,232,12
LX_t 254,12,22528
LX_aa 60,236,28
LX_w 4
LX_l 72,1,2048
LX_aa 64,240,0
LX_aa 64,244,16
LX_aa 68,240,4
LX_l 76,1,3072
LX_aa 68,244,20
LX_t 224,9,49152
LX_aa 72,240,8
LX_t 226,9,51200
LX_aa 72,244,24
LX_t 228,9,53248
LX_aa 76,240,12
LX_t 230,9,55296
LX_aa 76,244,28
LX_w 4
LX_aa 80,248,0
LX_aa 80,252,16
LX_l 80,23,0
LX_aa 84,248,4
LX_aa 84,252,20
LX_t 232,10,49152
LX_aa 88,248,8
LX_t 234,10,51200
LX_aa 88,252,24
LX_t 236,10,53248
LX_aa 92,248,12
LX_t 238,10,55296
LX_aa 92,252,28
LX_w 4
LX_aa 96,224,0
LX_l 84,23,1024
LX_aa 96,228,16
LX_aa 100,224,4
LX_aa 100,228,20
LX_t 240,11,49152
LX_aa 104,224,8
LX_t 242,11,51200
LX_aa 104,228,24
LX_t 244,11,53248
LX_aa 108,224,12
LX_t 246,11,55296
LX_aa 108,228,28
LX_w 4
LX_l 88,23,2048
LX_aa 112,232,0
LX_aa 112,236,16
LX_aa 116,232,4
LX_l 92,23,3072
LX_aa 116,236,20
LX_t 248,12,49152
LX_aa 120,232,8
LX_t 250,12,51200
LX_aa 120,236,24
LX_t 252,12,53248
LX_aa 124,232,12
LX_t 254,12,55296
LX_aa 124,236,28
LX_w 4
LX_aa 128,240,0
LX_aa 128,244,16
buffer_load_dword a223, v22, s[28:31], 0 offen
LX_aa 132,240,4
v_add_u32 v22, 917504, v22
LX_aa 132,244,20
LX_t 224,13,16384
LX_aa 136,240,8
LX_t 226,13,18432
LX_aa 136,244,24
LX_t 228,13,20480
LX_aa 140,240,12
LX_t 230,13,22528
LX_aa 140,244,28
LX_w 4
s_mov_b32 s35, 4128768
s_add_u32 m0, s39, 8192
LX_aa 144,248,0
buffer_load_dwordx4 v2, s[16:19], s35 offen  lds
LX_aa 144,252,16
LX_aa 148,248,4
LX_aa 148,252,20
LX_t 232,14,16384
LX_aa 152,248,8
LX_t 234,14,18432
LX_aa 152,252,24
LX_t 236,14,20480
LX_aa 156,248,12
s_add_u32 m0, s39, 9216
LX_t 238,14,22528
LX_aa 156,252,28
LX_w 4
buffer_load_dwordx4 v3, s[16:19], s35 offen  lds
LX_aa 160,224,0
LX_aa 160,228,16
s_add_u32 m0, s39, 40960
LX_aa 164,224,4
buffer_load_dwordx4 v4, s[16:19], s35 offen  lds
LX_aa 164,228,20
LX_t 240,15,16384
LX_aa 168,224,8
LX_t 242,15,18432
LX_aa 168,228,24
LX_t 244,15,20480
LX_aa 172,224,12
LX_t 246,15,22528
LX_aa 172,228,28
LX_w 4
LX_aa 176,232,0
s_add_u32 m0, s39, 41984
LX_aa 176,236,16
buffer_load_dwordx4 v5, s[16:19], s35 offen  lds
LX_aa 180,232,4
LX_aa 180,236,20
LX_t 248,16,16384
LX_aa 184,232,8
LX_t 250,16,18432
LX_aa 184,236,24
LX_t 252,16,20480
LX_aa 188,232,12
LX_t 254,16,22528
LX_aa 188,236,28
LX_w 4
s_add_u32 m0, s39, 73728
LX_aa 192,240,0
buffer_load_dwordx4 v6, s[16:19], s35 offen  lds
LX_aa 192,244,16
LX_aa 196,240,4
LX_aa 196,244,20
LX_t 224,17,8192
LX_aa 200,240,8
LX_t 226,17,9216
LX_aa 200,244,24
LX_t 228,17,10240
LX_aa 204,240,12
s_add_u32 m0, s39, 74752
LX_t 230,17,11264
LX_aa 204,244,28
)ASM"
R"ASM(LX_w 4
buffer_load_dwordx4 v7, s[16:19], s35 offen  lds
LX_aa 208,248,0
LX_aa 208,252,16
s_add_u32 m0, s45, 102400
LX_aa 212,248,4
buffer_load_dwordx4 v8, s[16:19], s35 offen  lds
LX_aa 212,252,20
LX_t 232,20,8192
LX_aa 216,248,8
LX_t 234,20,9216
LX_aa 216,252,24
LX_t 236,20,10240
LX_aa 220,248,12
LX_t 238,20,11264
LX_aa 220,252,28
LX_w 4
LX_aa 224,224,0
LX_aa 224,228,16
LX_aa 228,224,4
LX_aa 228,228,20
s_waitcnt vmcnt(32) lgkmcnt(0)
s_barrier
LX_t 240,9,24576
LX_aa 232,224,8
LX_t 242,9,26624
LX_aa 232,228,24
LX_t 244,9,28672
LX_aa 236,224,12
LX_t 246,9,30720
LX_aa 236,228,28
LX_aa 240,232,0
LX_aa 240,236,16
LX_aa 244,232,4
LX_aa 244,236,20
LX_t 248,10,24576
LX_aa 248,232,8
LX_t 250,10,26624
LX_aa 248,236,24
LX_t 252,10,28672
LX_aa 252,232,12
LX_t 254,10,30720
LX_aa 252,236,28
LX_w 4
s_waitcnt vmcnt(24)
LX_aa 32,240,32
s_add_u32 s33, s38, 73728
LX_aa 32,244,48
LX_l 96,1,0
LX_aa 36,240,36
LX_aa 36,244,52
LX_t 224,11,24576
LX_aa 40,240,40
LX_t 226,11,26624
LX_aa 40,244,56
LX_t 228,11,28672
LX_aa 44,240,44
LX_t 230,11,30720
LX_aa 44,244,60
LX_w 4
LX_l 100,1,1024
LX_aa 48,248,32
LX_aa 48,252,48
LX_aa 52,248,36
LX_l 104,1,2048
LX_aa 52,252,52
LX_t 232,12,24576
LX_aa 56,248,40
LX_t 234,12,26624
LX_aa 56,252,56
LX_t 236,12,28672
LX_aa 60,248,44
LX_t 238,12,30720
LX_aa 60,252,60
LX_w 4
LX_aa 64,224,32
LX_aa 64,228,48
LX_l 108,1,3072
LX_aa 68,224,36
LX_aa 68,228,52
LX_t 240,9,57344
LX_aa 72,224,40
LX_t 242,9,59392
LX_aa 72,228,56
LX_t 244,9,61440
LX_aa 76,224,44
LX_t 246,9,63488
LX_aa 76,228,60
LX_w 4
LX_aa 80,232,32
LX_l 112,23,0
LX_aa 80,236,48
LX_aa 84,232,36
LX_aa 84,236,52
LX_t 248,10,57344
LX_aa 88,232,40
LX_t 250,10,59392
LX_aa 88,236,56
LX_t 252,10,61440
LX_aa 92,232,44
LX_t 254,10,63488
LX_aa 92,236,60
LX_w 4
LX_l 116,23,1024
LX_aa 96,240,32
LX_aa 96,244,48
LX_aa 100,240,36
LX_l 120,23,2048
LX_aa 100,244,52
LX_t 224,11,57344
LX_aa 104,240,40
LX_t 226,11,59392
LX_aa 104,244,56
LX_t 228,11,61440
LX_aa 108,240,44
LX_t 230,11,63488
LX_aa 108,244,60
LX_w 4
LX_aa 112,248,32
LX_aa 112,252,48
LX_l 124,23,3072
LX_aa 116,248,36
LX_aa 116,252,52
LX_t 232,12,57344
LX_aa 120,248,40
LX_t 234,12,59392
LX_aa 120,252,56
LX_t 236,12,61440
LX_aa 124,248,44
LX_t 238,12,63488
LX_aa 124,252,60
LX_w 4
LX_aa 128,224,32
buffer_load_dword a223, v31, s[20:23], 0 offen sc1
LX_aa 128,228,48
LX_aa 132,224,36
buffer_load_dword a223, v22, s[28:31], 0 offen
LX_aa 132,228,52
LX_t 240,13,24576
v_add_u32 v22, 917504, v22
LX_aa 136,224,40
LX_t 242,13,26624
LX_aa 136,228,56
LX_t 244,13,28672
LX_aa 140,224,44
LX_t 246,13,30720
LX_aa 140,228,60
LX_F0_pad4:
s_cbranch_execz LX_F0_pad7
LX_w 4
LX_aa 144,232,32
s_mov_b32 s35, 4587520
s_add_u32 m0, s39, 16384
LX_aa 144,236,48
buffer_load_dwordx4 v2, s[16:19], s35 offen  lds
LX_aa 148,232,36
LX_aa 148,236,52
LX_t 248,14,24576
LX_aa 152,232,40
LX_t 250,14,26624
LX_aa 152,236,56
LX_t 252,14,28672
LX_aa 156,232,44
LX_t 254,14,30720
LX_aa 156,236,60
LX_w 4
s_add_u32 m0, s39, 17408
LX_aa 160,240,32
buffer_load_dwordx4 v3, s[16:19], s35 offen  lds
LX_aa 160,244,48
LX_aa 164,240,36
LX_aa 164,244,52
LX_t 224,15,24576
LX_aa 168,240,40
LX_t 226,15,26624
LX_aa 168,244,56
LX_t 228,15,28672
LX_aa 172,240,44
s_add_u32 m0, s39, 49152
LX_t 230,15,30720
LX_aa 172,244,60
LX_w 4
buffer_load_dwordx4 v4, s[16:19], s35 offen  lds
LX_aa 176,248,32
LX_aa 176,252,48
s_add_u32 m0, s39, 50176
LX_aa 180,248,36
buffer_load_dwordx4 v5, s[16:19], s35 offen  lds
LX_aa 180,252,52
LX_t 232,16,24576
LX_aa 184,248,40
LX_t 234,16,26624
LX_aa 184,252,56
LX_t 236,16,28672
LX_aa 188,248,44
LX_t 238,16,30720
LX_aa 188,252,60
LX_w 4
LX_aa 192,224,32
s_add_u32 m0, s39, 81920
LX_aa 192,228,48
buffer_load_dwordx4 v6, s[16:19], s35 offen  lds
LX_aa 196,224,36
LX_aa 196,228,52
LX_t 240,17,12288
LX_aa 200,224,40
LX_t 242,17,13312
LX_aa 200,228,56
LX_t 244,17,14336
LX_aa 204,224,44
LX_t 246,17,15360
LX_aa 204,228,60
LX_w 4
s_add_u32 m0, s39, 82944
LX_aa 208,232,32
buffer_load_dwordx4 v7, s[16:19], s35 offen  lds
LX_aa 208,236,48
LX_aa 212,232,36
LX_aa 212,236,52
LX_t 248,20,12288
LX_aa 216,232,40
LX_t 250,20,13312
LX_aa 216,236,56
LX_t 252,20,14336
LX_aa 220,232,44
s_add_u32 m0, s45, 106496
LX_t 254,20,15360
LX_aa 220,236,60
LX_w 4
buffer_load_dwordx4 v8, s[16:19], s35 offen  lds
LX_aa 224,240,32
LX_aa 224,244,48
LX_aa 228,240,36
LX_aa 228,244,52
s_waitcnt vmcnt(33) lgkmcnt(0)
s_barrier
LX_t 224,9,0
LX_aa 232,240,40
LX_t 226,9,2048
LX_aa 232,244,56
LX_t 228,9,4096
LX_aa 236,240,44
LX_t 230,9,6144
LX_aa 236,244,60
LX_aa 240,248,32
LX_aa 240,252,48
LX_aa 244,248,36
LX_aa 244,252,52
LX_t 232,10,0
LX_aa 248,248,40
LX_t 234,10,2048
LX_aa 248,252,56
LX_t 236,10,4096
LX_aa 252,248,44
LX_t 238,10,6144
LX_aa 252,252,60
LX_w 4
s_waitcnt vmcnt(25)
LX_aa 32,224,64
s_add_u32 s33, s38, 81920
LX_aa 32,228,80
LX_l 128,1,0
LX_aa 36,224,68
LX_aa 36,228,84
LX_t 240,11,0
LX_aa 40,224,72
LX_t 242,11,2048
LX_aa 40,228,88
LX_t 244,11,4096
LX_aa 44,224,76
LX_t 246,11,6144
LX_aa 44,228,92
LX_w 4
LX_l 132,1,1024
LX_aa 48,232,64
LX_aa 48,236,80
LX_l 136,1,2048
LX_aa 52,232,68
LX_aa 52,236,84
LX_t 248,12,0
LX_aa 56,232,72
LX_t 250,12,2048
LX_aa 56,236,88
LX_t 252,12,4096
LX_aa 60,232,76
LX_t 254,12,6144
LX_aa 60,236,92
LX_w 4
LX_aa 64,240,64
LX_l 140,1,3072
LX_aa 64,244,80
LX_aa 68,240,68
LX_l 144,23,0
LX_aa 68,244,84
LX_t 224,9,32768
LX_aa 72,240,72
LX_t 226,9,34816
LX_aa 72,244,88
LX_t 228,9,36864
LX_aa 76,240,76
LX_t 230,9,38912
LX_aa 76,244,92
LX_w 4
LX_aa 80,248,64
LX_aa 80,252,80
LX_l 148,23,1024
LX_aa 84,248,68
LX_aa 84,252,84
LX_t 232,10,32768
LX_aa 88,248,72
LX_t 234,10,34816
LX_aa 88,252,88
LX_t 236,10,36864
LX_aa 92,248,76
LX_t 238,10,38912
LX_aa 92,252,92
LX_w 4
LX_l 152,23,2048
LX_aa 96,224,64
LX_aa 96,228,80
LX_aa 100,224,68
LX_l 156,23,3072
LX_aa 100,228,84
LX_t 240,11,32768
LX_aa 104,224,72
LX_t 242,11,34816
LX_aa 104,228,88
LX_t 244,11,36864
LX_aa 108,224,76
LX_t 246,11,38912
LX_aa 108,228,92
LX_w 4
s_add_u32 s33, s38, 90112
LX_aa 112,232,64
LX_l 160,1,0
LX_aa 112,236,80
LX_aa 116,232,68
LX_aa 116,236,84
LX_t 248,12,32768
LX_aa 120,232,72
LX_t 250,12,34816
LX_aa 120,236,88
LX_t 252,12,36864
LX_aa 124,232,76
LX_t 254,12,38912
LX_aa 124,236,92
LX_w 4
LX_l 164,1,1024
LX_aa 128,240,64
LX_aa 128,244,80
LX_l 168,1,2048
LX_aa 132,240,68
LX_aa 132,244,84
LX_t 224,13,0
LX_aa 136,240,72
LX_t 226,13,2048
LX_aa 136,244,88
LX_t 228,13,4096
LX_aa 140,240,76
LX_t 230,13,6144
LX_aa 140,244,92
LX_w 4
LX_l 172,1,3072
LX_aa 144,248,64
LX_aa 144,252,80
LX_aa 148,248,68
buffer_load_dword a223, v22, s[28:31], 0 offen
LX_aa 148,252,84
LX_t 232,14,0
v_add_u32 v22, 4288544768, v22
LX_aa 152,248,72
LX_t 234,14,2048
LX_aa 152,252,88
LX_t 236,14,4096
LX_aa 156,248,76
LX_t 238,14,6144
LX_aa 156,252,92
LX_w 4
s_mov_b32 s35, 5046272
s_add_u32 m0, s39, 24576
LX_aa 160,224,64
buffer_load_dwordx4 v2, s[16:19], s35 offen  lds
LX_aa 160,228,80
LX_aa 164,224,68
LX_aa 164,228,84
LX_t 240,15,0
LX_aa 168,224,72
LX_t 242,15,2048
LX_aa 168,228,88
LX_t 244,15,4096
LX_aa 172,224,76
s_add_u32 m0, s39, 25600
LX_t 246,15,6144
LX_aa 172,228,92
LX_w 4
buffer_load_dwordx4 v3, s[16:19], s35 offen  lds
LX_aa 176,232,64
s_add_u32 m0, s39, 57344
LX_aa 176,236,80
buffer_load_dwordx4 v4, s[16:19], s35 offen  lds
LX_aa 180,232,68
LX_aa 180,236,84
LX_t 248,16,0
LX_aa 184,232,72
LX_t 250,16,2048
LX_aa 184,236,88
LX_t 252,16,4096
LX_aa 188,232,76
LX_t 254,16,6144
LX_aa 188,236,92
LX_w 4
s_add_u32 m0, s39, 58368
LX_aa 192,240,64
buffer_load_dwordx4 v5, s[16:19], s35 offen  lds
LX_aa 192,244,80
s_add_u32 m0, s39, 90112
LX_aa 196,240,68
buffer_load_dwordx4 v6, s[16:19], s35 offen  lds
LX_aa 196,244,84
LX_t 224,17,0
LX_aa 200,240,72
LX_t 226,17,1024
LX_aa 200,244,88
LX_t 228,17,2048
LX_aa 204,240,76
LX_t 230,17,3072
LX_aa 204,244,92
LX_w 4
LX_aa 208,248,64
s_add_u32 m0, s39, 91136
LX_aa 208,252,80
buffer_load_dwordx4 v7, s[16:19], s35 offen  lds
LX_aa 212,248,68
LX_aa 212,252,84
LX_t 232,20,0
LX_aa 216,248,72
LX_t 234,20,1024
LX_aa 216,252,88
LX_t 236,20,2048
LX_aa 220,248,76
s_add_u32 m0, s45, 110592
LX_t 238,20,3072
LX_aa 220,252,92
LX_w 4
buffer_load_dwordx4 v8, s[16:19], s35 offen  lds
LX_aa 224,224,64
LX_aa 224,228,80
LX_aa 228,224,68
LX_aa 228,228,84
s_waitcnt vmcnt(37) lgkmcnt(0)
s_barrier
LX_t 240,9,8192
LX_aa 232,224,72
LX_t 242,9,10240
LX_aa 232,228,88
LX_t 244,9,12288
LX_aa 236,224,76
LX_t 246,9,14336
LX_aa 236,228,92
LX_aa 240,232,64
LX_aa 240,236,80
LX_aa 244,232,68
LX_aa 244,236,84
LX_t 248,10,8192
LX_aa 248,232,72
LX_t 250,10,10240
LX_aa 248,236,88
LX_t 252,10,12288
LX_aa 252,232,76
LX_t 254,10,14336
LX_aa 252,236,92
LX_w 4
s_waitcnt vmcnt(29)
LX_aa 32,240,96
LX_aa 32,244,112
LX_l 176,23,0
LX_aa 36,240,100
LX_aa 36,244,116
LX_t 224,11,8192
LX_aa 40,240,104
LX_t 226,11,10240
LX_aa 40,244,120
LX_t 228,11,12288
LX_aa 44,240,108
LX_t 230,11,14336
LX_aa 44,244,124
LX_w 4
LX_l 180,23,1024
LX_aa 48,248,96
LX_aa 48,252,112
LX_l 184,23,2048
LX_aa 52,248,100
LX_aa 52,252,116
LX_t 232,12,8192
LX_aa 56,248,104
LX_t 234,12,10240
LX_aa 56,252,120
LX_t 236,12,12288
LX_aa 60,248,108
LX_t 238,12,14336
LX_aa 60,252,124
LX_w 4
LX_aa 64,224,96
LX_l 188,23,3072
LX_aa 64,228,112
s_add_u32 s33, s38, 98304
LX_aa 68,224,100
LX_l 0,1,0
LX_aa 68,228,116
LX_t 240,9,40960
LX_aa 72,224,104
LX_t 242,9,43008
LX_aa 72,228,120
LX_t 244,9,45056
LX_aa 76,224,108
LX_t 246,9,47104
LX_aa 76,228,124
LX_w 4
LX_aa 80,232,96
LX_aa 80,236,112
LX_l 4,1,1024
LX_aa 84,232,100
LX_aa 84,236,116
LX_t 248,10,40960
LX_aa 88,232,104
LX_t 250,10,43008
LX_aa 88,236,120
LX_t 252,10,45056
LX_aa 92,232,108
LX_t 254,10,47104
LX_aa 92,236,124
LX_w 4
LX_l 8,1,2048
LX_aa 96,240,96
LX_aa 96,244,112
LX_aa 100,240,100
LX_l 12,1,3072
LX_aa 100,244,116
LX_t 224,11,40960
LX_aa 104,240,104
LX_t 226,11,43008
LX_aa 104,244,120
LX_t 228,11,45056
LX_aa 108,240,108
LX_t 230,11,47104
LX_aa 108,244,124
LX_w 4
LX_aa 112,248,96
LX_l 16,23,0
LX_aa 112,252,112
LX_aa 116,248,100
LX_aa 116,252,116
LX_t 232,12,40960
LX_aa 120,248,104
LX_t 234,12,43008
LX_aa 120,252,120
LX_t 236,12,45056
LX_aa 124,248,108
LX_t 238,12,47104
LX_aa 124,252,124
LX_w 4
LX_l 20,23,1024
LX_aa 128,224,96
LX_aa 128,228,112
LX_l 24,23,2048
LX_aa 132,224,100
LX_aa 132,228,116
LX_t 240,13,8192
LX_aa 136,224,104
LX_t 242,13,10240
LX_aa 136,228,120
LX_t 244,13,12288
LX_aa 140,224,108
LX_t 246,13,14336
LX_aa 140,228,124
LX_w 4
LX_l 28,23,3072
LX_aa 144,232,96
LX_aa 144,236,112
LX_aa 148,232,100
buffer_load_dword a223, v22, s[84:87], 0 offen
LX_aa 148,236,116
LX_t 248,14,8192
v_add_u32 v22, 917504, v22
LX_aa 152,232,104
LX_t 250,14,10240
LX_aa 152,236,120
LX_t 252,14,12288
LX_aa 156,232,108
LX_t 254,14,14336
LX_aa 156,236,124
LX_w 4
s_mov_b32 s35, 5505024
s_add_u32 m0, s39, 0
LX_aa 160,240,96
buffer_load_dwordx4 v2, s[16:19], s35 offen  lds
LX_aa 160,244,112
LX_aa 164,240,100
LX_aa 164,244,116
LX_t 224,15,8192
LX_aa 168,240,104
LX_t 226,15,10240
LX_aa 168,244,120
LX_t 228,15,12288
LX_aa 172,240,108
s_add_u32 m0, s39, 1024
LX_t 230,15,14336
LX_aa 172,244,124
LX_w 4
buffer_load_dwordx4 v3, s[16:19], s35 offen  lds
LX_aa 176,248,96
s_add_u32 m0, s39, 32768
LX_aa 176,252,112
buffer_load_dwordx4 v4, s[16:19], s35 offen  lds
LX_aa 180,248,100
LX_aa 180,252,116
LX_t 232,16,8192
LX_aa 184,248,104
LX_t 234,16,10240
LX_aa 184,252,120
LX_t 236,16,12288
LX_aa 188,248,108
LX_t 238,16,14336
LX_aa 188,252,124
LX_w 4
s_add_u32 m0, s39, 33792
LX_aa 192,224,96
buffer_load_dwordx4 v5, s[16:19], s35 offen  lds
LX_aa 192,228,112
s_add_u32 m0, s39, 65536
LX_aa 196,224,100
buffer_load_dwordx4 v6, s[16:19], s35 offen  lds
LX_aa 196,228,116
LX_t 240,17,4096
LX_aa 200,224,104
LX_t 242,17,5120
LX_aa 200,228,120
LX_t 244,17,6144
LX_aa 204,224,108
LX_t 246,17,7168
LX_aa 204,228,124
LX_w 4
LX_aa 208,232,96
s_add_u32 m0, s39, 66560
LX_aa 208,236,112
buffer_load_dwordx4 v7, s[16:19], s35 offen  lds
LX_aa 212,232,100
LX_aa 212,236,116
LX_t 248,20,4096
LX_aa 216,232,104
LX_t 250,20,5120
LX_aa 216,236,120
LX_t 252,20,6144
LX_aa 220,232,108
s_add_u32 m0, s45, 98304
LX_t 254,20,7168
LX_aa 220,236,124
LX_w 4
buffer_load_dwordx4 v8, s[16:19], s35 offen  lds
LX_aa 224,240,96
LX_aa 224,244,112
LX_aa 228,240,100
LX_aa 228,244,116
)ASM"
R"ASM(s_waitcnt vmcnt(40) lgkmcnt(0)
s_barrier
LX_t 224,9,16384
LX_aa 232,240,104
LX_t 226,9,18432
LX_aa 232,244,120
LX_t 228,9,20480
LX_aa 236,240,108
LX_t 230,9,22528
LX_aa 236,244,124
LX_aa 240,248,96
LX_aa 240,252,112
LX_aa 244,248,100
LX_aa 244,252,116
LX_t 232,10,16384
LX_aa 248,248,104
LX_t 234,10,18432
LX_aa 248,252,120
LX_F0_pad5:
s_cbranch_execz LX_F0_pad8
LX_t 236,10,20480
LX_aa 252,248,108
LX_t 238,10,22528
LX_aa 252,252,124
LX_w 4
s_waitcnt vmcnt(32)
LX_aa 32,224,128
s_add_u32 s33, s38, 106496
LX_aa 32,228,144
LX_l 32,1,0
LX_aa 36,224,132
LX_aa 36,228,148
LX_t 240,11,16384
LX_aa 40,224,136
LX_t 242,11,18432
LX_aa 40,228,152
LX_t 244,11,20480
LX_aa 44,224,140
LX_t 246,11,22528
LX_aa 44,228,156
LX_w 4
LX_l 36,1,1024
LX_aa 48,232,128
LX_aa 48,236,144
LX_l 40,1,2048
LX_aa 52,232,132
LX_aa 52,236,148
LX_t 248,12,16384
LX_aa 56,232,136
LX_t 250,12,18432
LX_aa 56,236,152
LX_t 252,12,20480
LX_aa 60,232,140
LX_t 254,12,22528
LX_aa 60,236,156
LX_w 4
LX_aa 64,240,128
LX_l 44,1,3072
LX_aa 64,244,144
LX_aa 68,240,132
LX_l 48,23,0
LX_aa 68,244,148
LX_t 224,9,49152
LX_aa 72,240,136
LX_t 226,9,51200
LX_aa 72,244,152
LX_t 228,9,53248
LX_aa 76,240,140
LX_t 230,9,55296
LX_aa 76,244,156
LX_w 4
LX_aa 80,248,128
LX_aa 80,252,144
LX_l 52,23,1024
LX_aa 84,248,132
LX_aa 84,252,148
LX_t 232,10,49152
LX_aa 88,248,136
LX_t 234,10,51200
LX_aa 88,252,152
LX_t 236,10,53248
LX_aa 92,248,140
LX_t 238,10,55296
LX_aa 92,252,156
LX_w 4
LX_l 56,23,2048
LX_aa 96,224,128
LX_aa 96,228,144
LX_aa 100,224,132
LX_l 60,23,3072
LX_aa 100,228,148
LX_t 240,11,49152
LX_aa 104,224,136
LX_t 242,11,51200
LX_aa 104,228,152
LX_t 244,11,53248
LX_aa 108,224,140
LX_t 246,11,55296
LX_aa 108,228,156
LX_w 4
s_add_u32 s33, s38, 114688
LX_aa 112,232,128
LX_l 64,1,0
LX_aa 112,236,144
LX_aa 116,232,132
LX_aa 116,236,148
LX_t 248,12,49152
LX_aa 120,232,136
LX_t 250,12,51200
LX_aa 120,236,152
LX_t 252,12,53248
LX_aa 124,232,140
LX_t 254,12,55296
LX_aa 124,236,156
LX_w 4
LX_l 68,1,1024
LX_aa 128,240,128
LX_aa 128,244,144
LX_l 72,1,2048
LX_aa 132,240,132
LX_aa 132,244,148
LX_t 224,13,16384
LX_aa 136,240,136
LX_t 226,13,18432
LX_aa 136,244,152
LX_t 228,13,20480
LX_aa 140,240,140
LX_t 230,13,22528
LX_aa 140,244,156
LX_w 4
LX_l 76,1,3072
LX_aa 144,248,128
LX_aa 144,252,144
LX_aa 148,248,132
buffer_load_dword a223, v22, s[84:87], 0 offen
LX_aa 148,252,148
LX_t 232,14,16384
v_add_u32 v22, 917504, v22
LX_aa 152,248,136
LX_t 234,14,18432
LX_aa 152,252,152
LX_t 236,14,20480
LX_aa 156,248,140
LX_t 238,14,22528
LX_aa 156,252,156
LX_w 4
s_mov_b32 s35, 5963776
s_add_u32 m0, s39, 8192
LX_aa 160,224,128
buffer_load_dwordx4 v2, s[16:19], s35 offen  lds
LX_aa 160,228,144
LX_aa 164,224,132
LX_aa 164,228,148
LX_t 240,15,16384
LX_aa 168,224,136
LX_t 242,15,18432
LX_aa 168,228,152
LX_t 244,15,20480
LX_aa 172,224,140
s_add_u32 m0, s39, 9216
LX_t 246,15,22528
LX_aa 172,228,156
LX_w 4
buffer_load_dwordx4 v3, s[16:19], s35 offen  lds
LX_aa 176,232,128
s_add_u32 m0, s39, 40960
LX_aa 176,236,144
buffer_load_dwordx4 v4, s[16:19], s35 offen  lds
LX_aa 180,232,132
LX_aa 180,236,148
LX_t 248,16,16384
LX_aa 184,232,136
LX_t 250,16,18432
LX_aa 184,236,152
LX_t 252,16,20480
LX_aa 188,232,140
LX_t 254,16,22528
LX_aa 188,236,156
LX_w 4
s_add_u32 m0, s39, 41984
LX_aa 192,240,128
buffer_load_dwordx4 v5, s[16:19], s35 offen  lds
LX_aa 192,244,144
s_add_u32 m0, s39, 73728
LX_aa 196,240,132
buffer_load_dwordx4 v6, s[16:19], s35 offen  lds
LX_aa 196,244,148
LX_t 224,17,8192
LX_aa 200,240,136
LX_t 226,17,9216
LX_aa 200,244,152
LX_t 228,17,10240
LX_aa 204,240,140
LX_t 230,17,11264
LX_aa 204,244,156
LX_w 4
LX_aa 208,248,128
s_add_u32 m0, s39, 74752
LX_aa 208,252,144
buffer_load_dwordx4 v7, s[16:19], s35 offen  lds
LX_aa 212,248,132
LX_aa 212,252,148
LX_t 232,20,8192
LX_aa 216,248,136
LX_t 234,20,9216
LX_aa 216,252,152
LX_t 236,20,10240
LX_aa 220,248,140
s_add_u32 m0, s45, 102400
LX_t 238,20,11264
LX_aa 220,252,156
LX_w 4
buffer_load_dwordx4 v8, s[16:19], s35 offen  lds
LX_aa 224,224,128
LX_aa 224,228,144
LX_aa 228,224,132
LX_aa 228,228,148
s_waitcnt vmcnt(40) lgkmcnt(0)
s_barrier
LX_t 240,9,24576
LX_aa 232,224,136
LX_t 242,9,26624
LX_aa 232,228,152
LX_t 244,9,28672
LX_aa 236,224,140
LX_t 246,9,30720
LX_aa 236,228,156
LX_aa 240,232,128
LX_aa 240,236,144
LX_aa 244,232,132
LX_aa 244,236,148
LX_t 248,10,24576
LX_aa 248,232,136
LX_t 250,10,26624
LX_aa 248,236,152
LX_t 252,10,28672
LX_aa 252,232,140
LX_t 254,10,30720
LX_aa 252,236,156
LX_w 4
s_waitcnt vmcnt(36)
LX_aa 32,240,160
LX_aa 32,244,176
LX_l 80,23,0
LX_aa 36,240,164
LX_aa 36,244,180
LX_t 224,11,24576
LX_aa 40,240,168
LX_t 226,11,26624
LX_aa 40,244,184
LX_t 228,11,28672
LX_aa 44,240,172
LX_t 230,11,30720
LX_aa 44,244,188
LX_w 4
LX_l 84,23,1024
LX_aa 48,248,160
LX_aa 48,252,176
LX_l 88,23,2048
LX_aa 52,248,164
LX_aa 52,252,180
LX_t 232,12,24576
LX_aa 56,248,168
LX_t 234,12,26624
LX_aa 56,252,184
LX_t 236,12,28672
LX_aa 60,248,172
LX_t 238,12,30720
LX_aa 60,252,188
LX_w 4
LX_aa 64,224,160
LX_l 92,23,3072
LX_aa 64,228,176
s_add_u32 s33, s38, 122880
LX_aa 68,224,164
LX_l 96,1,0
LX_aa 68,228,180
LX_t 240,9,57344
LX_aa 72,224,168
LX_t 242,9,59392
LX_aa 72,228,184
LX_t 244,9,61440
LX_aa 76,224,172
LX_t 246,9,63488
LX_aa 76,228,188
LX_w 4
LX_aa 80,232,160
LX_aa 80,236,176
LX_l 100,1,1024
LX_aa 84,232,164
LX_aa 84,236,180
LX_t 248,10,57344
LX_aa 88,232,168
LX_t 250,10,59392
LX_aa 88,236,184
LX_t 252,10,61440
LX_aa 92,232,172
LX_t 254,10,63488
LX_aa 92,236,188
LX_w 4
LX_l 104,1,2048
LX_aa 96,240,160
LX_aa 96,244,176
LX_aa 100,240,164
LX_l 108,1,3072
LX_aa 100,244,180
LX_t 224,11,57344
LX_aa 104,240,168
LX_t 226,11,59392
LX_aa 104,244,184
LX_t 228,11,61440
LX_aa 108,240,172
LX_t 230,11,63488
LX_aa 108,244,188
LX_w 4
LX_aa 112,248,160
LX_l 112,23,0
LX_aa 112,252,176
LX_aa 116,248,164
LX_aa 116,252,180
LX_t 232,12,57344
LX_aa 120,248,168
LX_t 234,12,59392
LX_aa 120,252,184
LX_t 236,12,61440
LX_aa 124,248,172
LX_t 238,12,63488
LX_aa 124,252,188
LX_w 4
LX_l 116,23,1024
LX_aa 128,224,160
LX_aa 128,228,176
LX_l 120,23,2048
LX_aa 132,224,164
LX_aa 132,228,180
LX_t 240,13,24576
LX_aa 136,224,168
LX_t 242,13,26624
LX_aa 136,228,184
LX_t 244,13,28672
LX_aa 140,224,172
LX_t 246,13,30720
LX_aa 140,228,188
LX_w 4
LX_l 124,23,3072
LX_aa 144,232,160
LX_aa 144,236,176
LX_aa 148,232,164
buffer_load_dword a223, v22, s[84:87], 0 offen
LX_aa 148,236,180
LX_t 248,14,24576
v_add_u32 v22, 917504, v22
LX_aa 152,232,168
LX_t 250,14,26624
LX_aa 152,236,184
LX_t 252,14,28672
LX_aa 156,232,172
LX_t 254,14,30720
LX_aa 156,236,188
LX_w 4
s_mov_b32 s35, 6422528
s_add_u32 m0, s39, 16384
LX_aa 160,240,160
buffer_load_dwordx4 v2, s[16:19], s35 offen  lds
LX_aa 160,244,176
LX_aa 164,240,164
LX_aa 164,244,180
LX_t 224,15,24576
LX_aa 168,240,168
LX_t 226,15,26624
LX_aa 168,244,184
LX_t 228,15,28672
LX_aa 172,240,172
s_add_u32 m0, s39, 17408
LX_t 230,15,30720
LX_aa 172,244,188
LX_w 4
buffer_load_dwordx4 v3, s[16:19], s35 offen  lds
LX_aa 176,248,160
s_add_u32 m0, s39, 49152
LX_aa 176,252,176
buffer_load_dwordx4 v4, s[16:19], s35 offen  lds
LX_aa 180,248,164
LX_aa 180,252,180
LX_t 232,16,24576
LX_aa 184,248,168
LX_t 234,16,26624
LX_aa 184,252,184
LX_t 236,16,28672
LX_aa 188,248,172
LX_t 238,16,30720
LX_aa 188,252,188
LX_w 4
s_add_u32 m0, s39, 50176
LX_aa 192,224,160
buffer_load_dwordx4 v5, s[16:19], s35 offen  lds
LX_aa 192,228,176
s_add_u32 m0, s39, 81920
LX_aa 196,224,164
buffer_load_dwordx4 v6, s[16:19], s35 offen  lds
LX_aa 196,228,180
LX_t 240,17,12288
LX_aa 200,224,168
LX_t 242,17,13312
LX_aa 200,228,184
LX_t 244,17,14336
LX_aa 204,224,172
LX_t 246,17,15360
LX_aa 204,228,188
LX_w 4
LX_aa 208,232,160
s_add_u32 m0, s39, 82944
LX_aa 208,236,176
buffer_load_dwordx4 v7, s[16:19], s35 offen  lds
LX_aa 212,232,164
LX_aa 212,236,180
LX_t 248,20,12288
LX_aa 216,232,168
LX_t 250,20,13312
LX_aa 216,236,184
LX_t 252,20,14336
LX_aa 220,232,172
s_add_u32 m0, s45, 106496
LX_t 254,20,15360
LX_aa 220,236,188
LX_w 4
buffer_load_dwordx4 v8, s[16:19], s35 offen  lds
LX_aa 224,240,160
LX_aa 224,244,176
LX_aa 228,240,164
LX_aa 228,244,180
s_waitcnt vmcnt(40) lgkmcnt(0)
s_barrier
LX_t 224,9,0
LX_aa 232,240,168
LX_t 226,9,2048
LX_aa 232,244,184
LX_t 228,9,4096
LX_aa 236,240,172
s_mov_b32 s35, 6881280
s_add_u32 m0, s39, 24576
LX_t 230,9,6144
LX_aa 236,244,188
buffer_load_dwordx4 v2, s[16:19], s35 offen  lds
LX_aa 240,248,160
s_add_u32 m0, s39, 25600
LX_aa 240,252,176
buffer_load_dwordx4 v3, s[16:19], s35 offen  lds
LX_aa 244,248,164
LX_aa 244,252,180
LX_t 232,10,0
LX_aa 248,248,168
LX_t 234,10,2048
LX_aa 248,252,184
LX_t 236,10,4096
LX_aa 252,248,172
s_add_u32 m0, s39, 57344
LX_t 238,10,6144
LX_aa 252,252,188
LX_w 4
s_waitcnt vmcnt(50)
buffer_load_dwordx4 v4, s[16:19], s35 offen  lds
LX_aa 32,224,0
LX_aa 32,228,16
s_add_u32 m0, s39, 58368
LX_aa 36,224,4
buffer_load_dwordx4 v5, s[16:19], s35 offen  lds
LX_aa 36,228,20
LX_t 240,11,0
LX_aa 40,224,8
LX_t 242,11,2048
LX_aa 40,228,24
LX_t 244,11,4096
LX_aa 44,224,12
LX_t 246,11,6144
LX_aa 44,228,28
LX_w 4
s_add_u32 m0, s39, 90112
LX_aa 48,232,0
buffer_load_dwordx4 v6, s[16:19], s35 offen  lds
LX_aa 48,236,16
LX_aa 52,232,4
LX_aa 52,236,20
LX_t 248,12,0
LX_aa 56,232,8
LX_t 250,12,2048
LX_aa 56,236,24
LX_t 252,12,4096
LX_aa 60,232,12
s_add_u32 m0, s39, 91136
LX_t 254,12,6144
LX_aa 60,236,28
LX_w 4
buffer_load_dwordx4 v7, s[16:19], s35 offen  lds
LX_aa 64,240,0
s_add_u32 m0, s45, 110592
LX_aa 64,244,16
buffer_load_dwordx4 v8, s[16:19], s35 offen  lds
LX_aa 68,240,4
LX_aa 68,244,20
s_waitcnt vmcnt(27) lgkmcnt(0)
s_barrier
LX_t 224,9,8192
LX_aa 72,240,8
LX_t 226,9,10240
LX_aa 72,244,24
LX_t 228,9,12288
LX_aa 76,240,12
LX_t 230,9,14336
LX_aa 76,244,28
LX_aa 80,248,0
LX_aa 80,252,16
LX_aa 84,248,4
LX_aa 84,252,20
LX_t 232,10,8192
LX_aa 88,248,8
LX_t 234,10,10240
LX_aa 88,252,24
LX_t 236,10,12288
LX_aa 92,248,12
LX_t 238,10,14336
LX_aa 92,252,28
LX_w 4
s_waitcnt vmcnt(39)
LX_aa 32,224,32
v_bfe_u32 v31, %4, 4, 2
v_lshlrev_b32 v31, 8, v31
LX_aa 32,228,48
v_add_u32 v22, 128, v31
v_lshrrev_b32 v24, 1, v31
LX_aa 36,224,36
v_add_u32 v25, 1536, v24
v_add_u32 v24, 1024, v24
LX_aa 36,228,52
s_lshr_b32 s50, %2, 1
s_lshl_b32 s50, s50, 11
LX_t 240,11,8192
LX_aa 40,224,40
s_and_b32 s51, %2, 1
s_lshl_b32 s51, s51, 6
LX_t 242,11,10240
LX_aa 40,228,56
s_or_b32 s50, s50, s51
s_or_b32 s50, s50, 114688
LX_t 244,11,12288
LX_aa 44,224,44
LX_t 246,11,14336
v_xor_b32 v2, s50, v9
LX_aa 44,228,60
LX_w 4
s_add_u32 s35, s92, 0
s_add_u32 m0, s48, 114688
LX_aa 48,232,32
buffer_load_dwordx4 v28, s[28:31], s35 offen offset:0  lds
v_xor_b32 v3, 32, v2
LX_aa 48,236,48
v_xor_b32 v4, 64, v2
v_xor_b32 v5, 96, v2
LX_aa 52,232,36
LX_F0_pad6:
s_cbranch_execz LX_F0_pad9
v_xor_b32 v6, 2048, v2
v_xor_b32 v7, 2080, v2
LX_aa 52,236,52
LX_t 248,12,8192
v_xor_b32 v8, 2112, v2
LX_aa 56,232,40
LX_t 250,12,10240
v_xor_b32 v21, 2144, v2
LX_aa 56,236,56
LX_t 252,12,12288
LX_aa 60,232,44
s_add_u32 m0, s48, 116608
LX_t 254,12,14336
LX_aa 60,236,60
LX_w 4
buffer_load_dwordx4 v28, s[28:31], s35 offen offset:128  lds
LX_aa 64,240,32
LX_aa 64,244,48
s_add_u32 m0, s48, 115712
LX_aa 68,240,36
buffer_load_dwordx4 v29, s[28:31], s35 offen offset:0  lds
LX_aa 68,244,52
s_waitcnt vmcnt(10) lgkmcnt(0)
s_barrier
LX_t 224,9,16384
LX_aa 72,240,40
LX_t 226,9,18432
LX_aa 72,244,56
LX_t 228,9,20480
LX_aa 76,240,44
LX_t 230,9,22528
LX_aa 76,244,60
LX_aa 80,248,32
s_add_u32 m0, s48, 117632
LX_aa 80,252,48
buffer_load_dwordx4 v29, s[28:31], s35 offen offset:128  lds
LX_aa 84,248,36
LX_aa 84,252,52
LX_t 232,10,16384
LX_aa 88,248,40
LX_t 234,10,18432
LX_aa 88,252,56
LX_t 236,10,20480
LX_aa 92,248,44
LX_t 238,10,22528
LX_aa 92,252,60
LX_w 4
s_waitcnt vmcnt(27)
s_add_u32 s35, s92, 131072
s_add_u32 m0, s48, 131072
LX_aa 32,224,64
buffer_load_dwordx4 v28, s[28:31], s35 offen offset:0  lds
LX_aa 32,228,80
LX_aa 36,224,68
LX_aa 36,228,84
LX_t 240,11,16384
LX_aa 40,224,72
LX_t 242,11,18432
LX_aa 40,228,88
LX_t 244,11,20480
LX_aa 44,224,76
s_add_u32 m0, s48, 132992
LX_t 246,11,22528
LX_aa 44,228,92
LX_w 4
buffer_load_dwordx4 v28, s[28:31], s35 offen offset:128  lds
LX_aa 48,232,64
LX_aa 48,236,80
s_add_u32 m0, s48, 132096
LX_aa 52,232,68
)ASM"
R"ASM(buffer_load_dwordx4 v29, s[28:31], s35 offen offset:0  lds
LX_aa 52,236,84
LX_t 248,12,16384
LX_aa 56,232,72
LX_t 250,12,18432
LX_aa 56,236,88
LX_t 252,12,20480
LX_aa 60,232,76
LX_t 254,12,22528
LX_aa 60,236,92
LX_w 4
LX_aa 64,240,64
s_add_u32 m0, s48, 134016
LX_aa 64,244,80
buffer_load_dwordx4 v29, s[28:31], s35 offen offset:128  lds
LX_aa 68,240,68
LX_aa 68,244,84
s_waitcnt vmcnt(8) lgkmcnt(0)
s_barrier
LX_t 224,9,24576
LX_aa 72,240,72
LX_t 226,9,26624
LX_aa 72,244,88
LX_t 228,9,28672
LX_aa 76,240,76
LX_t 230,9,30720
LX_aa 76,244,92
s_add_u32 s35, s92, 262144
s_add_u32 m0, s48, 147456
LX_aa 80,248,64
buffer_load_dwordx4 v28, s[28:31], s35 offen offset:0  lds
LX_aa 80,252,80
LX_aa 84,248,68
LX_aa 84,252,84
LX_t 232,10,24576
LX_aa 88,248,72
LX_t 234,10,26624
LX_aa 88,252,88
LX_t 236,10,28672
LX_aa 92,248,76
s_add_u32 m0, s48, 149376
LX_t 238,10,30720
LX_aa 92,252,92
LX_w 4
s_waitcnt vmcnt(24)
buffer_load_dwordx4 v28, s[28:31], s35 offen offset:128  lds
LX_aa 32,224,96
LX_aa 32,228,112
s_add_u32 m0, s48, 148480
LX_aa 36,224,100
buffer_load_dwordx4 v29, s[28:31], s35 offen offset:0  lds
LX_aa 36,228,116
LX_t 240,11,24576
LX_aa 40,224,104
LX_t 242,11,26624
LX_aa 40,228,120
LX_t 244,11,28672
LX_aa 44,224,108
LX_t 246,11,30720
LX_aa 44,228,124
LX_w 4
LX_aa 48,232,96
s_add_u32 m0, s48, 150400
LX_aa 48,236,112
buffer_load_dwordx4 v29, s[28:31], s35 offen offset:128  lds
LX_aa 52,232,100
LX_aa 52,236,116
LX_t 248,12,24576
LX_aa 56,232,104
LX_t 250,12,26624
LX_aa 56,236,120
LX_t 252,12,28672
LX_aa 60,232,108
LX_t 254,12,30720
LX_aa 60,236,124
LX_w 4
LX_aa 64,240,96
LX_aa 64,244,112
LX_aa 68,240,100
LX_aa 68,244,116
LX_w 0
s_barrier
LX_t 224,9,32768
LX_aa 72,240,104
LX_t 226,9,34816
LX_aa 72,244,120
s_add_u32 s49, s88, 0
LX_t 228,9,36864
v_mul_f32_e64 v32, v32, |v32|
LX_aa 76,240,108
s_add_u32 s35, s93, 0
s_add_u32 m0, s48, 0
LX_t 230,9,38912
LX_aa 76,244,124
buffer_load_dwordx4 v28, s[28:31], s35 offen offset:0  lds
v_mul_f32_e64 v33, v33, |v33|
LX_aa 80,248,96
v_mul_f32_e64 v34, v34, |v34|
v_mul_f32_e64 v35, v35, |v35|
LX_aa 80,252,112
s_add_u32 m0, s48, 1920
v_mul_f32_e64 v48, v48, |v48|
v_mul_f32_e64 v49, v49, |v49|
LX_aa 84,248,100
buffer_load_dwordx4 v28, s[28:31], s35 offen offset:128  lds
v_mul_f32_e64 v50, v50, |v50|
LX_aa 84,252,116
LX_t 232,10,32768
v_mul_f32_e64 v51, v51, |v51|
LX_aa 88,248,104
LX_t 234,10,34816
LX_c 32,32,33
LX_aa 88,252,120
LX_t 236,10,36864
LX_c 33,34,35
LX_aa 92,248,108
LX_t 238,10,38912
LX_c 34,48,49
LX_aa 92,252,124
LX_w 4
LX_c 35,50,51
v_pk_max_i16 v32, v32, 0
LX_aa 96,224,0
v_pk_max_i16 v33, v33, 0
v_pk_max_i16 v34, v34, 0
LX_aa 96,228,16
s_add_u32 m0, s48, 1024
v_pk_max_i16 v35, v35, 0
LX_aa 100,224,4
buffer_load_dwordx4 v29, s[28:31], s35 offen offset:0  lds
LX_aa 100,228,20
LX_t 240,9,40960
LX_aa 104,224,8
LX_t 242,9,43008
LX_aa 104,228,24
LX_t 244,9,45056
LX_aa 108,224,12
LX_t 246,9,47104
LX_aa 108,228,28
LX_w 4
buffer_store_dwordx4 v[32:35], v1, s[20:23], s49 offen offset:0 sc1
v_mul_f32_e64 v36, v36, |v36|
LX_aa 112,232,0
s_add_u32 m0, s48, 2944
v_mul_f32_e64 v37, v37, |v37|
v_mul_f32_e64 v38, v38, |v38|
LX_aa 112,236,16
buffer_load_dwordx4 v29, s[28:31], s35 offen offset:128  lds
v_mul_f32_e64 v39, v39, |v39|
LX_aa 116,232,4
v_mul_f32_e64 v52, v52, |v52|
v_mul_f32_e64 v53, v53, |v53|
LX_aa 116,236,20
LX_t 248,10,40960
v_mul_f32_e64 v54, v54, |v54|
LX_aa 120,232,8
LX_t 250,10,43008
v_mul_f32_e64 v55, v55, |v55|
LX_aa 120,236,24
LX_t 252,10,45056
LX_c 36,36,37
LX_aa 124,232,12
LX_t 254,10,47104
LX_c 37,38,39
LX_aa 124,236,28
LX_w 4
LX_c 38,52,53
LX_c 39,54,55
LX_aa 96,240,32
s_add_u32 s35, s94, 0
s_add_u32 m0, s48, 16384
v_pk_max_i16 v36, v36, 0
LX_aa 96,244,48
buffer_load_dwordx4 v28, s[28:31], s35 offen offset:0  lds
v_pk_max_i16 v37, v37, 0
LX_aa 100,240,36
v_pk_max_i16 v38, v38, 0
v_pk_max_i16 v39, v39, 0
LX_aa 100,244,52
LX_t 224,9,49152
LX_aa 104,240,40
LX_t 226,9,51200
LX_aa 104,244,56
LX_t 228,9,53248
LX_aa 108,240,44
LX_t 230,9,55296
LX_aa 108,244,60
LX_w 4
s_add_u32 m0, s48, 18304
buffer_store_dwordx4 v[36:39], v1, s[20:23], s49 offen offset:1024 sc1
LX_aa 112,248,32
buffer_load_dwordx4 v28, s[28:31], s35 offen offset:128  lds
v_mul_f32_e64 v40, v40, |v40|
LX_aa 112,252,48
v_mul_f32_e64 v41, v41, |v41|
v_mul_f32_e64 v42, v42, |v42|
LX_aa 116,248,36
v_mul_f32_e64 v43, v43, |v43|
v_mul_f32_e64 v56, v56, |v56|
LX_aa 116,252,52
LX_t 232,10,49152
v_mul_f32_e64 v57, v57, |v57|
LX_aa 120,248,40
LX_t 234,10,51200
v_mul_f32_e64 v58, v58, |v58|
LX_aa 120,252,56
LX_t 236,10,53248
v_mul_f32_e64 v59, v59, |v59|
LX_aa 124,248,44
LX_t 238,10,55296
LX_c 40,40,41
LX_aa 124,252,60
LX_w 4
s_add_u32 m0, s48, 17408
LX_c 41,42,43
LX_aa 96,224,64
buffer_load_dwordx4 v29, s[28:31], s35 offen offset:0  lds
LX_c 42,56,57
LX_aa 96,228,80
LX_c 43,58,59
v_pk_max_i16 v40, v40, 0
LX_aa 100,224,68
v_pk_max_i16 v41, v41, 0
v_pk_max_i16 v42, v42, 0
LX_aa 100,228,84
LX_t 240,9,57344
v_pk_max_i16 v43, v43, 0
LX_aa 104,224,72
LX_t 242,9,59392
LX_aa 104,228,88
LX_t 244,9,61440
LX_aa 108,224,76
s_add_u32 m0, s48, 19328
LX_t 246,9,63488
LX_aa 108,228,92
LX_w 4
buffer_load_dwordx4 v29, s[28:31], s35 offen offset:128  lds
LX_aa 112,232,64
buffer_store_dwordx4 v[40:43], v1, s[20:23], s49 offen offset:2048 sc1
v_mul_f32_e64 v44, v44, |v44|
LX_aa 112,236,80
v_mul_f32_e64 v45, v45, |v45|
v_mul_f32_e64 v46, v46, |v46|
LX_aa 116,232,68
v_mul_f32_e64 v47, v47, |v47|
v_mul_f32_e64 v60, v60, |v60|
LX_aa 116,236,84
LX_t 248,10,57344
v_mul_f32_e64 v61, v61, |v61|
LX_aa 120,232,72
LX_t 250,10,59392
v_mul_f32_e64 v62, v62, |v62|
LX_aa 120,236,88
LX_t 252,10,61440
v_mul_f32_e64 v63, v63, |v63|
LX_aa 124,232,76
LX_t 254,10,63488
LX_c 44,44,45
LX_aa 124,236,92
LX_w 4
LX_c 45,46,47
LX_c 46,60,61
LX_aa 96,240,96
LX_c 47,62,63
v_pk_max_i16 v44, v44, 0
LX_aa 96,244,112
v_pk_max_i16 v45, v45, 0
v_pk_max_i16 v46, v46, 0
LX_aa 100,240,100
v_pk_max_i16 v47, v47, 0
LX_aa 100,244,116
LX_t 224,11,32768
LX_aa 104,240,104
LX_t 226,11,34816
LX_aa 104,244,120
LX_t 228,11,36864
LX_aa 108,240,108
LX_t 230,11,38912
LX_aa 108,244,124
LX_w 4
buffer_store_dwordx4 v[44:47], v1, s[20:23], s49 offen offset:3072 sc1
s_add_u32 s49, s88, 4096
LX_aa 112,248,96
v_mul_f32_e64 v64, v64, |v64|
v_mul_f32_e64 v65, v65, |v65|
LX_aa 112,252,112
v_mul_f32_e64 v66, v66, |v66|
v_mul_f32_e64 v67, v67, |v67|
LX_aa 116,248,100
v_mul_f32_e64 v80, v80, |v80|
v_mul_f32_e64 v81, v81, |v81|
LX_aa 116,252,116
LX_t 232,12,32768
v_mul_f32_e64 v82, v82, |v82|
LX_aa 120,248,104
LX_t 234,12,34816
v_mul_f32_e64 v83, v83, |v83|
LX_aa 120,252,120
LX_t 236,12,36864
LX_c 64,64,65
LX_aa 124,248,108
LX_t 238,12,38912
LX_c 65,66,67
LX_aa 124,252,124
LX_w 4
LX_c 66,80,81
LX_c 67,82,83
LX_aa 128,224,0
v_pk_max_i16 v64, v64, 0
v_pk_max_i16 v65, v65, 0
LX_aa 128,228,16
v_pk_max_i16 v66, v66, 0
v_pk_max_i16 v67, v67, 0
LX_aa 132,224,4
buffer_store_dwordx4 v[64:67], v1, s[20:23], s49 offen offset:0 sc1
v_mul_f32_e64 v68, v68, |v68|
LX_aa 132,228,20
LX_t 240,11,40960
v_mul_f32_e64 v69, v69, |v69|
LX_aa 136,224,8
LX_t 242,11,43008
v_mul_f32_e64 v70, v70, |v70|
LX_aa 136,228,24
LX_t 244,11,45056
v_mul_f32_e64 v71, v71, |v71|
LX_aa 140,224,12
LX_t 246,11,47104
v_mul_f32_e64 v84, v84, |v84|
LX_aa 140,228,28
LX_w 4
v_mul_f32_e64 v85, v85, |v85|
v_mul_f32_e64 v86, v86, |v86|
LX_aa 144,232,0
v_mul_f32_e64 v87, v87, |v87|
LX_c 68,68,69
LX_aa 144,236,16
LX_c 69,70,71
LX_c 70,84,85
LX_aa 148,232,4
LX_c 71,86,87
v_pk_max_i16 v68, v68, 0
LX_aa 148,236,20
LX_t 248,12,40960
v_pk_max_i16 v69, v69, 0
LX_aa 152,232,8
LX_t 250,12,43008
v_pk_max_i16 v70, v70, 0
LX_aa 152,236,24
LX_t 252,12,45056
v_pk_max_i16 v71, v71, 0
LX_aa 156,232,12
LX_t 254,12,47104
LX_aa 156,236,28
LX_w 4
buffer_store_dwordx4 v[68:71], v1, s[20:23], s49 offen offset:1024 sc1
v_mul_f32_e64 v72, v72, |v72|
LX_aa 128,240,32
v_mul_f32_e64 v73, v73, |v73|
v_mul_f32_e64 v74, v74, |v74|
LX_aa 128,244,48
v_mul_f32_e64 v75, v75, |v75|
v_mul_f32_e64 v88, v88, |v88|
LX_aa 132,240,36
v_mul_f32_e64 v89, v89, |v89|
v_mul_f32_e64 v90, v90, |v90|
LX_aa 132,244,52
LX_t 224,11,49152
v_mul_f32_e64 v91, v91, |v91|
LX_aa 136,240,40
LX_t 226,11,51200
LX_c 72,72,73
LX_aa 136,244,56
LX_t 228,11,53248
LX_c 73,74,75
LX_aa 140,240,44
LX_t 230,11,55296
LX_c 74,88,89
LX_aa 140,244,60
LX_w 4
LX_c 75,90,91
v_pk_max_i16 v72, v72, 0
LX_aa 144,248,32
v_pk_max_i16 v73, v73, 0
v_pk_max_i16 v74, v74, 0
LX_aa 144,252,48
v_pk_max_i16 v75, v75, 0
LX_aa 148,248,36
buffer_store_dwordx4 v[72:75], v1, s[20:23], s49 offen offset:2048 sc1
v_mul_f32_e64 v76, v76, |v76|
LX_aa 148,252,52
LX_t 232,12,49152
v_mul_f32_e64 v77, v77, |v77|
LX_aa 152,248,40
LX_t 234,12,51200
v_mul_f32_e64 v78, v78, |v78|
LX_aa 152,252,56
LX_t 236,12,53248
v_mul_f32_e64 v79, v79, |v79|
LX_aa 156,248,44
LX_t 238,12,55296
v_mul_f32_e64 v92, v92, |v92|
LX_aa 156,252,60
LX_w 4
v_mul_f32_e64 v93, v93, |v93|
v_mul_f32_e64 v94, v94, |v94|
LX_aa 128,224,64
v_mul_f32_e64 v95, v95, |v95|
LX_c 76,76,77
LX_aa 128,228,80
LX_c 77,78,79
LX_c 78,92,93
LX_aa 132,224,68
LX_c 79,94,95
v_pk_max_i16 v76, v76, 0
LX_aa 132,228,84
LX_t 240,11,57344
v_pk_max_i16 v77, v77, 0
LX_aa 136,224,72
LX_t 242,11,59392
v_pk_max_i16 v78, v78, 0
LX_aa 136,228,88
LX_t 244,11,61440
v_pk_max_i16 v79, v79, 0
LX_aa 140,224,76
LX_t 246,11,63488
LX_aa 140,228,92
LX_w 4
buffer_store_dwordx4 v[76:79], v1, s[20:23], s49 offen offset:3072 sc1
s_add_u32 s49, s88, 8192
LX_aa 144,232,64
v_mul_f32_e64 v96, v96, |v96|
v_mul_f32_e64 v97, v97, |v97|
LX_aa 144,236,80
v_mul_f32_e64 v98, v98, |v98|
v_mul_f32_e64 v99, v99, |v99|
LX_aa 148,232,68
v_mul_f32_e64 v112, v112, |v112|
v_mul_f32_e64 v113, v113, |v113|
LX_aa 148,236,84
LX_t 248,12,57344
v_mul_f32_e64 v114, v114, |v114|
LX_aa 152,232,72
LX_t 250,12,59392
v_mul_f32_e64 v115, v115, |v115|
LX_aa 152,236,88
LX_t 252,12,61440
LX_c 96,96,97
LX_aa 156,232,76
LX_t 254,12,63488
LX_c 97,98,99
LX_aa 156,236,92
LX_w 4
LX_c 98,112,113
LX_c 99,114,115
LX_aa 128,240,96
v_pk_max_i16 v96, v96, 0
v_pk_max_i16 v97, v97, 0
LX_aa 128,244,112
LX_F0_pad7:
s_cbranch_execz LX_F0_pad10
v_pk_max_i16 v98, v98, 0
v_pk_max_i16 v99, v99, 0
LX_aa 132,240,100
buffer_store_dwordx4 v[96:99], v1, s[20:23], s49 offen offset:0 sc1
v_mul_f32_e64 v100, v100, |v100|
LX_aa 132,244,116
LX_w 0
s_barrier
LX_t 224,13,0
LX_aa 136,240,104
LX_t 226,13,2048
v_mul_f32_e64 v101, v101, |v101|
LX_aa 136,244,120
LX_t 228,13,4096
v_mul_f32_e64 v102, v102, |v102|
LX_aa 140,240,108
s_add_u32 s35, s95, 0
s_add_u32 m0, s48, 32768
LX_t 230,13,6144
LX_aa 140,244,124
buffer_load_dwordx4 v28, s[28:31], s35 offen offset:0  lds
v_mul_f32_e64 v103, v103, |v103|
LX_aa 144,248,96
v_mul_f32_e64 v116, v116, |v116|
v_mul_f32_e64 v117, v117, |v117|
LX_aa 144,252,112
s_add_u32 m0, s48, 34688
v_mul_f32_e64 v118, v118, |v118|
v_mul_f32_e64 v119, v119, |v119|
LX_aa 148,248,100
buffer_load_dwordx4 v28, s[28:31], s35 offen offset:128  lds
LX_c 100,100,101
LX_aa 148,252,116
LX_t 232,14,0
LX_c 101,102,103
LX_aa 152,248,104
LX_t 234,14,2048
LX_c 102,116,117
LX_aa 152,252,120
LX_t 236,14,4096
LX_c 103,118,119
LX_aa 156,248,108
LX_t 238,14,6144
v_pk_max_i16 v100, v100, 0
LX_aa 156,252,124
LX_w 4
v_pk_max_i16 v101, v101, 0
v_pk_max_i16 v102, v102, 0
LX_aa 160,224,0
v_pk_max_i16 v103, v103, 0
LX_aa 160,228,16
s_add_u32 m0, s48, 33792
buffer_store_dwordx4 v[100:103], v1, s[20:23], s49 offen offset:1024 sc1
v_mul_f32_e64 v104, v104, |v104|
LX_aa 164,224,4
buffer_load_dwordx4 v29, s[28:31], s35 offen offset:0  lds
v_mul_f32_e64 v105, v105, |v105|
LX_aa 164,228,20
LX_t 240,13,8192
v_mul_f32_e64 v106, v106, |v106|
LX_aa 168,224,8
LX_t 242,13,10240
v_mul_f32_e64 v107, v107, |v107|
LX_aa 168,228,24
LX_t 244,13,12288
v_mul_f32_e64 v120, v120, |v120|
LX_aa 172,224,12
LX_t 246,13,14336
v_mul_f32_e64 v121, v121, |v121|
LX_aa 172,228,28
LX_w 4
v_mul_f32_e64 v122, v122, |v122|
v_mul_f32_e64 v123, v123, |v123|
LX_aa 176,232,0
s_add_u32 m0, s48, 35712
LX_c 104,104,105
LX_c 105,106,107
LX_aa 176,236,16
buffer_load_dwordx4 v29, s[28:31], s35 offen offset:128  lds
LX_c 106,120,121
LX_aa 180,232,4
LX_c 107,122,123
v_pk_max_i16 v104, v104, 0
LX_aa 180,236,20
LX_t 248,14,8192
v_pk_max_i16 v105, v105, 0
)ASM"
R"ASM(LX_aa 184,232,8
LX_t 250,14,10240
v_pk_max_i16 v106, v106, 0
LX_aa 184,236,24
LX_t 252,14,12288
v_pk_max_i16 v107, v107, 0
LX_aa 188,232,12
LX_t 254,14,14336
LX_aa 188,236,28
LX_w 4
buffer_store_dwordx4 v[104:107], v1, s[20:23], s49 offen offset:2048 sc1
v_mul_f32_e64 v108, v108, |v108|
LX_aa 160,240,32
s_add_u32 s35, s93, 131072
s_add_u32 m0, s48, 49152
v_mul_f32_e64 v109, v109, |v109|
LX_aa 160,244,48
buffer_load_dwordx4 v28, s[28:31], s35 offen offset:0  lds
v_mul_f32_e64 v110, v110, |v110|
LX_aa 164,240,36
v_mul_f32_e64 v111, v111, |v111|
v_mul_f32_e64 v124, v124, |v124|
LX_aa 164,244,52
LX_t 224,13,16384
v_mul_f32_e64 v125, v125, |v125|
LX_aa 168,240,40
LX_t 226,13,18432
v_mul_f32_e64 v126, v126, |v126|
LX_aa 168,244,56
LX_t 228,13,20480
v_mul_f32_e64 v127, v127, |v127|
LX_aa 172,240,44
LX_t 230,13,22528
LX_c 108,108,109
LX_aa 172,244,60
LX_w 4
s_add_u32 m0, s48, 51072
LX_c 109,110,111
LX_aa 176,248,32
buffer_load_dwordx4 v28, s[28:31], s35 offen offset:128  lds
LX_c 110,124,125
LX_aa 176,252,48
LX_c 111,126,127
v_pk_max_i16 v108, v108, 0
LX_aa 180,248,36
v_pk_max_i16 v109, v109, 0
v_pk_max_i16 v110, v110, 0
LX_aa 180,252,52
LX_t 232,14,16384
v_pk_max_i16 v111, v111, 0
LX_aa 184,248,40
LX_t 234,14,18432
LX_aa 184,252,56
LX_t 236,14,20480
LX_aa 188,248,44
LX_t 238,14,22528
LX_aa 188,252,60
LX_w 4
s_add_u32 m0, s48, 50176
buffer_store_dwordx4 v[108:111], v1, s[20:23], s49 offen offset:3072 sc1
LX_aa 160,224,64
s_add_u32 s49, s88, 12288
buffer_load_dwordx4 v29, s[28:31], s35 offen offset:0  lds
v_mul_f32_e64 v128, v128, |v128|
LX_aa 160,228,80
v_mul_f32_e64 v129, v129, |v129|
v_mul_f32_e64 v130, v130, |v130|
LX_aa 164,224,68
v_mul_f32_e64 v131, v131, |v131|
v_mul_f32_e64 v144, v144, |v144|
LX_aa 164,228,84
LX_t 240,13,24576
v_mul_f32_e64 v145, v145, |v145|
LX_aa 168,224,72
LX_t 242,13,26624
v_mul_f32_e64 v146, v146, |v146|
LX_aa 168,228,88
LX_t 244,13,28672
v_mul_f32_e64 v147, v147, |v147|
LX_aa 172,224,76
s_add_u32 m0, s48, 52096
LX_t 246,13,30720
LX_c 128,128,129
LX_aa 172,228,92
LX_w 4
buffer_load_dwordx4 v29, s[28:31], s35 offen offset:128  lds
LX_c 129,130,131
LX_aa 176,232,64
s_waitcnt vmcnt(12)
buffer_store_dwordx2 v[26:27], v30, s[20:23], s82 offen offset:1024 sc1
LX_c 130,144,145
LX_aa 176,236,80
LX_c 131,146,147
v_pk_max_i16 v128, v128, 0
LX_aa 180,232,68
v_pk_max_i16 v129, v129, 0
v_pk_max_i16 v130, v130, 0
LX_aa 180,236,84
LX_t 248,14,24576
v_pk_max_i16 v131, v131, 0
LX_aa 184,232,72
LX_t 250,14,26624
LX_aa 184,236,88
LX_t 252,14,28672
LX_aa 188,232,76
LX_t 254,14,30720
LX_aa 188,236,92
LX_w 4
buffer_store_dwordx4 v[128:131], v1, s[20:23], s49 offen offset:0 sc1
v_mul_f32_e64 v132, v132, |v132|
LX_aa 160,240,96
v_mul_f32_e64 v133, v133, |v133|
v_mul_f32_e64 v134, v134, |v134|
LX_aa 160,244,112
v_mul_f32_e64 v135, v135, |v135|
v_mul_f32_e64 v148, v148, |v148|
LX_aa 164,240,100
v_mul_f32_e64 v149, v149, |v149|
v_mul_f32_e64 v150, v150, |v150|
LX_aa 164,244,116
LX_t 224,15,0
v_mul_f32_e64 v151, v151, |v151|
LX_aa 168,240,104
LX_t 226,15,2048
LX_c 132,132,133
LX_aa 168,244,120
LX_t 228,15,4096
LX_c 133,134,135
LX_aa 172,240,108
LX_t 230,15,6144
LX_c 134,148,149
LX_aa 172,244,124
LX_w 4
LX_c 135,150,151
v_pk_max_i16 v132, v132, 0
LX_aa 176,248,96
v_pk_max_i16 v133, v133, 0
v_pk_max_i16 v134, v134, 0
LX_aa 176,252,112
v_pk_max_i16 v135, v135, 0
LX_aa 180,248,100
buffer_store_dwordx4 v[132:135], v1, s[20:23], s49 offen offset:1024 sc1
v_mul_f32_e64 v136, v136, |v136|
LX_aa 180,252,116
LX_t 232,16,0
v_mul_f32_e64 v137, v137, |v137|
LX_aa 184,248,104
LX_t 234,16,2048
v_mul_f32_e64 v138, v138, |v138|
LX_aa 184,252,120
LX_t 236,16,4096
v_mul_f32_e64 v139, v139, |v139|
LX_aa 188,248,108
LX_t 238,16,6144
v_mul_f32_e64 v152, v152, |v152|
LX_aa 188,252,124
LX_w 4
v_mul_f32_e64 v153, v153, |v153|
v_mul_f32_e64 v154, v154, |v154|
LX_aa 192,224,0
v_mul_f32_e64 v155, v155, |v155|
LX_c 136,136,137
LX_aa 192,228,16
LX_c 137,138,139
LX_c 138,152,153
LX_aa 196,224,4
LX_c 139,154,155
v_pk_max_i16 v136, v136, 0
LX_aa 196,228,20
LX_t 240,15,8192
v_pk_max_i16 v137, v137, 0
LX_aa 200,224,8
LX_t 242,15,10240
v_pk_max_i16 v138, v138, 0
LX_aa 200,228,24
LX_t 244,15,12288
v_pk_max_i16 v139, v139, 0
LX_aa 204,224,12
LX_t 246,15,14336
LX_aa 204,228,28
LX_w 4
buffer_store_dwordx4 v[136:139], v1, s[20:23], s49 offen offset:2048 sc1
v_mul_f32_e64 v140, v140, |v140|
LX_aa 208,232,0
v_mul_f32_e64 v141, v141, |v141|
v_mul_f32_e64 v142, v142, |v142|
LX_aa 208,236,16
v_mul_f32_e64 v143, v143, |v143|
v_mul_f32_e64 v156, v156, |v156|
LX_aa 212,232,4
v_mul_f32_e64 v157, v157, |v157|
v_mul_f32_e64 v158, v158, |v158|
LX_aa 212,236,20
LX_t 248,16,8192
v_mul_f32_e64 v159, v159, |v159|
LX_aa 216,232,8
LX_t 250,16,10240
LX_c 140,140,141
LX_aa 216,236,24
LX_t 252,16,12288
LX_c 141,142,143
LX_aa 220,232,12
LX_t 254,16,14336
LX_c 142,156,157
LX_aa 220,236,28
LX_w 4
LX_c 143,158,159
v_pk_max_i16 v140, v140, 0
LX_aa 192,240,32
v_pk_max_i16 v141, v141, 0
v_pk_max_i16 v142, v142, 0
LX_aa 192,244,48
v_pk_max_i16 v143, v143, 0
LX_aa 196,240,36
buffer_store_dwordx4 v[140:143], v1, s[20:23], s49 offen offset:3072 sc1
s_add_u32 s49, s88, 16384
v_mul_f32_e64 v160, v160, |v160|
LX_aa 196,244,52
LX_t 224,15,16384
v_mul_f32_e64 v161, v161, |v161|
LX_aa 200,240,40
LX_t 226,15,18432
v_mul_f32_e64 v162, v162, |v162|
LX_aa 200,244,56
LX_t 228,15,20480
v_mul_f32_e64 v163, v163, |v163|
LX_aa 204,240,44
LX_t 230,15,22528
v_mul_f32_e64 v176, v176, |v176|
LX_aa 204,244,60
LX_w 4
v_mul_f32_e64 v177, v177, |v177|
v_mul_f32_e64 v178, v178, |v178|
LX_aa 208,248,32
v_mul_f32_e64 v179, v179, |v179|
LX_c 160,160,161
LX_aa 208,252,48
LX_c 161,162,163
LX_c 162,176,177
LX_aa 212,248,36
LX_c 163,178,179
v_pk_max_i16 v160, v160, 0
LX_aa 212,252,52
LX_t 232,16,16384
v_pk_max_i16 v161, v161, 0
LX_aa 216,248,40
LX_t 234,16,18432
v_pk_max_i16 v162, v162, 0
LX_aa 216,252,56
LX_t 236,16,20480
v_pk_max_i16 v163, v163, 0
LX_aa 220,248,44
LX_t 238,16,22528
LX_aa 220,252,60
LX_w 4
buffer_store_dwordx4 v[160:163], v1, s[20:23], s49 offen offset:0 sc1
v_mul_f32_e64 v164, v164, |v164|
LX_aa 192,224,64
v_mul_f32_e64 v165, v165, |v165|
v_mul_f32_e64 v166, v166, |v166|
LX_aa 192,228,80
v_mul_f32_e64 v167, v167, |v167|
v_mul_f32_e64 v180, v180, |v180|
LX_aa 196,224,68
v_mul_f32_e64 v181, v181, |v181|
v_mul_f32_e64 v182, v182, |v182|
LX_aa 196,228,84
LX_t 240,15,24576
v_mul_f32_e64 v183, v183, |v183|
LX_aa 200,224,72
LX_t 242,15,26624
LX_c 164,164,165
LX_aa 200,228,88
LX_t 244,15,28672
LX_c 165,166,167
LX_aa 204,224,76
LX_t 246,15,30720
LX_c 166,180,181
LX_aa 204,228,92
LX_w 4
LX_c 167,182,183
v_pk_max_i16 v164, v164, 0
LX_aa 208,232,64
v_pk_max_i16 v165, v165, 0
v_pk_max_i16 v166, v166, 0
LX_aa 208,236,80
v_pk_max_i16 v167, v167, 0
LX_aa 212,232,68
buffer_store_dwordx4 v[164:167], v1, s[20:23], s49 offen offset:1024 sc1
v_mul_f32_e64 v168, v168, |v168|
LX_aa 212,236,84
LX_t 248,16,24576
v_mul_f32_e64 v169, v169, |v169|
LX_aa 216,232,72
LX_t 250,16,26624
v_mul_f32_e64 v170, v170, |v170|
LX_aa 216,236,88
LX_t 252,16,28672
v_mul_f32_e64 v171, v171, |v171|
LX_aa 220,232,76
LX_t 254,16,30720
v_mul_f32_e64 v184, v184, |v184|
LX_aa 220,236,92
LX_w 4
v_mul_f32_e64 v185, v185, |v185|
v_mul_f32_e64 v186, v186, |v186|
LX_aa 192,240,96
v_mul_f32_e64 v187, v187, |v187|
LX_c 168,168,169
LX_aa 192,244,112
LX_c 169,170,171
LX_c 170,184,185
LX_aa 196,240,100
LX_c 171,186,187
v_pk_max_i16 v168, v168, 0
LX_aa 196,244,116
LX_w 0
s_barrier
LX_t 224,17,0
LX_aa 200,240,104
LX_t 226,17,1024
v_pk_max_i16 v169, v169, 0
LX_aa 200,244,120
LX_t 228,17,2048
v_pk_max_i16 v170, v170, 0
LX_aa 204,240,108
s_add_u32 s35, s94, 131072
s_add_u32 m0, s48, 65536
LX_t 230,17,3072
LX_aa 204,244,124
buffer_load_dwordx4 v28, s[28:31], s35 offen offset:0  lds
v_pk_max_i16 v171, v171, 0
LX_aa 208,248,96
buffer_store_dwordx4 v[168:171], v1, s[20:23], s49 offen offset:2048 sc1
v_mul_f32_e64 v172, v172, |v172|
LX_aa 208,252,112
s_add_u32 m0, s48, 67456
v_mul_f32_e64 v173, v173, |v173|
v_mul_f32_e64 v174, v174, |v174|
LX_aa 212,248,100
buffer_load_dwordx4 v28, s[28:31], s35 offen offset:128  lds
v_mul_f32_e64 v175, v175, |v175|
LX_aa 212,252,116
LX_t 232,20,0
v_mul_f32_e64 v188, v188, |v188|
LX_aa 216,248,104
LX_t 234,20,1024
v_mul_f32_e64 v189, v189, |v189|
LX_aa 216,252,120
LX_t 236,20,2048
v_mul_f32_e64 v190, v190, |v190|
LX_aa 220,248,108
LX_t 238,20,3072
v_mul_f32_e64 v191, v191, |v191|
LX_aa 220,252,124
LX_w 4
LX_c 172,172,173
LX_c 173,174,175
LX_aa 224,224,0
LX_c 174,188,189
LX_c 175,190,191
LX_aa 224,228,16
s_add_u32 m0, s48, 66560
v_pk_max_i16 v172, v172, 0
v_pk_max_i16 v173, v173, 0
LX_aa 228,224,4
buffer_load_dwordx4 v29, s[28:31], s35 offen offset:0  lds
v_pk_max_i16 v174, v174, 0
LX_aa 228,228,20
LX_t 240,17,4096
v_pk_max_i16 v175, v175, 0
LX_aa 232,224,8
LX_t 242,17,5120
LX_aa 232,228,24
LX_t 244,17,6144
LX_aa 236,224,12
LX_t 246,17,7168
LX_aa 236,228,28
LX_w 4
buffer_store_dwordx4 v[172:175], v1, s[20:23], s49 offen offset:3072 sc1
s_add_u32 s49, s88, 20480
LX_aa 240,232,0
s_add_u32 m0, s48, 68480
v_mul_f32_e64 v192, v192, |v192|
v_mul_f32_e64 v193, v193, |v193|
LX_aa 240,236,16
buffer_load_dwordx4 v29, s[28:31], s35 offen offset:128  lds
v_mul_f32_e64 v194, v194, |v194|
LX_aa 244,232,4
v_mul_f32_e64 v195, v195, |v195|
v_mul_f32_e64 v208, v208, |v208|
LX_aa 244,236,20
LX_t 248,20,4096
v_mul_f32_e64 v209, v209, |v209|
LX_aa 248,232,8
LX_t 250,20,5120
v_mul_f32_e64 v210, v210, |v210|
LX_aa 248,236,24
LX_t 252,20,6144
v_mul_f32_e64 v211, v211, |v211|
LX_aa 252,232,12
LX_t 254,20,7168
LX_c 192,192,193
LX_aa 252,236,28
LX_w 4
buffer_load_dwordx2 v[18:19], v24, s[20:23], s80 offen sc1
LX_c 193,194,195
LX_aa 224,240,32
s_add_u32 s35, s95, 131072
s_add_u32 m0, s48, 81920
LX_c 194,208,209
LX_aa 224,244,48
buffer_load_dwordx4 v28, s[28:31], s35 offen offset:0  lds
LX_c 195,210,211
LX_aa 228,240,36
v_pk_max_i16 v192, v192, 0
v_pk_max_i16 v193, v193, 0
LX_aa 228,244,52
LX_t 224,17,8192
v_pk_max_i16 v194, v194, 0
LX_aa 232,240,40
LX_t 226,17,9216
v_pk_max_i16 v195, v195, 0
LX_aa 232,244,56
LX_t 228,17,10240
LX_aa 236,240,44
LX_t 230,17,11264
LX_aa 236,244,60
LX_w 4
s_add_u32 m0, s48, 83840
buffer_store_dwordx4 v[192:195], v1, s[20:23], s49 offen offset:0 sc1
LX_aa 240,248,32
buffer_load_dwordx4 v28, s[28:31], s35 offen offset:128  lds
v_mul_f32_e64 v196, v196, |v196|
LX_aa 240,252,48
v_mul_f32_e64 v197, v197, |v197|
v_mul_f32_e64 v198, v198, |v198|
LX_aa 244,248,36
LX_F0_pad8:
s_cbranch_execz LX_F0_pad11
v_mul_f32_e64 v199, v199, |v199|
v_mul_f32_e64 v212, v212, |v212|
LX_aa 244,252,52
LX_t 232,20,8192
v_mul_f32_e64 v213, v213, |v213|
LX_aa 248,248,40
LX_t 234,20,9216
v_mul_f32_e64 v214, v214, |v214|
LX_aa 248,252,56
LX_t 236,20,10240
v_mul_f32_e64 v215, v215, |v215|
LX_aa 252,248,44
LX_t 238,20,11264
LX_c 196,196,197
LX_aa 252,252,60
LX_w 4
s_add_u32 m0, s48, 82944
LX_c 197,198,199
LX_aa 224,224,64
buffer_load_dwordx4 v29, s[28:31], s35 offen offset:0  lds
LX_c 198,212,213
LX_aa 224,228,80
LX_c 199,214,215
v_pk_max_i16 v196, v196, 0
LX_aa 228,224,68
v_pk_max_i16 v197, v197, 0
v_pk_max_i16 v198, v198, 0
LX_aa 228,228,84
LX_t 240,17,12288
v_pk_max_i16 v199, v199, 0
LX_aa 232,224,72
LX_t 242,17,13312
LX_aa 232,228,88
LX_t 244,17,14336
LX_aa 236,224,76
s_add_u32 m0, s48, 84864
LX_t 246,17,15360
LX_aa 236,228,92
LX_w 4
buffer_load_dwordx4 v29, s[28:31], s35 offen offset:128  lds
LX_aa 240,232,64
buffer_store_dwordx4 v[196:199], v1, s[20:23], s49 offen offset:1024 sc1
v_mul_f32_e64 v200, v200, |v200|
LX_aa 240,236,80
v_mul_f32_e64 v201, v201, |v201|
v_mul_f32_e64 v202, v202, |v202|
LX_aa 244,232,68
v_mul_f32_e64 v203, v203, |v203|
v_mul_f32_e64 v216, v216, |v216|
LX_aa 244,236,84
LX_t 248,20,12288
v_mul_f32_e64 v217, v217, |v217|
LX_aa 248,232,72
LX_t 250,20,13312
v_mul_f32_e64 v218, v218, |v218|
LX_aa 248,236,88
LX_t 252,20,14336
v_mul_f32_e64 v219, v219, |v219|
LX_aa 252,232,76
LX_t 254,20,15360
LX_c 200,200,201
LX_aa 252,236,92
LX_w 4
LX_c 201,202,203
LX_c 202,216,217
LX_aa 224,240,96
LX_c 203,218,219
v_pk_max_i16 v200, v200, 0
LX_aa 224,244,112
v_pk_max_i16 v201, v201, 0
v_pk_max_i16 v202, v202, 0
LX_aa 228,240,100
)ASM"
R"ASM(v_pk_max_i16 v203, v203, 0
LX_aa 228,244,116
s_waitcnt vmcnt(48) lgkmcnt(0)
s_barrier
LX_t 224,4,0
LX_aa 232,240,104
LX_t 226,4,4096
LX_aa 232,244,120
LX_t 228,4,8192
LX_aa 236,240,108
LX_t 230,4,12288
LX_aa 236,244,124
buffer_store_dwordx4 v[200:203], v1, s[20:23], s49 offen offset:2048 sc1
v_mul_f32_e64 v204, v204, |v204|
LX_aa 240,248,96
v_mul_f32_e64 v205, v205, |v205|
v_mul_f32_e64 v206, v206, |v206|
LX_aa 240,252,112
v_mul_f32_e64 v207, v207, |v207|
v_mul_f32_e64 v220, v220, |v220|
LX_aa 244,248,100
v_mul_f32_e64 v221, v221, |v221|
v_mul_f32_e64 v222, v222, |v222|
LX_aa 244,252,116
LX_t 232,5,0
v_mul_f32_e64 v223, v223, |v223|
LX_aa 248,248,104
LX_t 234,5,4096
LX_c 204,204,205
LX_aa 248,252,120
LX_t 236,5,8192
LX_c 205,206,207
LX_aa 252,248,108
LX_t 238,5,12288
LX_c 206,220,221
LX_aa 252,252,124
LX_w 4
LX_c 207,222,223
v_pk_max_i16 v204, v204, 0
v_mfma_f32_16x16x32_bf16 v[48:51], a[224:227], v[32:35], 0
s_add_u32 s35, s93, 262144
s_add_u32 m0, s48, 98304
v_pk_max_i16 v205, v205, 0
v_pk_max_i16 v206, v206, 0
LX_av 48,228,64
buffer_load_dwordx4 v28, s[28:31], s35 offen offset:0  lds
v_pk_max_i16 v207, v207, 0
v_mfma_f32_16x16x32_bf16 v[52:55], a[224:227], v[36:39], 0
buffer_store_dwordx4 v[204:207], v1, s[20:23], s49 offen offset:3072 sc1
LX_av 52,228,68
LX_t 240,6,0
v_mfma_f32_16x16x32_bf16 v[56:59], a[224:227], v[40:43], 0
LX_t 242,6,4096
LX_av 56,228,72
s_sub_u32 s49, s44, s43
s_lshl_b32 s49, s49, 6
LX_t 244,6,8192
v_mfma_f32_16x16x32_bf16 v[60:63], a[224:227], v[44:47], 0
s_add_u32 s49, s49, s88
LX_t 246,6,12288
v_mul_f32_e64 v224, v224, |v224|
LX_av 60,228,76
LX_w 4
v_mul_f32_e64 v225, v225, |v225|
v_mul_f32_e64 v226, v226, |v226|
v_mfma_f32_16x16x32_bf16 v[80:83], a[232:235], v[32:35], 0
v_mul_f32_e64 v227, v227, |v227|
v_mul_f32_e64 v240, v240, |v240|
LX_av 80,236,64
v_mul_f32_e64 v241, v241, |v241|
v_mul_f32_e64 v242, v242, |v242|
v_mfma_f32_16x16x32_bf16 v[84:87], a[232:235], v[36:39], 0
v_mul_f32_e64 v243, v243, |v243|
LX_c 224,224,225
LX_av 84,236,68
LX_t 248,7,0
LX_c 225,226,227
v_mfma_f32_16x16x32_bf16 v[88:91], a[232:235], v[40:43], 0
LX_t 250,7,4096
LX_c 226,240,241
LX_av 88,236,72
LX_t 252,7,8192
LX_c 227,242,243
v_mfma_f32_16x16x32_bf16 v[92:95], a[232:235], v[44:47], 0
LX_t 254,7,12288
v_pk_max_i16 v224, v224, 0
LX_av 92,236,76
LX_w 4
s_add_u32 m0, s48, 100224
v_pk_max_i16 v225, v225, 0
v_pk_max_i16 v226, v226, 0
v_mfma_f32_16x16x32_bf16 v[112:115], a[240:243], v[32:35], 0
buffer_load_dwordx4 v28, s[28:31], s35 offen offset:128  lds
v_pk_max_i16 v227, v227, 0
LX_av 112,244,64
buffer_store_dwordx4 v[224:227], v1, s[20:23], s49 offen offset:0 sc1
v_mul_f32_e64 v228, v228, |v228|
v_mfma_f32_16x16x32_bf16 v[116:119], a[240:243], v[36:39], 0
v_mul_f32_e64 v229, v229, |v229|
v_mul_f32_e64 v230, v230, |v230|
LX_av 116,244,68
LX_t 224,8,0
v_mul_f32_e64 v231, v231, |v231|
v_mfma_f32_16x16x32_bf16 v[120:123], a[240:243], v[40:43], 0
LX_t 226,8,4096
v_mul_f32_e64 v244, v244, |v244|
LX_av 120,244,72
LX_t 228,8,8192
v_mul_f32_e64 v245, v245, |v245|
v_mfma_f32_16x16x32_bf16 v[124:127], a[240:243], v[44:47], 0
LX_t 230,8,12288
v_mul_f32_e64 v246, v246, |v246|
LX_av 124,244,76
LX_w 4
v_mul_f32_e64 v247, v247, |v247|
LX_c 228,228,229
v_mfma_f32_16x16x32_bf16 v[144:147], a[248:251], v[32:35], 0
LX_c 229,230,231
LX_c 230,244,245
LX_av 144,252,64
LX_c 231,246,247
v_pk_max_i16 v228, v228, 0
v_mfma_f32_16x16x32_bf16 v[148:151], a[248:251], v[36:39], 0
v_pk_max_i16 v229, v229, 0
v_pk_max_i16 v230, v230, 0
LX_av 148,252,68
LX_t 232,21,0
v_pk_max_i16 v231, v231, 0
v_mfma_f32_16x16x32_bf16 v[152:155], a[248:251], v[40:43], 0
LX_t 234,21,4096
LX_av 152,252,72
LX_t 236,21,8192
v_mfma_f32_16x16x32_bf16 v[156:159], a[248:251], v[44:47], 0
LX_t 238,21,12288
LX_av 156,252,76
LX_w 4
buffer_store_dwordx4 v[228:231], v1, s[20:23], s49 offen offset:1024 sc1
v_mul_f32_e64 v232, v232, |v232|
v_mfma_f32_16x16x32_bf16 v[176:179], a[224:227], v[32:35], 0
v_mul_f32_e64 v233, v233, |v233|
v_mul_f32_e64 v234, v234, |v234|
LX_av 176,228,64
v_mul_f32_e64 v235, v235, |v235|
v_mul_f32_e64 v248, v248, |v248|
v_mfma_f32_16x16x32_bf16 v[180:183], a[224:227], v[36:39], 0
v_mul_f32_e64 v249, v249, |v249|
v_mul_f32_e64 v250, v250, |v250|
LX_av 180,228,68
LX_t 240,4,16384
v_mul_f32_e64 v251, v251, |v251|
v_mfma_f32_16x16x32_bf16 v[184:187], a[224:227], v[40:43], 0
LX_t 242,4,20480
LX_c 232,232,233
LX_av 184,228,72
LX_t 244,4,24576
LX_c 233,234,235
v_mfma_f32_16x16x32_bf16 v[188:191], a[224:227], v[44:47], 0
LX_t 246,4,28672
LX_c 234,248,249
LX_av 188,228,76
LX_w 4
LX_c 235,250,251
v_pk_max_i16 v232, v232, 0
v_mfma_f32_16x16x32_bf16 v[208:211], a[232:235], v[32:35], 0
s_waitcnt vmcnt(21)
buffer_store_dwordx2 v[26:27], v30, s[20:23], s82 offen offset:1536 sc1
v_pk_max_i16 v233, v233, 0
LX_av 208,236,64
v_pk_max_i16 v234, v234, 0
v_pk_max_i16 v235, v235, 0
v_mfma_f32_16x16x32_bf16 v[212:215], a[232:235], v[36:39], 0
buffer_store_dwordx4 v[232:235], v1, s[20:23], s49 offen offset:2048 sc1
v_mul_f32_e64 v236, v236, |v236|
LX_av 212,236,68
LX_t 248,5,16384
v_mul_f32_e64 v237, v237, |v237|
v_mfma_f32_16x16x32_bf16 v[216:219], a[232:235], v[40:43], 0
LX_t 250,5,20480
v_mul_f32_e64 v238, v238, |v238|
LX_av 216,236,72
LX_t 252,5,24576
v_mul_f32_e64 v239, v239, |v239|
v_mfma_f32_16x16x32_bf16 v[220:223], a[232:235], v[44:47], 0
LX_t 254,5,28672
v_mul_f32_e64 v252, v252, |v252|
LX_av 220,236,76
LX_w 4
v_mul_f32_e64 v253, v253, |v253|
v_mul_f32_e64 v254, v254, |v254|
LX_av 48,240,96
v_mul_f32_e64 v255, v255, |v255|
LX_c 236,236,237
LX_av 48,244,128
s_waitcnt vmcnt(14)
v_cmp_ne_u64 vcc, s[56:57], v[18:19]
LX_c 237,238,239
LX_av 52,240,100
LX_c 238,252,253
LX_c 239,254,255
LX_av 52,244,132
s_cbranch_vccnz LX_c16
LX_c16r:
LX_t 224,6,16384
v_pk_max_i16 v236, v236, 0
LX_av 56,240,104
LX_t 226,6,20480
v_pk_max_i16 v237, v237, 0
LX_av 56,244,136
LX_t 228,6,24576
v_pk_max_i16 v238, v238, 0
LX_av 60,240,108
LX_t 230,6,28672
v_pk_max_i16 v239, v239, 0
LX_av 60,244,140
LX_w 4
s_add_u32 s33, s89, 0
buffer_store_dwordx4 v[236:239], v1, s[20:23], s49 offen offset:3072 sc1
LX_av 80,248,96
LX_l 0,1,0
LX_av 80,252,128
LX_av 84,248,100
LX_l 4,1,1024
LX_av 84,252,132
LX_t 232,7,16384
LX_av 88,248,104
LX_t 234,7,20480
LX_av 88,252,136
LX_t 236,7,24576
LX_av 92,248,108
LX_t 238,7,28672
LX_av 92,252,140
LX_w 4
LX_l 8,1,2048
LX_av 112,224,96
LX_av 112,228,128
LX_l 12,1,3072
LX_av 116,224,100
LX_av 116,228,132
LX_t 240,8,16384
LX_av 120,224,104
LX_t 242,8,20480
LX_av 120,228,136
LX_t 244,8,24576
LX_av 124,224,108
LX_t 246,8,28672
LX_av 124,228,140
LX_w 4
LX_l 16,23,0
LX_av 144,232,96
LX_av 144,236,128
LX_l 20,23,1024
LX_av 148,232,100
LX_av 148,236,132
LX_t 248,21,16384
LX_av 152,232,104
LX_t 250,21,20480
LX_av 152,236,136
LX_t 252,21,24576
LX_av 156,232,108
LX_t 254,21,28672
LX_av 156,236,140
LX_w 4
LX_l 24,23,2048
LX_av 176,240,96
LX_av 176,244,128
LX_l 28,23,3072
LX_av 180,240,100
LX_av 180,244,132
LX_t 224,2,0
LX_av 184,240,104
LX_t 226,2,4096
LX_av 184,244,136
LX_t 228,2,8192
LX_av 188,240,108
s_add_u32 s33, s90, 0
LX_t 230,2,12288
LX_av 188,244,140
LX_w 4
LX_l 32,1,0
LX_av 208,248,96
LX_av 208,252,128
LX_l 36,1,1024
LX_av 212,248,100
LX_av 212,252,132
LX_t 232,3,0
LX_av 216,248,104
LX_t 234,3,4096
LX_av 216,252,136
LX_t 236,3,8192
LX_av 220,248,108
LX_t 238,3,12288
LX_av 220,252,140
LX_w 4
LX_l 40,1,2048
v_mfma_f32_16x16x32_bf16 v[240:243], a[224:227], v[32:35], 0
LX_av 240,228,64
v_mfma_f32_16x16x32_bf16 v[244:247], a[224:227], v[36:39], 0
LX_l 44,1,3072
LX_av 244,228,68
LX_t 240,2,16384
v_mfma_f32_16x16x32_bf16 v[248:251], a[224:227], v[40:43], 0
LX_t 242,2,20480
LX_av 248,228,72
LX_t 244,2,24576
v_mfma_f32_16x16x32_bf16 v[252:255], a[224:227], v[44:47], 0
LX_t 246,2,28672
LX_av 252,228,76
LX_w 4
LX_l 48,23,0
v_mfma_f32_16x16x32_bf16 v[224:227], a[232:235], v[32:35], 0
LX_av 224,236,64
v_mfma_f32_16x16x32_bf16 v[228:231], a[232:235], v[36:39], 0
LX_l 52,23,1024
LX_av 228,236,68
LX_t 248,3,16384
v_mfma_f32_16x16x32_bf16 v[232:235], a[232:235], v[40:43], 0
LX_t 250,3,20480
LX_av 232,236,72
LX_t 252,3,24576
v_mfma_f32_16x16x32_bf16 v[236:239], a[232:235], v[44:47], 0
LX_t 254,3,28672
LX_av 236,236,76
LX_w 4
LX_l 56,23,2048
LX_av 240,240,96
LX_av 240,244,128
LX_av 244,240,100
LX_av 244,244,132
LX_t 224,4,32768
LX_av 248,240,104
LX_t 226,4,36864
LX_av 248,244,136
LX_t 228,4,40960
LX_av 252,240,108
LX_t 230,4,45056
LX_av 252,244,140
LX_w 4
LX_l 60,23,3072
LX_av 224,248,96
s_add_u32 s33, s91, 0
LX_av 224,252,128
LX_l 64,1,0
LX_av 228,248,100
LX_av 228,252,132
LX_t 232,5,32768
LX_av 232,248,104
LX_t 234,5,36864
LX_av 232,252,136
LX_t 236,5,40960
LX_av 236,248,108
LX_t 238,5,45056
LX_av 236,252,140
LX_w 4
LX_l 68,1,1024
LX_av 48,224,160
LX_av 48,228,192
LX_l 72,1,2048
LX_av 52,224,164
LX_av 52,228,196
LX_t 240,6,32768
LX_av 56,224,168
LX_t 242,6,36864
LX_av 56,228,200
LX_t 244,6,40960
LX_av 60,224,172
LX_t 246,6,45056
LX_av 60,228,204
LX_w 4
LX_l 76,1,3072
LX_av 80,232,160
LX_av 80,236,192
LX_l 80,23,0
LX_av 84,232,164
LX_av 84,236,196
LX_t 248,7,32768
LX_av 88,232,168
LX_t 250,7,36864
LX_av 88,236,200
LX_t 252,7,40960
LX_av 92,232,172
LX_t 254,7,45056
LX_av 92,236,204
LX_w 4
LX_l 84,23,1024
LX_av 112,240,160
LX_av 112,244,192
LX_l 88,23,2048
LX_av 116,240,164
LX_av 116,244,196
LX_t 224,8,32768
LX_av 120,240,168
LX_t 226,8,36864
LX_av 120,244,200
LX_t 228,8,40960
LX_av 124,240,172
LX_t 230,8,45056
LX_av 124,244,204
LX_w 4
LX_l 92,23,3072
LX_av 144,248,160
LX_av 144,252,192
s_add_u32 m0, s48, 99328
LX_av 148,248,164
buffer_load_dwordx4 v29, s[28:31], s35 offen offset:0  lds
LX_av 148,252,196
LX_t 232,21,32768
LX_av 152,248,168
LX_t 234,21,36864
LX_av 152,252,200
LX_t 236,21,40960
LX_av 156,248,172
s_add_u32 m0, s48, 101248
LX_t 238,21,45056
LX_av 156,252,204
LX_w 4
buffer_load_dwordx4 v29, s[28:31], s35 offen offset:128  lds
LX_av 176,224,160
LX_av 176,228,192
LX_av 180,224,164
LX_av 180,228,196
LX_t 240,2,32768
LX_av 184,224,168
LX_t 242,2,36864
LX_av 184,228,200
LX_t 244,2,40960
LX_av 188,224,172
LX_t 246,2,45056
LX_av 188,228,204
LX_w 4
LX_av 208,232,160
LX_av 208,236,192
LX_av 212,232,164
LX_av 212,236,196
LX_t 248,3,32768
LX_av 216,232,168
LX_t 250,3,36864
LX_av 216,236,200
LX_t 252,3,40960
LX_av 220,232,172
LX_t 254,3,45056
LX_av 220,236,204
LX_w 4
LX_av 240,240,160
LX_av 240,244,192
LX_av 244,240,164
v_add_u32 v4, 4294852608, v4
LX_av 244,244,196
s_waitcnt vmcnt(61) lgkmcnt(0)
s_barrier
LX_t 224,4,0
LX_av 248,240,168
LX_t 226,4,4096
LX_av 248,244,200
LX_t 228,4,8192
LX_av 252,240,172
s_add_u32 s35, s94, 262144
s_add_u32 m0, s48, 114688
LX_t 230,4,12288
LX_av 252,244,204
buffer_load_dwordx4 v28, s[28:31], s35 offen offset:0  lds
LX_av 224,248,160
LX_F0_pad9:
s_cbranch_execz LX_F0_pad12
LX_av 224,252,192
s_add_u32 m0, s48, 116608
LX_av 228,248,164
buffer_load_dwordx4 v28, s[28:31], s35 offen offset:128  lds
v_add_u32 v5, 4294852608, v5
LX_av 228,252,196
LX_t 232,5,0
LX_av 232,248,168
LX_t 234,5,4096
LX_av 232,252,200
LX_t 236,5,8192
LX_av 236,248,172
LX_t 238,5,12288
LX_av 236,252,204
LX_w 4
s_waitcnt vmcnt(20)
buffer_load_dwordx2 v[18:19], v25, s[20:23], s80 offen sc1
LX_aa 48,224,0
LX_aa 48,228,16
LX_aa 52,224,4
v_add_u32 v6, 4294852608, v6
LX_aa 52,228,20
LX_t 240,6,0
LX_aa 56,224,8
LX_t 242,6,4096
LX_aa 56,228,24
LX_t 244,6,8192
LX_aa 60,224,12
s_add_u32 m0, s48, 115712
LX_t 246,6,12288
LX_aa 60,228,28
LX_w 4
buffer_load_dwordx4 v29, s[28:31], s35 offen offset:0  lds
LX_aa 80,232,0
LX_aa 80,236,16
LX_aa 84,232,4
v_add_u32 v7, 4294852608, v7
LX_aa 84,236,20
LX_t 248,7,0
LX_aa 88,232,8
LX_t 250,7,4096
LX_aa 88,236,24
LX_t 252,7,8192
LX_aa 92,232,12
LX_t 254,7,12288
LX_aa 92,236,28
LX_w 4
s_waitcnt vmcnt(30)
s_add_u32 m0, s48, 117632
buffer_store_dwordx2 v[26:27], v30, s[20:23], s83 offen offset:128 sc1
LX_aa 112,240,0
buffer_load_dwordx4 v29, s[28:31], s35 offen offset:128  lds
LX_aa 112,244,16
LX_aa 116,240,4
v_add_u32 v8, 4294852608, v8
LX_aa 116,244,20
LX_t 224,8,0
LX_aa 120,240,8
LX_t 226,8,4096
LX_aa 120,244,24
LX_t 228,8,8192
LX_aa 124,240,12
s_add_u32 s35, s95, 262144
s_add_u32 m0, s48, 131072
LX_t 230,8,12288
LX_aa 124,244,28
)ASM"
R"ASM(LX_w 4
buffer_load_dwordx4 v28, s[28:31], s35 offen offset:0  lds
LX_aa 144,248,0
LX_aa 144,252,16
LX_aa 148,248,4
v_add_u32 v21, 4294852608, v21
LX_aa 148,252,20
LX_t 232,21,0
LX_aa 152,248,8
LX_t 234,21,4096
LX_aa 152,252,24
LX_t 236,21,8192
LX_aa 156,248,12
s_add_u32 m0, s48, 132992
LX_t 238,21,12288
LX_aa 156,252,28
LX_w 4
buffer_load_dwordx4 v28, s[28:31], s35 offen offset:128  lds
LX_aa 176,224,0
LX_aa 176,228,16
LX_aa 180,224,4
v_add_u32 v2, 4294852608, v2
LX_aa 180,228,20
LX_t 240,2,0
LX_aa 184,224,8
LX_t 242,2,4096
LX_aa 184,228,24
LX_t 244,2,8192
LX_aa 188,224,12
s_add_u32 m0, s48, 132096
LX_t 246,2,12288
LX_aa 188,228,28
LX_w 4
buffer_load_dwordx4 v29, s[28:31], s35 offen offset:0  lds
LX_aa 208,232,0
LX_aa 208,236,16
LX_aa 212,232,4
v_add_u32 v3, 4294852608, v3
LX_aa 212,236,20
LX_t 248,3,0
LX_aa 216,232,8
LX_t 250,3,4096
LX_aa 216,236,24
LX_t 252,3,8192
LX_aa 220,232,12
LX_t 254,3,12288
LX_aa 220,236,28
LX_w 4
s_add_u32 m0, s48, 134016
LX_aa 240,240,0
buffer_load_dwordx4 v29, s[28:31], s35 offen offset:128  lds
LX_aa 240,244,16
LX_aa 244,240,4
LX_aa 244,244,20
LX_t 224,4,16384
LX_aa 248,240,8
LX_t 226,4,20480
LX_aa 248,244,24
LX_t 228,4,24576
LX_aa 252,240,12
LX_t 230,4,28672
LX_aa 252,244,28
LX_w 4
LX_aa 224,248,0
s_add_u32 s35, s60, 393216
s_add_u32 m0, s48, 147456
LX_aa 224,252,16
buffer_load_dwordx4 v28, s[28:31], s35 offen offset:0  lds
LX_aa 228,248,4
LX_aa 228,252,20
LX_t 232,5,16384
LX_aa 232,248,8
LX_t 234,5,20480
LX_aa 232,252,24
LX_t 236,5,24576
LX_aa 236,248,12
LX_t 238,5,28672
LX_aa 236,252,28
LX_w 4
s_waitcnt vmcnt(21)
LX_aa 48,224,32
LX_aa 48,228,48
s_add_u32 m0, s48, 149376
LX_aa 52,224,36
buffer_load_dwordx4 v28, s[28:31], s35 offen offset:128  lds
LX_aa 52,228,52
LX_t 240,6,16384
LX_aa 56,224,40
LX_t 242,6,20480
LX_aa 56,228,56
LX_t 244,6,24576
LX_aa 60,224,44
LX_t 246,6,28672
LX_aa 60,228,60
LX_w 4
LX_aa 80,232,32
LX_aa 80,236,48
LX_aa 84,232,36
LX_aa 84,236,52
LX_t 248,7,16384
LX_aa 88,232,40
LX_t 250,7,20480
LX_aa 88,236,56
LX_t 252,7,24576
LX_aa 92,232,44
s_add_u32 m0, s48, 148480
LX_t 254,7,28672
LX_aa 92,236,60
LX_w 4
buffer_load_dwordx4 v29, s[28:31], s35 offen offset:0  lds
LX_aa 112,240,32
LX_aa 112,244,48
LX_aa 116,240,36
LX_aa 116,244,52
LX_t 224,8,16384
LX_aa 120,240,40
LX_t 226,8,20480
LX_aa 120,244,56
LX_t 228,8,24576
LX_aa 124,240,44
s_add_u32 m0, s48, 150400
LX_t 230,8,28672
LX_aa 124,244,60
LX_w 4
buffer_load_dwordx4 v29, s[28:31], s35 offen offset:128  lds
LX_aa 144,248,32
LX_aa 144,252,48
LX_aa 148,248,36
LX_aa 148,252,52
LX_t 232,21,16384
LX_aa 152,248,40
LX_t 234,21,20480
LX_aa 152,252,56
LX_t 236,21,24576
LX_aa 156,248,44
LX_t 238,21,28672
LX_aa 156,252,60
LX_w 4
LX_aa 176,224,32
LX_aa 176,228,48
s_waitcnt vmcnt(11)
v_cmp_ne_u64 vcc, s[56:57], v[18:19]
LX_aa 180,224,36
LX_aa 180,228,52
s_cbranch_vccnz LX_c18
LX_c18r:
LX_t 240,2,16384
LX_aa 184,224,40
LX_t 242,2,20480
LX_aa 184,228,56
LX_t 244,2,24576
LX_aa 188,224,44
s_add_u32 s33, s89, 8192
LX_t 246,2,28672
LX_aa 188,228,60
LX_w 4
LX_l 96,1,0
LX_aa 208,232,32
LX_aa 208,236,48
LX_l 100,1,1024
LX_aa 212,232,36
LX_aa 212,236,52
LX_t 248,3,16384
LX_aa 216,232,40
LX_t 250,3,20480
LX_aa 216,236,56
LX_t 252,3,24576
LX_aa 220,232,44
LX_t 254,3,28672
LX_aa 220,236,60
LX_w 4
LX_l 104,1,2048
LX_aa 240,240,32
LX_aa 240,244,48
LX_l 108,1,3072
LX_aa 244,240,36
LX_aa 244,244,52
LX_t 224,4,32768
LX_aa 248,240,40
LX_t 226,4,36864
LX_aa 248,244,56
LX_t 228,4,40960
LX_aa 252,240,44
LX_t 230,4,45056
LX_aa 252,244,60
LX_w 4
LX_l 112,23,0
LX_aa 224,248,32
LX_aa 224,252,48
LX_l 116,23,1024
LX_aa 228,248,36
LX_aa 228,252,52
LX_t 232,5,32768
LX_aa 232,248,40
LX_t 234,5,36864
LX_aa 232,252,56
LX_t 236,5,40960
LX_aa 236,248,44
LX_t 238,5,45056
LX_aa 236,252,60
LX_w 4
s_waitcnt vmcnt(22)
LX_l 120,23,2048
LX_aa 48,224,64
LX_aa 48,228,80
LX_l 124,23,3072
LX_aa 52,224,68
LX_aa 52,228,84
LX_t 240,6,32768
LX_aa 56,224,72
LX_t 242,6,36864
LX_aa 56,228,88
LX_t 244,6,40960
LX_aa 60,224,76
s_add_u32 s33, s90, 8192
LX_t 246,6,45056
LX_aa 60,228,92
LX_w 4
LX_l 128,1,0
LX_aa 80,232,64
LX_aa 80,236,80
LX_l 132,1,1024
LX_aa 84,232,68
LX_aa 84,236,84
LX_t 248,7,32768
LX_aa 88,232,72
LX_t 250,7,36864
LX_aa 88,236,88
LX_t 252,7,40960
LX_aa 92,232,76
LX_t 254,7,45056
LX_aa 92,236,92
LX_w 4
LX_l 136,1,2048
LX_aa 112,240,64
LX_aa 112,244,80
LX_l 140,1,3072
LX_aa 116,240,68
buffer_load_dwordx2 v[18:19], v22, s[20:23], s80 offen sc1
LX_aa 116,244,84
LX_t 224,8,32768
LX_aa 120,240,72
LX_t 226,8,36864
LX_aa 120,244,88
LX_t 228,8,40960
LX_aa 124,240,76
LX_t 230,8,45056
LX_aa 124,244,92
LX_w 4
LX_l 144,23,0
LX_aa 144,248,64
LX_aa 144,252,80
LX_l 148,23,1024
LX_aa 148,248,68
LX_aa 148,252,84
LX_t 232,21,32768
LX_aa 152,248,72
LX_t 234,21,36864
LX_aa 152,252,88
LX_t 236,21,40960
LX_aa 156,248,76
LX_t 238,21,45056
LX_aa 156,252,92
LX_w 4
LX_l 152,23,2048
LX_aa 176,224,64
LX_aa 176,228,80
LX_aa 180,224,68
LX_l 156,23,3072
LX_aa 180,228,84
LX_t 240,2,32768
LX_aa 184,224,72
LX_t 242,2,36864
LX_aa 184,228,88
LX_t 244,2,40960
LX_aa 188,224,76
s_add_u32 s33, s91, 8192
LX_t 246,2,45056
LX_aa 188,228,92
LX_w 4
LX_l 160,1,0
LX_aa 208,232,64
LX_aa 208,236,80
LX_aa 212,232,68
LX_aa 212,236,84
LX_t 248,3,32768
LX_aa 216,232,72
LX_t 250,3,36864
LX_aa 216,236,88
LX_t 252,3,40960
LX_aa 220,232,76
LX_t 254,3,45056
LX_aa 220,236,92
LX_w 4
LX_l 164,1,1024
LX_aa 240,240,64
LX_aa 240,244,80
LX_l 168,1,2048
LX_aa 244,240,68
LX_aa 244,244,84
s_waitcnt vmcnt(63) lgkmcnt(0)
s_barrier
LX_t 224,4,49152
LX_aa 248,240,72
LX_t 226,4,53248
LX_aa 248,244,88
LX_t 228,4,57344
LX_aa 252,240,76
LX_t 230,4,61440
LX_aa 252,244,92
LX_l 172,1,3072
LX_aa 224,248,64
LX_aa 224,252,80
LX_l 176,23,0
LX_aa 228,248,68
LX_aa 228,252,84
LX_t 232,5,49152
LX_aa 232,248,72
LX_t 234,5,53248
LX_aa 232,252,88
LX_t 236,5,57344
LX_aa 236,248,76
LX_t 238,5,61440
LX_aa 236,252,92
LX_w 4
s_waitcnt vmcnt(14)
LX_l 180,23,1024
LX_aa 48,224,96
LX_aa 48,228,112
LX_l 184,23,2048
LX_aa 52,224,100
LX_aa 52,228,116
LX_t 240,6,49152
LX_aa 56,224,104
LX_t 242,6,53248
LX_aa 56,228,120
LX_t 244,6,57344
LX_aa 60,224,108
LX_t 246,6,61440
LX_aa 60,228,124
LX_w 4
LX_l 188,23,3072
LX_aa 80,232,96
s_add_u32 s35, s60, 1310720
s_add_u32 m0, s48, 0
LX_aa 80,236,112
buffer_load_dwordx4 v28, s[28:31], s35 offen offset:0  lds
LX_aa 84,232,100
LX_aa 84,236,116
LX_t 248,7,49152
LX_aa 88,232,104
LX_t 250,7,53248
LX_aa 88,236,120
LX_t 252,7,57344
LX_aa 92,232,108
s_add_u32 m0, s48, 1920
LX_t 254,7,61440
LX_aa 92,236,124
LX_w 4
buffer_load_dwordx4 v28, s[28:31], s35 offen offset:128  lds
LX_aa 112,240,96
LX_aa 112,244,112
LX_aa 116,240,100
LX_aa 116,244,116
LX_t 224,8,49152
LX_aa 120,240,104
LX_t 226,8,53248
LX_aa 120,244,120
LX_t 228,8,57344
LX_aa 124,240,108
s_add_u32 m0, s48, 1024
LX_t 230,8,61440
LX_aa 124,244,124
LX_w 4
buffer_load_dwordx4 v29, s[28:31], s35 offen offset:0  lds
LX_aa 144,248,96
LX_aa 144,252,112
LX_aa 148,248,100
LX_aa 148,252,116
LX_t 232,21,49152
LX_aa 152,248,104
LX_t 234,21,53248
LX_aa 152,252,120
LX_t 236,21,57344
LX_aa 156,248,108
s_add_u32 m0, s48, 2944
LX_t 238,21,61440
LX_aa 156,252,124
LX_w 4
buffer_load_dwordx4 v29, s[28:31], s35 offen offset:128  lds
LX_aa 176,224,96
LX_aa 176,228,112
LX_aa 180,224,100
LX_aa 180,228,116
LX_t 240,2,49152
LX_aa 184,224,104
LX_t 242,2,53248
LX_aa 184,228,120
LX_t 244,2,57344
LX_aa 188,224,108
s_add_u32 s35, s46, 0
s_add_u32 m0, s48, 16384
LX_t 246,2,61440
LX_aa 188,228,124
LX_w 4
buffer_load_dwordx4 v28, s[84:87], s35 offen offset:0  lds
LX_aa 208,232,96
LX_aa 208,236,112
LX_aa 212,232,100
LX_aa 212,236,116
LX_t 248,3,49152
LX_aa 216,232,104
LX_t 250,3,53248
LX_aa 216,236,120
LX_t 252,3,57344
LX_aa 220,232,108
LX_t 254,3,61440
LX_aa 220,236,124
LX_w 4
s_add_u32 m0, s48, 18304
LX_aa 240,240,96
buffer_load_dwordx4 v28, s[84:87], s35 offen offset:128  lds
LX_aa 240,244,112
LX_aa 244,240,100
v_add_u32 v4, 65536, v4
LX_aa 244,244,116
LX_t 224,4,0
LX_aa 248,240,104
LX_t 226,4,4096
LX_aa 248,244,120
LX_t 228,4,8192
LX_aa 252,240,108
LX_t 230,4,12288
LX_aa 252,244,124
LX_w 4
s_waitcnt vmcnt(18)
v_cmp_ne_u64 vcc, s[56:57], v[18:19]
LX_aa 224,248,96
s_add_u32 m0, s48, 17408
LX_aa 224,252,112
s_cbranch_vccnz LX_c20
LX_c20r:
buffer_load_dwordx4 v29, s[84:87], s35 offen offset:0  lds
LX_aa 228,248,100
v_add_u32 v5, 65536, v5
LX_aa 228,252,116
LX_t 232,5,0
LX_aa 232,248,104
LX_t 234,5,4096
LX_aa 232,252,120
LX_t 236,5,8192
LX_aa 236,248,108
s_add_u32 s33, s89, 16384
LX_t 238,5,12288
LX_aa 236,252,124
LX_w 4
s_waitcnt vmcnt(15)
LX_l 192,1,0
LX_aa 48,224,128
LX_aa 48,228,144
LX_l 196,1,1024
LX_aa 52,224,132
v_add_u32 v6, 65536, v6
LX_aa 52,228,148
LX_t 240,6,0
LX_aa 56,224,136
LX_t 242,6,4096
LX_aa 56,228,152
LX_t 244,6,8192
LX_aa 60,224,140
LX_F0_pad10:
s_cbranch_execz LX_F0_padret
LX_t 246,6,12288
LX_aa 60,228,156
LX_w 4
LX_l 200,1,2048
LX_aa 80,232,128
LX_aa 80,236,144
LX_l 204,1,3072
LX_aa 84,232,132
v_add_u32 v7, 65536, v7
LX_aa 84,236,148
LX_t 248,7,0
LX_aa 88,232,136
LX_t 250,7,4096
LX_aa 88,236,152
LX_t 252,7,8192
LX_aa 92,232,140
LX_t 254,7,12288
LX_aa 92,236,156
LX_w 4
LX_l 208,23,0
LX_aa 112,240,128
LX_aa 112,244,144
LX_l 212,23,1024
LX_aa 116,240,132
v_add_u32 v8, 65536, v8
LX_aa 116,244,148
LX_t 224,8,0
LX_aa 120,240,136
LX_t 226,8,4096
LX_aa 120,244,152
LX_t 228,8,8192
LX_aa 124,240,140
LX_t 230,8,12288
LX_aa 124,244,156
LX_w 4
LX_l 216,23,2048
LX_aa 144,248,128
LX_aa 144,252,144
LX_l 220,23,3072
LX_aa 148,248,132
v_add_u32 v21, 65536, v21
LX_aa 148,252,148
LX_t 232,21,0
LX_aa 152,248,136
LX_t 234,21,4096
LX_aa 152,252,152
LX_t 236,21,8192
LX_aa 156,248,140
s_add_u32 s33, s90, 16384
LX_t 238,21,12288
LX_aa 156,252,156
LX_w 4
buffer_load_dwordx4 v[96:99], v1, s[20:23], s33 offen offset:0 sc1
LX_aa 176,224,128
LX_aa 176,228,144
buffer_load_dwordx4 v[100:103], v1, s[20:23], s33 offen offset:1024 sc1
LX_aa 180,224,132
v_add_u32 v2, 65536, v2
LX_aa 180,228,148
LX_t 240,2,0
LX_aa 184,224,136
LX_t 242,2,4096
LX_aa 184,228,152
LX_t 244,2,8192
LX_aa 188,224,140
LX_t 246,2,12288
LX_aa 188,228,156
LX_w 4
buffer_load_dwordx4 v[104:107], v1, s[20:23], s33 offen offset:2048 sc1
LX_aa 208,232,128
LX_aa 208,236,144
buffer_load_dwordx4 v[108:111], v1, s[20:23], s33 offen offset:3072 sc1
LX_aa 212,232,132
v_add_u32 v3, 65536, v3
LX_aa 212,236,148
LX_t 248,3,0
LX_aa 216,232,136
LX_t 250,3,4096
LX_aa 216,236,152
LX_t 252,3,8192
LX_aa 220,232,140
LX_t 254,3,12288
LX_aa 220,236,156
LX_w 4
buffer_load_dwordx4 v[128:131], v23, s[20:23], s33 offen offset:0 sc1
LX_aa 240,240,128
LX_aa 240,244,144
buffer_load_dwordx4 v[132:135], v23, s[20:23], s33 offen offset:1024 sc1
LX_aa 244,240,132
buffer_load_dwordx2 v[18:19], v31, s[20:23], s80 offen sc1
LX_aa 244,244,148
LX_t 224,4,16384
LX_aa 248,240,136
LX_t 226,4,20480
LX_aa 248,244,152
LX_t 228,4,24576
LX_aa 252,240,140
LX_t 230,4,28672
LX_aa 252,244,156
LX_w 4
buffer_load_dwordx4 v[136:139], v23, s[20:23], s33 offen offset:2048 sc1
LX_aa 224,248,128
LX_aa 224,252,144
LX_aa 228,248,132
buffer_load_dwordx4 v[140:143], v23, s[20:23], s33 offen offset:3072 sc1
LX_aa 228,252,148
LX_t 232,5,16384
LX_aa 232,248,136
LX_t 234,5,20480
LX_aa 232,252,152
LX_t 236,5,24576
LX_aa 236,248,140
s_add_u32 s33, s91, 16384
LX_t 238,5,28672
LX_aa 236,252,156
LX_w 4
s_waitcnt vmcnt(24)
buffer_load_dwordx4 v[160:163], v1, s[20:23], s33 offen offset:0 sc1
LX_aa 48,224,160
LX_aa 48,228,176
LX_aa 52,224,164
LX_aa 52,228,180
LX_t 240,6,16384
LX_aa 56,224,168
LX_t 242,6,20480
LX_aa 56,228,184
LX_t 244,6,24576
LX_aa 60,224,172
LX_t 246,6,28672
LX_aa 60,228,188
LX_w 4
buffer_load_dwordx4 v[164:167], v1, s[20:23], s33 offen offset:1024 sc1
LX_aa 80,232,160
LX_aa 80,236,176
buffer_load_dwordx4 v[168:171], v1, s[20:23], s33 offen offset:2048 sc1
LX_aa 84,232,164
LX_aa 84,236,180
LX_t 248,7,16384
LX_aa 88,232,168
LX_t 250,7,20480
LX_aa 88,236,184
LX_t 252,7,24576
LX_aa 92,232,172
LX_t 254,7,28672
LX_aa 92,236,188
LX_w 4
buffer_load_dwordx4 v[172:175], v1, s[20:23], s33 offen offset:3072 sc1
LX_aa 112,240,160
LX_aa 112,244,176
buffer_load_dwordx4 v[192:195], v23, s[20:23], s33 offen offset:0 sc1
LX_aa 116,240,164
LX_aa 116,244,180
LX_t 224,8,16384
LX_aa 120,240,168
)ASM"
R"ASM(LX_t 226,8,20480
LX_aa 120,244,184
LX_t 228,8,24576
LX_aa 124,240,172
LX_t 230,8,28672
LX_aa 124,244,188
LX_w 4
buffer_load_dwordx4 v[196:199], v23, s[20:23], s33 offen offset:1024 sc1
LX_aa 144,248,160
LX_aa 144,252,176
buffer_load_dwordx4 v[200:203], v23, s[20:23], s33 offen offset:2048 sc1
LX_aa 148,248,164
LX_aa 148,252,180
LX_t 232,21,16384
LX_aa 152,248,168
LX_t 234,21,20480
LX_aa 152,252,184
LX_t 236,21,24576
LX_aa 156,248,172
LX_t 238,21,28672
LX_aa 156,252,188
LX_w 4
buffer_load_dwordx4 v[204:207], v23, s[20:23], s33 offen offset:3072 sc1
LX_aa 176,224,160
s_add_u32 m0, s48, 19328
LX_aa 176,228,176
buffer_load_dwordx4 v29, s[84:87], s35 offen offset:128  lds
LX_aa 180,224,164
LX_aa 180,228,180
LX_t 240,2,16384
LX_aa 184,224,168
LX_t 242,2,20480
LX_aa 184,228,184
LX_t 244,2,24576
LX_aa 188,224,172
s_add_u32 s33, s34, 57344
LX_t 246,2,28672
LX_aa 188,228,188
LX_w 4
LX_l 0,1,0
LX_aa 208,232,160
LX_aa 208,236,176
LX_l 4,1,1024
LX_aa 212,232,164
LX_aa 212,236,180
LX_t 248,3,16384
LX_aa 216,232,168
LX_t 250,3,20480
LX_aa 216,236,184
LX_t 252,3,24576
LX_aa 220,232,172
LX_t 254,3,28672
LX_aa 220,236,188
LX_w 4
LX_l 8,1,2048
LX_aa 240,240,160
LX_aa 240,244,176
LX_aa 244,240,164
LX_l 12,1,3072
LX_aa 244,244,180
s_waitcnt vmcnt(63) lgkmcnt(0)
s_barrier
LX_t 224,4,32768
LX_aa 248,240,168
LX_t 226,4,36864
LX_aa 248,244,184
LX_t 228,4,40960
LX_aa 252,240,172
LX_t 230,4,45056
LX_aa 252,244,188
LX_l 16,23,0
LX_aa 224,248,160
LX_aa 224,252,176
LX_aa 228,248,164
LX_aa 228,252,180
LX_t 232,5,32768
LX_aa 232,248,168
LX_t 234,5,36864
LX_aa 232,252,184
LX_t 236,5,40960
LX_aa 236,248,172
LX_t 238,5,45056
LX_aa 236,252,188
LX_w 4
s_waitcnt vmcnt(23)
LX_l 20,23,1024
LX_aa 48,224,192
LX_aa 48,228,208
LX_l 24,23,2048
LX_aa 52,224,196
LX_aa 52,228,212
LX_t 240,6,32768
LX_aa 56,224,200
LX_t 242,6,36864
LX_aa 56,228,216
LX_t 244,6,40960
LX_aa 60,224,204
LX_t 246,6,45056
LX_aa 60,228,220
LX_w 4
LX_l 28,23,3072
LX_aa 80,232,192
s_add_u32 s33, s34, 114688
LX_aa 80,236,208
LX_l 32,1,0
LX_aa 84,232,196
LX_aa 84,236,212
LX_t 248,7,32768
LX_aa 88,232,200
LX_t 250,7,36864
LX_aa 88,236,216
LX_t 252,7,40960
LX_aa 92,232,204
LX_t 254,7,45056
LX_aa 92,236,220
LX_w 4
LX_l 36,1,1024
LX_aa 112,240,192
LX_aa 112,244,208
LX_l 40,1,2048
LX_aa 116,240,196
LX_aa 116,244,212
LX_t 224,8,32768
LX_aa 120,240,200
LX_t 226,8,36864
LX_aa 120,244,216
LX_t 228,8,40960
LX_aa 124,240,204
LX_t 230,8,45056
LX_aa 124,244,220
LX_w 4
s_waitcnt vmcnt(22)
LX_l 44,1,3072
v_cmp_ne_u64 vcc, s[56:57], v[18:19]
LX_aa 144,248,192
LX_aa 144,252,208
s_cbranch_vccnz LX_c22
LX_c22r:
LX_l 48,23,0
LX_aa 148,248,196
LX_aa 148,252,212
s_lshl_b32 s53, %3, 9
s_add_u32 s53, s53, 256
LX_t 232,21,32768
LX_aa 152,248,200
s_lshl_b32 s54, %2, 6
s_add_u32 s53, s53, s54
LX_t 234,21,36864
LX_aa 152,252,216
s_lshl_b32 s54, s41, 17
s_add_u32 s54, s54, s53
LX_t 236,21,40960
LX_aa 156,248,204
s_add_u32 s55, s54, 65536
LX_t 238,21,45056
v_and_b32 v9, 63, %4
LX_aa 156,252,220
LX_w 4
LX_l 52,23,1024
v_and_b32 v10, 15, v9
LX_aa 176,224,192
v_lshrrev_b32 v11, 4, v9
v_lshlrev_b32 v14, 3, v11
LX_aa 176,228,208
s_mov_b64 s[16:17], s[96:97]
s_and_b32 s17, s17, 0xffff
LX_l 56,23,2048
LX_aa 180,224,196
s_mov_b32 s18, 2048
buffer_load_dwordx2 v[64:65], v14, s[16:19], s53 offen offset:0
LX_aa 180,228,212
LX_t 240,4,49152
LX_aa 184,224,200
LX_t 242,4,53248
LX_aa 184,228,216
LX_t 244,4,57344
LX_aa 188,224,204
LX_t 246,4,61440
LX_aa 188,228,220
LX_w 4
LX_l 60,23,3072
LX_aa 208,232,192
buffer_load_dwordx2 v[66:67], v14, s[16:19], s53 offen offset:32
s_add_u32 s49, s48, 81920
v_lshl_add_u32 v12, v10, 6, v14
LX_aa 208,236,208
s_add_u32 s33, s34, 0
v_add_u32 v12, s49, v12
v_lshl_add_u32 v13, v9, 4, s49
LX_aa 212,232,196
LX_l 64,1,0
v_lshrrev_b32 v15, 2, v9
LX_aa 212,236,212
LX_t 248,5,49152
v_lshlrev_b32 v15, 12, v15
LX_aa 216,232,200
LX_t 250,5,53248
v_and_b32 v14, 48, v13
LX_aa 216,236,216
LX_t 252,5,57344
v_add_u32 v15, v15, v14
LX_aa 220,232,204
LX_t 254,5,61440
v_lshlrev_b32 v11, 2, v11
LX_aa 220,236,220
LX_w 4
s_waitcnt vmcnt(28)
LX_l 68,1,1024
LX_av 48,240,96
v_sub_u32 v11, v10, v11
v_mov_b32 v16, 0x3f80
LX_av 48,244,128
v_mov_b32 v17, 1.0
v_cmp_eq_u32 vcc, 0, v11
LX_av 52,240,100
LX_l 72,1,2048
v_mov_b32 v70, 0
LX_av 52,244,132
LX_t 224,6,49152
v_mov_b32 v72, 0
LX_av 56,240,104
LX_t 226,6,53248
v_cndmask_b32 v68, 0, v16, vcc
LX_av 56,244,136
LX_t 228,6,57344
v_cmp_eq_u32 vcc, 1, v11
LX_av 60,240,108
s_nop 0
s_nop 0
LX_t 230,6,61440
LX_av 60,244,140
LX_w 4
LX_l 76,1,3072
v_cndmask_b32 v68, v68, v17, vcc
LX_av 80,248,96
v_mov_b32 v74, v68
v_cmp_eq_u32 vcc, 2, v11
LX_av 80,252,128
v_mov_b32 v71, 0
v_mov_b32 v73, 0
LX_av 84,248,100
v_cndmask_b32 v69, 0, v16, vcc
v_cmp_eq_u32 vcc, 3, v11
LX_av 84,252,132
s_nop 0
s_nop 0
LX_t 232,7,49152
LX_av 88,248,104
LX_t 234,7,53248
v_cndmask_b32 v69, v69, v17, vcc
LX_av 88,252,136
LX_t 236,7,57344
v_mov_b32 v75, v69
LX_av 92,248,108
s_mov_b64 s[16:17], s[98:99]
s_and_b32 s17, s17, 0xffff
LX_t 238,7,61440
LX_av 92,252,140
LX_w 4
s_mov_b32 s18, 4194304
LX_l 80,23,0
LX_av 112,224,96
v_lshrrev_b32 v20, 4, v9
v_min_u32 v22, 1, v20
LX_av 112,228,128
LX_l 84,23,1024
v_min_u32 v20, 2, v20
LX_av 116,224,100
v_lshlrev_b32 v20, 7, v20
v_lshlrev_b32 v22, 7, v22
LX_av 116,228,132
s_lshl_b32 s52, s81, 9
s_add_u32 s52, s52, 36175872
LX_t 240,8,49152
LX_av 120,224,104
s_xor_b32 s50, s81, 4
s_lshl_b32 s96, s50, 9
LX_t 242,8,53248
LX_av 120,228,136
s_add_u32 s96, s96, 36175872
s_bfe_u32 s50, s50, 0x50002
LX_t 244,8,57344
LX_av 124,224,108
s_mul_i32 s50, s50, 12288
s_add_u32 s50, s50, s38
LX_t 246,8,61440
LX_av 124,228,140
LX_w 4
s_add_u32 s42, s50, 720896
LX_l 88,23,2048
LX_av 144,232,96
s_xor_b32 s50, s81, 8
s_lshl_b32 s97, s50, 9
LX_av 144,236,128
s_add_u32 s97, s97, 36175872
s_bfe_u32 s50, s50, 0x50002
LX_l 92,23,3072
LX_av 148,232,100
s_mul_i32 s50, s50, 12288
s_add_u32 s50, s50, s38
s_add_u32 s43, s50, 724992
LX_av 148,236,132
s_xor_b32 s50, s81, 12
s_lshl_b32 s98, s50, 9
LX_t 248,21,49152
LX_av 152,232,104
s_add_u32 s98, s98, 36175872
s_bfe_u32 s50, s50, 0x50002
LX_t 250,21,53248
LX_av 152,236,136
s_mul_i32 s50, s50, 12288
s_add_u32 s50, s50, s38
LX_t 252,21,57344
LX_av 156,232,108
s_add_u32 s35, s46, 131072
s_add_u32 m0, s48, 32768
LX_t 254,21,61440
LX_av 156,236,140
LX_w 4
s_add_u32 s44, s50, 729088
buffer_load_dwordx4 v28, s[84:87], s35 offen offset:0  lds
LX_av 176,240,96
s_add_u32 s33, s34, 8192
s_bfe_u32 s51, s81, 0x50002
s_mul_i32 s51, s51, 12288
LX_av 176,244,128
s_add_u32 s51, s51, s38
s_add_u32 s51, s51, 720896
LX_l 96,1,0
LX_av 180,240,100
s_add_u32 s50, s51, 8192
v_add_u32 v4, 65536, v4
LX_av 180,244,132
LX_t 224,4,0
LX_av 184,240,104
LX_t 226,4,4096
LX_av 184,244,136
LX_t 228,4,8192
LX_av 188,240,108
s_add_u32 m0, s48, 34688
LX_t 230,4,12288
LX_av 188,244,140
LX_w 4
buffer_load_dwordx4 v28, s[84:87], s35 offen offset:128  lds
LX_av 208,248,96
LX_av 208,252,128
LX_l 100,1,1024
LX_av 212,248,100
v_add_u32 v5, 65536, v5
LX_av 212,252,132
LX_t 232,5,0
LX_av 216,248,104
LX_t 234,5,4096
LX_av 216,252,136
LX_t 236,5,8192
LX_av 220,248,108
s_add_u32 m0, s48, 33792
LX_t 238,5,12288
LX_av 220,252,140
LX_w 4
s_waitcnt vmcnt(31)
buffer_load_dwordx4 v29, s[84:87], s35 offen offset:0  lds
LX_av 48,224,160
LX_av 48,228,192
LX_av 52,224,164
LX_l 104,1,2048
v_add_u32 v6, 65536, v6
LX_av 52,228,196
LX_t 240,6,0
LX_av 56,224,168
LX_t 242,6,4096
LX_av 56,228,200
LX_t 244,6,8192
LX_av 60,224,172
s_add_u32 m0, s48, 35712
LX_t 246,6,12288
LX_av 60,228,204
LX_w 4
buffer_load_dwordx4 v29, s[84:87], s35 offen offset:128  lds
LX_av 80,232,160
LX_av 80,236,192
LX_av 84,232,164
LX_l 108,1,3072
v_add_u32 v7, 65536, v7
LX_av 84,236,196
LX_t 248,7,0
LX_av 88,232,168
LX_t 250,7,4096
LX_av 88,236,200
LX_t 252,7,8192
LX_av 92,232,172
s_add_u32 s35, s46, 262144
s_add_u32 m0, s48, 49152
LX_t 254,7,12288
LX_av 92,236,204
LX_w 4
buffer_load_dwordx4 v28, s[84:87], s35 offen offset:0  lds
LX_av 112,240,160
LX_av 112,244,192
LX_av 116,240,164
v_add_u32 v8, 65536, v8
LX_av 116,244,196
LX_t 224,8,0
LX_av 120,240,168
LX_t 226,8,4096
LX_av 120,244,200
LX_F0_pad11:
s_cbranch_execz LX_F0_padret
LX_t 228,8,8192
LX_av 124,240,172
s_add_u32 m0, s48, 51072
LX_t 230,8,12288
LX_av 124,244,204
LX_w 4
buffer_load_dwordx4 v28, s[84:87], s35 offen offset:128  lds
LX_av 144,248,160
LX_av 144,252,192
LX_l 112,23,0
LX_av 148,248,164
v_add_u32 v21, 65536, v21
LX_av 148,252,196
LX_t 232,21,0
LX_av 152,248,168
LX_t 234,21,4096
LX_av 152,252,200
LX_t 236,21,8192
LX_av 156,248,172
s_add_u32 m0, s48, 50176
LX_t 238,21,12288
LX_av 156,252,204
LX_w 4
buffer_load_dwordx4 v29, s[84:87], s35 offen offset:0  lds
LX_av 176,224,160
LX_av 176,228,192
LX_l 116,23,1024
LX_av 180,224,164
LX_av 180,228,196
s_waitcnt vmcnt(39) lgkmcnt(0)
s_barrier
LX_t 240,4,16384
LX_av 184,224,168
LX_t 242,4,20480
LX_av 184,228,200
LX_t 244,4,24576
LX_av 188,224,172
LX_t 246,4,28672
LX_av 188,228,204
LX_l 120,23,2048
LX_av 208,232,160
s_add_u32 m0, s48, 52096
LX_av 208,236,192
buffer_load_dwordx4 v29, s[84:87], s35 offen offset:128  lds
LX_av 212,232,164
LX_av 212,236,196
LX_t 248,5,16384
LX_av 216,232,168
LX_t 250,5,20480
LX_av 216,236,200
LX_t 252,5,24576
LX_av 220,232,172
LX_t 254,5,28672
LX_av 220,236,204
LX_w 4
s_waitcnt vmcnt(33)
LX_l 124,23,3072
LX_aa 48,240,0
s_add_u32 s35, s46, 393216
s_add_u32 m0, s48, 65536
LX_aa 48,244,16
buffer_load_dwordx4 v28, s[84:87], s35 offen offset:0  lds
LX_aa 52,240,4
LX_aa 52,244,20
LX_t 224,6,16384
LX_aa 56,240,8
LX_t 226,6,20480
LX_aa 56,244,24
LX_t 228,6,24576
LX_aa 60,240,12
s_add_u32 s33, s34, 16384
LX_t 230,6,28672
LX_aa 60,244,28
LX_w 4
LX_l 128,1,0
LX_aa 80,248,0
LX_aa 80,252,16
s_add_u32 m0, s48, 67456
LX_aa 84,248,4
buffer_load_dwordx4 v28, s[84:87], s35 offen offset:128  lds
LX_aa 84,252,20
LX_t 232,7,16384
LX_aa 88,248,8
LX_t 234,7,20480
LX_aa 88,252,24
LX_t 236,7,24576
LX_aa 92,248,12
LX_t 238,7,28672
LX_aa 92,252,28
LX_w 4
LX_l 132,1,1024
LX_aa 112,224,0
LX_aa 112,228,16
LX_aa 116,224,4
LX_l 136,1,2048
LX_aa 116,228,20
LX_t 240,8,16384
LX_aa 120,224,8
LX_t 242,8,20480
LX_aa 120,228,24
LX_t 244,8,24576
LX_aa 124,224,12
s_add_u32 m0, s48, 66560
LX_t 246,8,28672
LX_aa 124,228,28
LX_w 4
buffer_load_dwordx4 v29, s[84:87], s35 offen offset:0  lds
LX_aa 144,232,0
LX_aa 144,236,16
LX_aa 148,232,4
LX_aa 148,236,20
LX_t 248,21,16384
LX_aa 152,232,8
LX_t 250,21,20480
LX_aa 152,236,24
LX_t 252,21,24576
LX_aa 156,232,12
s_add_u32 m0, s48, 68480
LX_t 254,21,28672
LX_aa 156,236,28
LX_w 4
buffer_load_dwordx4 v29, s[84:87], s35 offen offset:128  lds
LX_aa 176,240,0
LX_aa 176,244,16
LX_l 140,1,3072
LX_aa 180,240,4
v_add_u32 v4, 4294836224, v4
LX_aa 180,244,20
LX_t 224,4,0
LX_aa 184,240,8
LX_t 226,4,4096
LX_aa 184,244,24
LX_t 228,4,8192
LX_aa 188,240,12
LX_t 230,4,12288
LX_aa 188,244,28
LX_w 4
LX_l 144,23,0
LX_aa 208,248,0
LX_aa 208,252,16
LX_l 148,23,1024
LX_aa 212,248,4
v_add_u32 v5, 4294836224, v5
LX_aa 212,252,20
LX_t 232,5,0
LX_aa 216,248,8
LX_t 234,5,4096
LX_aa 216,252,24
LX_t 236,5,8192
LX_aa 220,248,12
LX_t 238,5,12288
LX_aa 220,252,28
LX_w 4
s_waitcnt vmcnt(35)
LX_l 152,23,2048
LX_aa 48,224,32
LX_aa 48,228,48
LX_l 156,23,3072
LX_aa 52,224,36
v_add_u32 v6, 4294836224, v6
LX_aa 52,228,52
LX_t 240,6,0
LX_aa 56,224,40
LX_t 242,6,4096
LX_aa 56,228,56
LX_t 244,6,8192
LX_aa 60,224,44
s_add_u32 s33, s34, 24576
LX_t 246,6,12288
LX_aa 60,228,60
LX_w 4
LX_l 160,1,0
LX_aa 80,232,32
LX_aa 80,236,48
LX_l 164,1,1024
LX_aa 84,232,36
v_add_u32 v7, 4294836224, v7
LX_aa 84,236,52
LX_t 248,7,0
LX_aa 88,232,40
LX_t 250,7,4096
LX_aa 88,236,56
LX_t 252,7,8192
LX_aa 92,232,44
LX_t 254,7,12288
LX_aa 92,236,60
LX_w 4
LX_l 168,1,2048
LX_aa 112,240,32
LX_aa 112,244,48
LX_aa 116,240,36
LX_l 172,1,3072
v_add_u32 v8, 4294836224, v8
LX_aa 116,244,52
LX_t 224,8,0
LX_aa 120,240,40
LX_t 226,8,4096
LX_aa 120,244,56
LX_t 228,8,8192
LX_aa 124,240,44
LX_t 230,8,12288
LX_aa 124,244,60
LX_w 4
LX_l 176,23,0
LX_aa 144,248,32
LX_aa 144,252,48
LX_aa 148,248,36
LX_l 180,23,1024
v_add_u32 v21, 4294836224, v21
LX_aa 148,252,52
LX_t 232,21,0
LX_aa 152,248,40
LX_t 234,21,4096
LX_aa 152,252,56
LX_t 236,21,8192
LX_aa 156,248,44
LX_t 238,21,12288
)ASM"
R"ASM(LX_aa 156,252,60
LX_w 4
LX_l 184,23,2048
LX_aa 176,224,32
LX_aa 176,228,48
LX_aa 180,224,36
LX_aa 180,228,52
LX_t 240,4,16384
LX_aa 184,224,40
LX_t 242,4,20480
LX_aa 184,228,56
LX_t 244,4,24576
LX_aa 188,224,44
LX_t 246,4,28672
LX_aa 188,228,60
LX_w 4
LX_l 188,23,3072
LX_aa 208,232,32
LX_aa 208,236,48
LX_aa 212,232,36
LX_aa 212,236,52
LX_t 248,5,16384
LX_aa 216,232,40
LX_t 250,5,20480
LX_aa 216,236,56
LX_t 252,5,24576
LX_aa 220,232,44
s_add_u32 m0, s49, 0
LX_t 254,5,28672
LX_aa 220,236,60
LX_w 4
s_waitcnt vmcnt(36)
buffer_load_dwordx4 v15, s[24:27], s54 offen offset:0 lds
LX_aa 48,240,64
LX_aa 48,244,80
s_add_u32 m0, s49, 4294966272
LX_aa 52,240,68
buffer_load_dwordx4 v15, s[24:27], s54 offen offset:2048 lds
LX_aa 52,244,84
LX_t 224,6,16384
LX_aa 56,240,72
LX_t 226,6,20480
LX_aa 56,244,88
LX_t 228,6,24576
LX_aa 60,240,76
LX_t 230,6,28672
LX_aa 60,244,92
LX_w 4
LX_aa 80,248,64
s_add_u32 m0, s49, 2048
LX_aa 80,252,80
buffer_load_dwordx4 v15, s[24:27], s55 offen offset:0 lds
LX_aa 84,248,68
LX_aa 84,252,84
LX_t 232,7,16384
LX_aa 88,248,72
LX_t 234,7,20480
LX_aa 88,252,88
LX_t 236,7,24576
LX_aa 92,248,76
LX_t 238,7,28672
LX_aa 92,252,92
LX_w 4
s_add_u32 m0, s49, 1024
LX_aa 112,224,64
buffer_load_dwordx4 v15, s[24:27], s55 offen offset:2048 lds
LX_aa 112,228,80
LX_aa 116,224,68
LX_aa 116,228,84
LX_t 240,8,16384
LX_aa 120,224,72
LX_t 242,8,20480
LX_aa 120,228,88
LX_t 244,8,24576
LX_aa 124,224,76
LX_t 246,8,28672
LX_aa 124,228,92
LX_w 4
LX_aa 144,232,64
LX_aa 144,236,80
LX_aa 148,232,68
LX_aa 148,236,84
LX_t 248,21,16384
LX_aa 152,232,72
LX_t 250,21,20480
LX_aa 152,236,88
LX_t 252,21,24576
LX_aa 156,232,76
LX_t 254,21,28672
LX_aa 156,236,92
LX_w 4
LX_aa 176,240,64
LX_aa 176,244,80
LX_aa 180,240,68
LX_aa 180,244,84
s_waitcnt vmcnt(17) lgkmcnt(0)
s_barrier
LX_t 224,4,32768
LX_aa 184,240,72
LX_t 226,4,36864
LX_aa 184,244,88
LX_t 228,4,40960
LX_aa 188,240,76
LX_t 230,4,45056
LX_aa 188,244,92
LX_aa 208,248,64
LX_aa 208,252,80
LX_aa 212,248,68
LX_aa 212,252,84
LX_t 232,4,49152
LX_aa 216,248,72
LX_t 234,4,53248
LX_aa 216,252,88
LX_t 236,4,57344
LX_aa 220,248,76
LX_t 238,4,61440
LX_aa 220,252,92
LX_w 4
s_waitcnt vmcnt(24)
LX_aa 48,224,96
LX_aa 48,228,112
LX_aa 52,224,100
v_add_u32 v4, 65536, v4
LX_aa 52,228,116
LX_t 240,4,0
LX_aa 56,224,104
LX_t 242,4,4096
LX_aa 56,228,120
LX_t 244,4,8192
LX_aa 60,224,108
LX_t 246,4,12288
LX_aa 60,228,124
LX_w 4
s_waitcnt vmcnt(12)
LX_aa 48,232,128
LX_aa 48,236,144
LX_aa 52,232,132
LX_aa 52,236,148
LX_t 248,5,32768
LX_aa 56,232,136
LX_t 250,5,36864
LX_aa 56,236,152
LX_t 252,5,40960
LX_aa 60,232,140
LX_t 254,5,45056
LX_aa 60,236,156
LX_w 4
s_waitcnt vmcnt(4)
LX_aa 48,240,160
LX_aa 48,244,176
LX_aa 52,240,164
LX_aa 52,244,180
LX_t 224,5,49152
LX_aa 56,240,168
LX_t 226,5,53248
LX_aa 56,244,184
LX_t 228,5,57344
LX_aa 60,240,172
LX_t 230,5,61440
LX_aa 60,244,188
LX_w 4
LX_aa 80,248,96
LX_aa 80,252,112
LX_aa 84,248,100
v_add_u32 v5, 65536, v5
LX_aa 84,252,116
LX_t 232,5,0
LX_aa 88,248,104
LX_t 234,5,4096
LX_aa 88,252,120
LX_t 236,5,8192
LX_c 48,48,49
LX_aa 92,248,108
LX_t 238,5,12288
LX_c 49,50,51
LX_aa 92,252,124
LX_w 4
LX_c 50,52,53
LX_c 51,54,55
LX_aa 80,224,128
buffer_store_dwordx4 v[48:51], v1, s[20:23], s42 offen offset:0 sc1
LX_c 56,56,57
LX_aa 80,228,144
LX_c 57,58,59
LX_c 58,60,61
LX_aa 84,224,132
LX_c 59,62,63
LX_aa 84,228,148
LX_t 240,6,32768
LX_aa 88,224,136
LX_t 242,6,36864
LX_aa 88,228,152
LX_t 244,6,40960
LX_aa 92,224,140
LX_t 246,6,45056
LX_aa 92,228,156
LX_w 4
buffer_store_dwordx4 v[56:59], v1, s[20:23], s42 offen offset:1024 sc1
LX_aa 80,232,160
LX_aa 80,236,176
LX_aa 84,232,164
LX_aa 84,236,180
LX_t 248,6,49152
LX_aa 88,232,168
LX_t 250,6,53248
LX_aa 88,236,184
LX_t 252,6,57344
LX_aa 92,232,172
LX_t 254,6,61440
LX_aa 92,236,188
LX_w 4
LX_aa 112,240,96
LX_aa 112,244,112
LX_aa 116,240,100
v_add_u32 v6, 65536, v6
LX_aa 116,244,116
LX_t 224,6,0
LX_aa 120,240,104
LX_t 226,6,4096
LX_aa 120,244,120
LX_t 228,6,8192
LX_c 80,80,81
LX_aa 124,240,108
LX_t 230,6,12288
LX_c 81,82,83
LX_aa 124,244,124
LX_w 4
LX_c 82,84,85
LX_c 83,86,87
LX_aa 112,248,128
buffer_store_dwordx4 v[80:83], v1, s[20:23], s42 offen offset:2048 sc1
LX_c 88,88,89
LX_aa 112,252,144
LX_c 89,90,91
LX_c 90,92,93
LX_aa 116,248,132
LX_c 91,94,95
LX_aa 116,252,148
LX_t 232,7,32768
LX_aa 120,248,136
LX_t 234,7,36864
LX_aa 120,252,152
LX_t 236,7,40960
LX_aa 124,248,140
LX_t 238,7,45056
LX_aa 124,252,156
LX_w 4
buffer_store_dwordx4 v[88:91], v1, s[20:23], s42 offen offset:3072 sc1
LX_aa 112,224,160
LX_aa 112,228,176
LX_aa 116,224,164
LX_aa 116,228,180
LX_t 240,7,49152
LX_aa 120,224,168
LX_t 242,7,53248
LX_aa 120,228,184
LX_t 244,7,57344
LX_aa 124,224,172
LX_t 246,7,61440
LX_aa 124,228,188
LX_w 4
LX_aa 144,232,96
LX_aa 144,236,112
LX_aa 148,232,100
v_add_u32 v7, 65536, v7
LX_aa 148,236,116
LX_t 248,7,0
LX_aa 152,232,104
LX_t 250,7,4096
LX_aa 152,236,120
LX_t 252,7,8192
LX_c 112,112,113
LX_aa 156,232,108
LX_t 254,7,12288
LX_c 113,114,115
LX_aa 156,236,124
LX_w 4
LX_c 114,116,117
LX_c 115,118,119
LX_aa 144,240,128
buffer_store_dwordx4 v[112:115], v1, s[20:23], s43 offen offset:0 sc1
LX_c 120,120,121
LX_aa 144,244,144
LX_c 121,122,123
LX_c 122,124,125
LX_aa 148,240,132
LX_c 123,126,127
LX_aa 148,244,148
LX_t 224,8,32768
LX_aa 152,240,136
LX_t 226,8,36864
LX_aa 152,244,152
LX_t 228,8,40960
LX_aa 156,240,140
LX_t 230,8,45056
LX_aa 156,244,156
LX_w 4
buffer_store_dwordx4 v[120:123], v1, s[20:23], s43 offen offset:1024 sc1
LX_aa 144,248,160
LX_aa 144,252,176
LX_aa 148,248,164
LX_aa 148,252,180
LX_t 232,8,49152
LX_aa 152,248,168
LX_t 234,8,53248
LX_aa 152,252,184
LX_t 236,8,57344
LX_aa 156,248,172
LX_t 238,8,61440
LX_aa 156,252,188
LX_w 4
LX_aa 176,224,96
LX_aa 176,228,112
LX_aa 180,224,100
v_add_u32 v8, 65536, v8
LX_aa 180,228,116
LX_F0_pad12:
s_cbranch_execz LX_F0_padret
LX_t 240,8,0
LX_aa 184,224,104
LX_t 242,8,4096
LX_aa 184,228,120
LX_t 244,8,8192
LX_c 144,144,145
LX_aa 188,224,108
LX_t 246,8,12288
LX_c 145,146,147
LX_aa 188,228,124
LX_w 4
LX_c 146,148,149
LX_c 147,150,151
LX_aa 176,232,128
buffer_store_dwordx4 v[144:147], v1, s[20:23], s43 offen offset:2048 sc1
LX_c 152,152,153
LX_aa 176,236,144
LX_c 153,154,155
LX_c 154,156,157
LX_aa 180,232,132
LX_c 155,158,159
LX_aa 180,236,148
LX_t 248,21,32768
LX_aa 184,232,136
LX_t 250,21,36864
LX_aa 184,236,152
LX_t 252,21,40960
LX_aa 188,232,140
LX_t 254,21,45056
LX_aa 188,236,156
LX_w 4
buffer_store_dwordx4 v[152:155], v1, s[20:23], s43 offen offset:3072 sc1
LX_aa 176,240,160
LX_aa 176,244,176
LX_aa 180,240,164
LX_aa 180,244,180
LX_t 224,21,49152
LX_aa 184,240,168
LX_t 226,21,53248
LX_aa 184,244,184
LX_t 228,21,57344
LX_aa 188,240,172
LX_t 230,21,61440
LX_aa 188,244,188
LX_w 4
LX_aa 208,248,96
LX_aa 208,252,112
LX_aa 212,248,100
v_add_u32 v21, 65536, v21
LX_aa 212,252,116
LX_t 232,21,0
LX_aa 216,248,104
LX_t 234,21,4096
LX_aa 216,252,120
LX_t 236,21,8192
LX_c 176,176,177
LX_aa 220,248,108
LX_t 238,21,12288
LX_c 177,178,179
LX_aa 220,252,124
LX_w 4
LX_c 178,180,181
LX_c 179,182,183
LX_aa 208,224,128
buffer_store_dwordx4 v[176:179], v1, s[20:23], s44 offen offset:0 sc1
LX_c 184,184,185
LX_aa 208,228,144
LX_c 185,186,187
LX_c 186,188,189
LX_aa 212,224,132
LX_c 187,190,191
LX_aa 212,228,148
LX_t 240,2,32768
LX_aa 216,224,136
LX_t 242,2,36864
LX_aa 216,228,152
LX_t 244,2,40960
LX_aa 220,224,140
LX_t 246,2,45056
LX_aa 220,228,156
LX_w 4
s_waitcnt vmcnt(5)
buffer_store_dwordx2 v[26:27], v30, s[20:23], s96 offen offset:0 sc1
LX_aa 208,232,160
buffer_store_dwordx4 v[184:187], v1, s[20:23], s44 offen offset:1024 sc1
LX_aa 208,236,176
LX_aa 212,232,164
LX_aa 212,236,180
LX_t 248,3,32768
LX_aa 216,232,168
LX_t 250,3,36864
LX_aa 216,236,184
LX_t 252,3,40960
LX_aa 220,232,172
LX_t 254,3,45056
LX_aa 220,236,188
LX_w 4
LX_aa 240,240,192
LX_aa 240,244,208
LX_aa 244,240,196
LX_aa 244,244,212
LX_t 224,2,49152
LX_aa 248,240,200
LX_t 226,2,53248
LX_aa 248,244,216
LX_t 228,2,57344
LX_c 208,208,209
LX_aa 252,240,204
LX_t 230,2,61440
LX_c 209,210,211
LX_aa 252,244,220
LX_w 4
LX_c 210,212,213
LX_c 211,214,215
LX_aa 224,248,192
buffer_store_dwordx4 v[208:211], v1, s[20:23], s44 offen offset:2048 sc1
LX_c 216,216,217
LX_aa 224,252,208
LX_c 217,218,219
LX_c 218,220,221
LX_aa 228,248,196
LX_c 219,222,223
LX_aa 228,252,212
LX_t 232,3,49152
LX_aa 232,248,200
LX_t 234,3,53248
LX_aa 232,252,216
LX_t 236,3,57344
LX_aa 236,248,204
LX_t 238,3,61440
LX_aa 236,252,220
LX_w 4
buffer_store_dwordx4 v[216:219], v1, s[20:23], s44 offen offset:3072 sc1
LX_av 240,224,96
LX_av 240,228,128
buffer_load_dwordx2 v[18:19], v20, s[20:23], s52 offen sc1
LX_av 244,224,100
v_add_u32 v2, 65536, v2
LX_av 244,228,132
LX_t 240,2,0
LX_av 248,224,104
LX_t 242,2,4096
LX_av 248,228,136
LX_t 244,2,8192
LX_av 252,224,108
LX_t 246,2,12288
LX_av 252,228,140
LX_w 4
LX_av 224,232,96
LX_av 224,236,128
LX_av 228,232,100
v_add_u32 v3, 65536, v3
LX_av 228,236,132
LX_t 248,3,0
LX_av 232,232,104
LX_t 250,3,4096
LX_av 232,236,136
LX_t 252,3,8192
LX_av 236,232,108
LX_t 254,3,12288
LX_av 236,236,140
LX_w 4
LX_av 240,240,160
LX_av 240,244,192
LX_av 244,240,164
LX_av 244,244,196
LX_t 224,2,16384
LX_av 248,240,168
LX_t 226,2,20480
LX_av 248,244,200
LX_t 228,2,24576
LX_av 252,240,172
LX_t 230,2,28672
LX_av 252,244,204
LX_w 4
s_waitcnt vmcnt(6)
buffer_store_dwordx2 v[26:27], v30, s[20:23], s97 offen offset:128 sc1
LX_av 224,248,160
LX_av 224,252,192
LX_av 228,248,164
LX_av 228,252,196
LX_t 232,3,16384
LX_av 232,248,168
LX_t 234,3,20480
LX_av 232,252,200
LX_t 236,3,24576
LX_av 236,248,172
LX_t 238,3,28672
LX_av 236,252,204
LX_w 4
LX_aa 240,224,0
LX_aa 240,228,16
LX_aa 244,224,4
v_add_u32 v2, 4294836224, v2
LX_aa 244,228,20
LX_t 240,2,0
LX_aa 248,224,8
LX_t 242,2,4096
LX_aa 248,228,24
LX_t 244,2,8192
LX_aa 252,224,12
LX_t 246,2,12288
LX_aa 252,228,28
LX_w 4
LX_aa 224,232,0
LX_aa 224,236,16
LX_aa 228,232,4
v_add_u32 v3, 4294836224, v3
LX_aa 228,236,20
LX_t 248,3,0
LX_aa 232,232,8
LX_t 250,3,4096
LX_aa 232,236,24
LX_t 252,3,8192
LX_aa 236,232,12
LX_t 254,3,12288
LX_aa 236,236,28
LX_w 4
LX_aa 240,240,32
LX_aa 240,244,48
LX_aa 244,240,36
LX_aa 244,244,52
LX_t 224,2,16384
LX_aa 248,240,40
LX_t 226,2,20480
LX_aa 248,244,56
LX_t 228,2,24576
LX_aa 252,240,44
LX_t 230,2,28672
LX_aa 252,244,60
LX_w 4
LX_aa 224,248,32
LX_aa 224,252,48
LX_aa 228,248,36
LX_aa 228,252,52
LX_t 232,3,16384
LX_aa 232,248,40
LX_t 234,3,20480
LX_aa 232,252,56
LX_t 236,3,24576
LX_aa 236,248,44
LX_t 238,3,28672
LX_aa 236,252,60
LX_w 4
s_waitcnt vmcnt(2)
buffer_store_dwordx2 v[26:27], v30, s[20:23], s98 offen offset:256 sc1
LX_aa 240,224,64
LX_aa 240,228,80
s_waitcnt vmcnt(2)
v_cmp_eq_u64 s[36:37], s[56:57], v[18:19]
LX_aa 244,224,68
s_nop 0
s_bitcmp1_b64 s[36:37], 0
s_cselect_b32 s53, 0, 1073737728
v_add_u32 v16, s53, v1
LX_aa 244,228,84
LX_t 240,2,32768
LX_aa 248,224,72
LX_t 242,2,36864
LX_aa 248,228,88
LX_t 244,2,40960
LX_aa 252,224,76
LX_t 246,2,45056
LX_aa 252,228,92
LX_w 4
buffer_load_dwordx4 v[48:51], v16, s[20:23], s51 offen offset:0 sc1
LX_aa 224,232,64
buffer_load_dwordx4 v[52:55], v16, s[20:23], s51 offen offset:1024 sc1
LX_aa 224,236,80
buffer_load_dwordx4 v[56:59], v16, s[20:23], s51 offen offset:2048 sc1
LX_aa 228,232,68
buffer_load_dwordx4 v[60:63], v16, s[20:23], s51 offen offset:3072 sc1
LX_aa 228,236,84
s_bitcmp1_b64 s[36:37], 16
s_cselect_b32 s53, 0, 1073737728
LX_t 248,3,32768
LX_aa 232,232,72
LX_t 250,3,36864
v_add_u32 v17, s53, v23
LX_aa 232,236,88
LX_t 252,3,40960
LX_aa 236,232,76
LX_t 254,3,45056
LX_aa 236,236,92
LX_w 4
buffer_load_dwordx4 v[80:83], v17, s[20:23], s51 offen offset:0 sc1
LX_aa 240,240,96
buffer_load_dwordx4 v[84:87], v17, s[20:23], s51 offen offset:1024 sc1
LX_aa 240,244,112
buffer_load_dwordx4 v[88:91], v17, s[20:23], s51 offen offset:2048 sc1
LX_aa 244,240,100
buffer_load_dwordx4 v[92:95], v17, s[20:23], s51 offen offset:3072 sc1
LX_aa 244,244,116
s_bitcmp1_b64 s[36:37], 32
s_cselect_b32 s53, 0, 1073737728
LX_t 224,2,49152
LX_aa 248,240,104
LX_t 226,2,53248
v_add_u32 v16, s53, v1
LX_aa 248,244,120
LX_t 228,2,57344
LX_aa 252,240,108
LX_t 230,2,61440
LX_aa 252,244,124
LX_w 4
buffer_load_dwordx4 v[112:115], v16, s[20:23], s50 offen offset:0 sc1
)ASM"
R"ASM(LX_aa 224,248,96
buffer_load_dwordx4 v[116:119], v16, s[20:23], s50 offen offset:1024 sc1
LX_aa 224,252,112
buffer_load_dwordx4 v[120:123], v16, s[20:23], s50 offen offset:2048 sc1
LX_aa 228,248,100
buffer_load_dwordx4 v[124:127], v16, s[20:23], s50 offen offset:3072 sc1
LX_aa 228,252,116
LX_t 232,3,49152
LX_aa 232,248,104
LX_t 234,3,53248
LX_aa 232,252,120
LX_t 236,3,57344
LX_aa 236,248,108
LX_t 238,3,61440
LX_aa 236,252,124
LX_w 4
LX_aa 240,224,128
LX_aa 240,228,144
LX_aa 244,224,132
v_add_u32 v2, 65536, v2
LX_aa 244,228,148
LX_t 240,2,0
LX_aa 248,224,136
LX_t 242,2,4096
LX_aa 248,228,152
LX_t 244,2,8192
LX_aa 252,224,140
LX_t 246,2,12288
LX_aa 252,228,156
LX_w 4
LX_aa 224,232,128
LX_aa 224,236,144
LX_aa 228,232,132
v_add_u32 v3, 65536, v3
LX_aa 228,236,148
LX_t 248,3,0
LX_aa 232,232,136
LX_t 250,3,4096
LX_aa 232,236,152
LX_t 252,3,8192
LX_aa 236,232,140
LX_t 254,3,12288
LX_aa 236,236,156
LX_w 4
LX_aa 240,240,160
LX_aa 240,244,176
LX_aa 244,240,164
LX_aa 244,244,180
LX_aa 248,240,168
LX_aa 248,244,184
LX_aa 252,240,172
LX_aa 252,244,188
LX_w 0
LX_aa 224,248,160
LX_aa 224,252,176
LX_aa 228,248,164
LX_aa 228,252,180
LX_aa 232,248,168
LX_aa 232,252,184
LX_aa 236,248,172
LX_aa 236,252,188
s_waitcnt vmcnt(28)
ds_read_b64 v[32:33], v12 offset:0
ds_read_b64 v[34:35], v12 offset:32
ds_read_b64 v[36:37], v12 offset:1024
ds_read_b64 v[38:39], v12 offset:1056
ds_read_b64 v[40:41], v12 offset:2048
ds_read_b64 v[42:43], v12 offset:2080
ds_read_b64 v[44:45], v12 offset:3072
ds_read_b64 v[46:47], v12 offset:3104
LX_vv 240,68,64
LX_vv 224,72,64
LX_vv 244,68,64
LX_vv 228,72,64
LX_vv 248,68,64
LX_vv 232,72,64
LX_vv 252,68,64
LX_vv 236,72,64
LX_w 0
LX_vv 240,68,32
LX_vv 224,72,32
LX_vv 244,68,36
LX_vv 228,72,36
LX_vv 248,68,40
LX_vv 232,72,40
LX_vv 252,68,44
LX_vv 236,72,44
s_mov_b32 s53, 0
s_bitcmp1_b64 s[36:37], 0
s_cbranch_scc0 LX_c24w
s_waitcnt vmcnt(8)
LX_c24a0:
LX_vv 240,68,48
LX_vv 244,72,48
LX_vv 224,68,56
LX_vv 228,72,56
LX_vv 248,68,52
LX_vv 252,72,52
LX_vv 232,68,60
LX_vv 236,72,60
s_mov_b32 s53, 1
s_bitcmp1_b64 s[36:37], 16
s_cbranch_scc0 LX_c24w
s_waitcnt vmcnt(4)
LX_c24a1:
LX_vv 240,68,80
LX_vv 244,72,80
LX_vv 224,68,88
LX_vv 228,72,88
LX_vv 248,68,84
LX_vv 252,72,84
LX_vv 232,68,92
LX_vv 236,72,92
s_mov_b32 s53, 2
s_bitcmp1_b64 s[36:37], 32
s_cbranch_scc0 LX_c24w
s_waitcnt vmcnt(0)
LX_c24a2:
LX_vv 240,68,112
LX_vv 244,72,112
LX_vv 224,68,120
LX_vv 228,72,120
LX_vv 248,68,116
LX_vv 252,72,116
LX_vv 232,68,124
LX_vv 236,72,124
s_branch LX_c24d
LX_c24w:
s_mov_b32 s40, 262144
LX_c24:
buffer_load_dwordx2 v[18:19], v20, s[20:23], s52 offen sc1
s_waitcnt vmcnt(0)
v_cmp_eq_u64 vcc, s[56:57], v[18:19]
s_sub_u32 s40, s40, 1
s_cbranch_scc1 LX_c24x
s_andn2_b64 s[46:47], vcc, s[36:37]
s_cbranch_scc0 LX_c24
s_or_b64 s[36:37], s[36:37], vcc
s_bitcmp1_b64 s[46:47], 0
s_cbranch_scc0 LX_c24n0
buffer_load_dwordx4 v[48:51], v1, s[20:23], s51 offen offset:0 sc1
buffer_load_dwordx4 v[52:55], v1, s[20:23], s51 offen offset:1024 sc1
buffer_load_dwordx4 v[56:59], v1, s[20:23], s51 offen offset:2048 sc1
buffer_load_dwordx4 v[60:63], v1, s[20:23], s51 offen offset:3072 sc1
LX_c24n0:
s_bitcmp1_b64 s[46:47], 16
s_cbranch_scc0 LX_c24n1
buffer_load_dwordx4 v[80:83], v23, s[20:23], s51 offen offset:0 sc1
buffer_load_dwordx4 v[84:87], v23, s[20:23], s51 offen offset:1024 sc1
buffer_load_dwordx4 v[88:91], v23, s[20:23], s51 offen offset:2048 sc1
buffer_load_dwordx4 v[92:95], v23, s[20:23], s51 offen offset:3072 sc1
LX_c24n1:
s_bitcmp1_b64 s[46:47], 32
s_cbranch_scc0 LX_c24n2
buffer_load_dwordx4 v[112:115], v1, s[20:23], s50 offen offset:0 sc1
buffer_load_dwordx4 v[116:119], v1, s[20:23], s50 offen offset:1024 sc1
buffer_load_dwordx4 v[120:123], v1, s[20:23], s50 offen offset:2048 sc1
buffer_load_dwordx4 v[124:127], v1, s[20:23], s50 offen offset:3072 sc1
LX_c24n2:
s_cmp_eq_u64 s[36:37], -1
s_cbranch_scc0 LX_c24
LX_c24x:
s_mov_b64 s[36:37], -1
s_cmp_eq_u32 s53, 2
s_waitcnt vmcnt(0)
s_cbranch_scc1 LX_c24a2
s_cmp_eq_u32 s53, 1
s_cbranch_scc1 LX_c24a1
s_branch LX_c24a0
LX_c24d:
s_nop 6
LX_c 240,240,241
LX_c 241,242,243
LX_c 242,224,225
LX_c 243,226,227
ds_write_b64 v12, v[240:241] offset:0
ds_write_b64 v12, v[242:243] offset:32
ds_read_b128 v[32:35], v13 offset:0
LX_c 244,244,245
LX_c 245,246,247
LX_c 246,228,229
LX_c 247,230,231
ds_write_b64 v12, v[244:245] offset:1024
ds_write_b64 v12, v[246:247] offset:1056
ds_read_b128 v[36:39], v13 offset:1024
LX_c 248,248,249
LX_c 249,250,251
LX_c 250,232,233
LX_c 251,234,235
ds_write_b64 v12, v[248:249] offset:2048
ds_write_b64 v12, v[250:251] offset:2080
ds_read_b128 v[40:43], v13 offset:2048
LX_c 252,252,253
LX_c 253,254,255
LX_c 254,236,237
LX_c 255,238,239
ds_write_b64 v12, v[252:253] offset:3072
ds_write_b64 v12, v[254:255] offset:3104
ds_read_b128 v[44:47], v13 offset:3072
LX_w 9
buffer_store_dwordx4 v[32:35], v15, s[16:19], s54 offen offset:0 sc1
LX_w 6
buffer_store_dwordx4 v[36:39], v15, s[16:19], s54 offen offset:2048 sc1
LX_w 3
buffer_store_dwordx4 v[40:43], v15, s[16:19], s55 offen offset:0 sc1
LX_w 0
buffer_store_dwordx4 v[44:47], v15, s[16:19], s55 offen offset:2048 sc1
s_endpgm
LX_c14:
s_mov_b32 s40, 262144
LX_15:
buffer_load_dwordx2 v[18:19], v24, s[20:23], 0 offen sc1
s_waitcnt vmcnt(0)
v_cmp_ne_u64 vcc, s[56:57], v[18:19]
s_sub_u32 s40, s40, 1
s_cbranch_scc1 LX_15d
s_nop 2
s_cbranch_vccnz LX_15
LX_15d:
s_branch LX_c14r
LX_c16:
s_mov_b32 s40, 262144
LX_17:
buffer_load_dwordx2 v[18:19], v24, s[20:23], s80 offen sc1
s_waitcnt vmcnt(0)
v_cmp_ne_u64 vcc, s[56:57], v[18:19]
s_sub_u32 s40, s40, 1
s_cbranch_scc1 LX_17d
s_nop 2
s_cbranch_vccnz LX_17
LX_17d:
s_branch LX_c16r
LX_c18:
s_mov_b32 s40, 262144
LX_19:
buffer_load_dwordx2 v[18:19], v25, s[20:23], s80 offen sc1
s_waitcnt vmcnt(0)
v_cmp_ne_u64 vcc, s[56:57], v[18:19]
s_sub_u32 s40, s40, 1
s_cbranch_scc1 LX_19d
s_nop 2
s_cbranch_vccnz LX_19
LX_19d:
s_branch LX_c18r
LX_c20:
s_mov_b32 s40, 262144
LX_21:
buffer_load_dwordx2 v[18:19], v22, s[20:23], s80 offen sc1
s_waitcnt vmcnt(0)
v_cmp_ne_u64 vcc, s[56:57], v[18:19]
s_sub_u32 s40, s40, 1
s_cbranch_scc1 LX_21d
s_nop 2
s_cbranch_vccnz LX_21
LX_21d:
s_branch LX_c20r
LX_c22:
s_mov_b32 s40, 262144
LX_23:
buffer_load_dwordx2 v[18:19], v31, s[20:23], s80 offen sc1
s_waitcnt vmcnt(0)
v_cmp_ne_u64 vcc, s[56:57], v[18:19]
s_sub_u32 s40, s40, 1
s_cbranch_scc1 LX_23d
s_nop 2
s_cbranch_vccnz LX_23
LX_23d:
s_branch LX_c22r
LX_F0_end:
.fill (LX_Q0_begin+122880-LX_F0_end)/4, 4, 0xbf800000

)ASM"
  :: "s"(ka), "s"(p), "s"(q), "s"(t), "v"(threadIdx.x), "s"(ty) : "v1","v2","v3","v4","v5","v6","v7","v8","v9","v10","v11","v12","v13","v14","v15","v16","v17","v18","v19","v20","v21","v22","v23","v24","v25","v26","v27","v28","v29","v30","v31","v32","v33","v34","v35","v36","v37","v38","v39","v40","v41","v42","v43","v44","v45","v46","v47","v48","v49","v50","v51","v52","v53","v54","v55","v56","v57","v58","v59","v60","v61","v62","v63","v64","v65","v66","v67","v68","v69","v70","v71","v72","v73","v74","v75","v76","v77","v78","v79","v80","v81","v82","v83","v84","v85","v86","v87","v88","v89","v90","v91","v92","v93","v94","v95","v96","v97","v98","v99","v100","v101","v102","v103","v104","v105","v106","v107","v108","v109","v110","v111","v112","v113","v114","v115","v116","v117","v118","v119","v120","v121","v122","v123","v124","v125","v126","v127","v128","v129","v130","v131","v132","v133","v134","v135","v136","v137","v138","v139","v140","v141","v142","v143","v144","v145","v146","v147","v148","v149","v150","v151","v152","v153","v154","v155","v156","v157","v158","v159","v160","v161","v162","v163","v164","v165","v166","v167","v168","v169","v170","v171","v172","v173","v174","v175","v176","v177","v178","v179","v180","v181","v182","v183","v184","v185","v186","v187","v188","v189","v190","v191","v192","v193","v194","v195","v196","v197","v198","v199","v200","v201","v202","v203","v204","v205","v206","v207","v208","v209","v210","v211","v212","v213","v214","v215","v216","v217","v218","v219","v220","v221","v222","v223","v224","v225","v226","v227","v228","v229","v230","v231","v232","v233","v234","v235","v236","v237","v238","v239","v240","v241","v242","v243","v244","v245","v246","v247","v248","v249","v250","v251","v252","v253","v254","v255","a0","a1","a2","a3","a4","a5","a6","a7","a8","a9","a10","a11","a12","a13","a14","a15","a16","a17","a18","a19","a20","a21","a22","a23","a24","a25","a26","a27","a28","a29","a30","a31","a32","a33","a34","a35","a36","a37","a38","a39","a40","a41","a42","a43","a44","a45","a46","a47","a48","a49","a50","a51","a52","a53","a54","a55","a56","a57","a58","a59","a60","a61","a62","a63","a64","a65","a66","a67","a68","a69","a70","a71","a72","a73","a74","a75","a76","a77","a78","a79","a80","a81","a82","a83","a84","a85","a86","a87","a88","a89","a90","a91","a92","a93","a94","a95","a96","a97","a98","a99","a100","a101","a102","a103","a104","a105","a106","a107","a108","a109","a110","a111","a112","a113","a114","a115","a116","a117","a118","a119","a120","a121","a122","a123","a124","a125","a126","a127","a128","a129","a130","a131","a132","a133","a134","a135","a136","a137","a138","a139","a140","a141","a142","a143","a144","a145","a146","a147","a148","a149","a150","a151","a152","a153","a154","a155","a156","a157","a158","a159","a160","a161","a162","a163","a164","a165","a166","a167","a168","a169","a170","a171","a172","a173","a174","a175","a176","a177","a178","a179","a180","a181","a182","a183","a184","a185","a186","a187","a188","a189","a190","a191","a192","a193","a194","a195","a196","a197","a198","a199","a200","a201","a202","a203","a204","a205","a206","a207","a208","a209","a210","a211","a212","a213","a214","a215","a216","a217","a218","a219","a220","a221","a222","a223","a224","a225","a226","a227","a228","a229","a230","a231","a232","a233","a234","a235","a236","a237","a238","a239","a240","a241","a242","a243","a244","a245","a246","a247","a248","a249","a250","a251","a252","a253","a254","a255","s16","s17","s18","s19","s20","s21","s22","s23","s24","s25","s26","s27","s28","s29","s30","s31","s33","s34","s35","s36","s37","s38","s39","s40","s41","s42","s43","s44","s45","s46","s47","s48","s49","s50","s51","s52","s53","s54","s55","s56","s57","s58","s59","s60","s61","s62","s63","s64","s65","s66","s67","s68","s69","s70","s71","s72","s73","s74","s75","s76","s77","s78","s79","s80","s81","s82","s83","s84","s85","s86","s87","s88","s89","s90","s91","s92","s93","s94","s95","s96","s97","s98","s99","vcc","scc","memory");
}

static void launch_all(void* const* d_in, void* d_out, void* d_ws, void* dbg1, void* dbg2, hipStream_t stream) {
  (void)hipFuncSetAttribute((const void*)k_fused, hipFuncAttributeMaxDynamicSharedMemorySize, 163840);
  k_fused<<<256, 256, 163840, stream>>>(d_in[0], d_ws, d_in[1], d_in[4], d_in[5], d_in[2], d_in[6], d_in[7], d_out);
}

extern "C" void kernel_launch(void* const* d_in, const int* in_sizes, int n_in, void* d_out, int out_size, void* d_ws, size_t ws_size, hipStream_t stream) {
  launch_all(d_in, d_out, d_ws, d_ws, d_ws, stream);
}
```
